```python
import torch

def _softmax(x, axis=-1):
    return torch.softmax(x, dim=axis)
def _gelu(x):
    return torch.nn.functional.gelu(x, approximate="tanh")
def _silu(x):
    return torch.nn.functional.silu(x)
def _sigmoid(x):
    return torch.sigmoid(x)
def _layernorm(x, axis=-1, eps=1e-5):
    m = x.mean(dim=axis, keepdim=True)
    v = x.var(dim=axis, keepdim=True, unbiased=False)
    return (x - m) / torch.sqrt(v + eps)
def _scatter_add(tgt, idx, src, axis=0):
    return tgt.clone().index_add_(axis, idx, src)
def _segment_sum(src, seg, n):
    out = torch.zeros((n,) + src.shape[1:], dtype=src.dtype)
    return out.index_add_(0, seg, src)
def _pad(x, pw):
    flat = tuple(v for pair in reversed(pw) for v in pair)
    return torch.nn.functional.pad(x, flat)

kernel_name = 'sequential_preLN__linear_attn_block__mixer_channel_mlp__prompt'

def setup_inputs():

    _w = (lambda a: (lambda t: t.to(torch.bfloat16) if t.is_floating_point() else t)(torch.as_tensor(a)))
    def _A_setup():
        B, S, D = 1, 2048, 640
        return {
            "x": _w((lambda *s: torch.randn(*s, dtype=torch.bfloat16) * 0.05)(B, S, D)),
            "wqkv": _w((lambda *s: torch.randn(*s, dtype=torch.bfloat16) * 0.05)(D, 3 * D)),
            "wo": _w((lambda *s: torch.randn(*s, dtype=torch.bfloat16) * 0.05)(D, D)),
        }
    _A_in = _A_setup()

    def _B_setup():
        B, S, D, F = 1, 2048, 640, 1536
        return {
            "x": _w((lambda *s: torch.randn(*s, dtype=torch.bfloat16) * 0.05)(B, S, D)),
            "w1": _w((lambda *s: torch.randn(*s, dtype=torch.bfloat16) * 1.6)(D, F) * (D ** -0.5)),
            "b1": _w((lambda *s: torch.randn(*s, dtype=torch.bfloat16) * 0.4)(F)),
            "w2": _w((lambda *s: torch.randn(*s, dtype=torch.bfloat16) * 0.05)(F, D) * (F ** -0.5)),
            "b2": _w((lambda *s: torch.randn(*s, dtype=torch.bfloat16) * 0.05)(D)),
        }
    _B_in = _B_setup()

    return {
        "A_x": _A_in["x"],
        "A_wqkv": _A_in["wqkv"],
        "A_wo": _A_in["wo"],
        "B_x": _B_in["x"],
        "B_w1": _B_in["w1"],
        "B_b1": _B_in["b1"],
        "B_w2": _B_in["w2"],
        "B_b2": _B_in["b2"],
    }

def reference(A_x, A_wqkv, A_wo, B_x, B_w1, B_b1, B_w2, B_b2):
    def _A_ref(x, wqkv, wo):
        B, S, D = x.shape
        H = 32; Dh = D // H
        qkv = (lambda x, *d: x.permute(*d))((x @ wqkv).reshape(B, S, 3, H, Dh), 2, 0, 3, 1, 4)
        q, k, v = qkv[0], qkv[1], qkv[2]
        phi = lambda t: torch.where(t > 0, t + 1, torch.exp(t)) + 1e-6
        pq, pk = phi(q), phi(k)
        kv = torch.einsum("bhsd,bhse->bhde", pk, v)
        z = torch.einsum("bhsd->bhd", pk)
        num = torch.einsum("bhqd,bhde->bhqe", pq, kv)
        den = torch.einsum("bhqd,bhd->bhq", pq, z)[..., None] + 1e-6
        o = num / den
        return (lambda x, *d: x.permute(*d))(o, 0, 2, 1, 3).reshape(B, S, D) @ wo

    def _B_ref(x, w1, b1, w2, b2):
        y = _layernorm(x, axis=-1)
        h = _gelu(torch.einsum("bsd,df->bsf", y, w1) + b1)
        return torch.einsum("bsf,fd->bsd", h, w2) + b2

    x = A_x
    h = x + _A_ref(_layernorm(x, axis=-1), A_wqkv, A_wo)
    h2 = h + _B_ref(_layernorm(h, axis=-1), B_w1, B_b1, B_w2, B_b2)
    return h2
```

```cpp
#include <hip/hip_runtime.h>
#include <hip/hip_bf16.h>
typedef unsigned short u16;
__device__ __forceinline__ float bf2f(u16 b){ return __uint_as_float(((unsigned)b)<<16); }
__device__ __forceinline__ u16 f2bf(float f){ unsigned u=__float_as_uint(f); u += 0x7fffu + ((u>>16)&1u); return (u16)(u>>16); }
constexpr int S=2048,D=640,F=1536,H=32,DH=20;

__global__ void k_ln(const u16* x, float* y, int twice){
  int row=blockIdx.x; int t=threadIdx.x;
  float v[10]; float s=0;
  for(int i=0;i<10;i++){ v[i]=bf2f(x[row*D+t+64*i]); s+=v[i]; }
  for(int r=0;r<=twice;r++){
    s=0; for(int i=0;i<10;i++) s+=v[i];
    for(int m=32;m>=1;m>>=1) s+=__shfl_xor(s,m);
    float mean=s/D; float q=0;
    for(int i=0;i<10;i++){ float d=v[i]-mean; q+=d*d; }
    for(int m=32;m>=1;m>>=1) q+=__shfl_xor(q,m);
    float rs=1.0f/sqrtf(q/D+1e-5f);
    for(int i=0;i<10;i++) v[i]=(v[i]-mean)*rs;
  }
  for(int i=0;i<10;i++) y[row*D+t+64*i]=v[i];
}
__global__ void k_gemm(const float* A, const u16* W, float* C, int N, int K){
  __shared__ float sa[16][65];
  int n=blockIdx.x*64+threadIdx.x%64; int ty=threadIdx.x/64;
  int m0=blockIdx.y*16;
  float acc[4]={0,0,0,0};
  for(int k0=0;k0<K;k0+=64){
    for(int e=threadIdx.x;e<16*64;e+=256) sa[e/64][e%64]=A[(m0+e/64)*K+k0+e%64];
    __syncthreads();
    for(int k=0;k<64;k++){ float w=bf2f(W[(k0+k)*N+n]); for(int j=0;j<4;j++) acc[j]+=sa[ty*4+j][k]*w; }
    __syncthreads();
  }
  for(int j=0;j<4;j++) C[(m0+ty*4+j)*N+n]=acc[j];
}
__device__ __forceinline__ float phi(float t){ return (t>0? t+1.0f : expf(t)) + 1e-6f; }
__global__ void k_kv(const float* qkv, float* kv){
  int h=blockIdx.x; int t=threadIdx.x; if(t>=420) return; int d=t/21,e=t%21;
  float acc=0;
  for(int s=0;s<S;s++){ float pk=phi(qkv[s*1920+640+h*20+d]); float v = e<20? qkv[s*1920+1280+h*20+e]:1.0f; acc+=pk*v; }
  kv[h*420+t]=acc;
}
__global__ void k_o(const float* qkv, const float* kv, float* o){
  int s=blockIdx.x; int c=threadIdx.x; int h=c/20,e=c%20;
  float num=0,den=0;
  for(int d=0;d<20;d++){ float pq=phi(qkv[s*1920+h*20+d]); num+=pq*kv[h*420+d*21+e]; den+=pq*kv[h*420+d*21+20]; }
  o[s*D+c]=num/(den+1e-6f);
}
__global__ void k_h(const u16* x, const float* a, u16* h){ int i=blockIdx.x*256+threadIdx.x; h[i]=f2bf(bf2f(x[i])+a[i]); }
__global__ void k_gelu(float* t, const u16* b1){ int i=blockIdx.x*256+threadIdx.x; float v=t[i]+bf2f(b1[i%F]); float u=0.7978845608f*(v+0.044715f*v*v*v); t[i]=0.5f*v*(1.0f+tanhf(u)); }
__global__ void k_out(const u16* h, const float* m, const u16* b2, u16* out){ int i=blockIdx.x*256+threadIdx.x; out[i]=f2bf(bf2f(h[i])+m[i]+bf2f(b2[i%D])); }

extern "C" void kernel_launch(void* const* d_in, const int* in_sizes, int n_in,
                              void* d_out, int out_size, void* d_ws, size_t ws_size,
                              hipStream_t stream) {
  const u16* x=(const u16*)d_in[0]; const u16* wqkv=(const u16*)d_in[1]; const u16* wo=(const u16*)d_in[2];
  const u16* w1=(const u16*)d_in[4]; const u16* b1=(const u16*)d_in[5]; const u16* w2=(const u16*)d_in[6]; const u16* b2=(const u16*)d_in[7];
  float* ws=(float*)d_ws;
  float* xn=ws; float* qkv=xn+S*D; float* kv=qkv+S*1920; float* o=kv+32*420; float* a=o+S*D; float* y=a+S*D; float* t=y+S*D; float* m=t+S*F; u16* h=(u16*)(m+S*D);
  k_ln<<<S,64,0,stream>>>(x,xn,0);
  k_gemm<<<dim3(1920/64,S/16),256,0,stream>>>(xn,wqkv,qkv,1920,640);
  k_kv<<<32,448,0,stream>>>(qkv,kv);
  k_o<<<S,640,0,stream>>>(qkv,kv,o);
  k_gemm<<<dim3(640/64,S/16),256,0,stream>>>(o,wo,a,640,640);
  k_h<<<S*D/256,256,0,stream>>>(x,a,h);
  k_ln<<<S,64,0,stream>>>(h,y,1);
  k_gemm<<<dim3(F/64,S/16),256,0,stream>>>(y,w1,t,F,640);
  k_gelu<<<S*F/256,256,0,stream>>>(t,b1);
  k_gemm<<<dim3(640/64,S/16),256,0,stream>>>(t,w2,m,640,F);
  k_out<<<S*D/256,256,0,stream>>>(h,m,b2,(u16*)d_out);
}
```

```cpp
#include <hip/hip_runtime.h>
#include <hip/hip_bf16.h>
#include <stdint.h>

#define LAS __attribute__((address_space(3)))
#define GAS __attribute__((address_space(1)))
#define DI __device__ __forceinline__
typedef __attribute__((ext_vector_type(8))) __bf16 bf16x8;
typedef __attribute__((ext_vector_type(2))) __bf16 bf16x2;
typedef __attribute__((ext_vector_type(4))) float f32x4;
typedef __attribute__((ext_vector_type(2))) float f32x2;
typedef __attribute__((ext_vector_type(4))) unsigned v4u;
typedef __attribute__((ext_vector_type(2))) unsigned v2u;
typedef __attribute__((ext_vector_type(4))) short s16x4;
typedef __amdgpu_buffer_rsrc_t rsrc_t;

constexpr int S = 2048, D = 640, F = 1536;
constexpr int ACT = S * D * 2;
constexpr int PK_OFF = 0, PV_OFF = PK_OFF + ACT, PQ_OFF = PV_OFF + ACT, OF_OFF = PQ_OFF + ACT, HF_OFF = OF_OFF + ACT;
constexpr int GF_OFF = HF_OFF + ACT, WOP_OFF = GF_OFF + S * F * 2, W1P_OFF = WOP_OFF + D * D * 2, W2P_OFF = W1P_OFF + D * F * 2;
constexpr int KVP_OFF = W2P_OFF + D * F * 2, ST_OFF = KVP_OFF + 32 * 8 * 4096, WS_END = ST_OFF + S * 16;

DI rsrc_t mkrs(const void* p, int bytes) { return __builtin_amdgcn_make_buffer_rsrc((void*)p, 0, bytes, 0x00020000); }
DI f32x4 mfma(v4u a, v4u b, f32x4 c) {
  return __builtin_amdgcn_mfma_f32_16x16x32_bf16(__builtin_bit_cast(bf16x8, a), __builtin_bit_cast(bf16x8, b), c, 0, 0, 0);
}
DI unsigned pk2(float a, float b) { f32x2 f = {a, b}; return __builtin_bit_cast(unsigned, __builtin_convertvector(f, bf16x2)); }
DI float bflo(unsigned u) { return __uint_as_float(u << 16); }
DI float bfhi(unsigned u) { return __uint_as_float(u & 0xffff0000u); }
DI v4u ldb(rsrc_t r, int voff, int soff) { return __builtin_amdgcn_raw_buffer_load_b128(r, voff, soff, 0); }
DI void stb(v4u v, rsrc_t r, int voff, int soff) { __builtin_amdgcn_raw_buffer_store_b128(v, r, voff, soff, 0); }
DI void glds(rsrc_t r, LAS char* dst, int voff, int soff) { __builtin_amdgcn_raw_ptr_buffer_load_lds(r, (LAS void*)dst, 16, voff, soff, 0, 0); }
DI v2u trread(LAS char* p) { return __builtin_bit_cast(v2u, __builtin_amdgcn_ds_read_tr16_b64_v4i16((LAS s16x4*)p)); }
#define WAITV(n) asm volatile("s_waitcnt vmcnt(" #n ")" ::: "memory")
#define BAR() do { asm volatile("s_waitcnt lgkmcnt(0)" ::: "memory"); __builtin_amdgcn_s_barrier(); asm volatile("" ::: "memory"); } while (0)
constexpr float LOG2E = 1.4426950408889634f;
DI float phi(float t) { return fmaxf(t, 0.f) + __builtin_amdgcn_fmed3f(__builtin_amdgcn_exp2f(t * LOG2E), 0.f, 1.f) + 1e-6f; }

__global__ __launch_bounds__(256) void k_stats(const unsigned short* x, char* ws) {
  const int row = blockIdx.x * 4 + (threadIdx.x >> 6), l = threadIdx.x & 63;
  float v[10], s = 0;
  for (int i = 0; i < 5; i++) { unsigned u = ((const unsigned*)x)[row * 320 + l + 64 * i]; v[2 * i] = bflo(u); v[2 * i + 1] = bfhi(u); s += v[2 * i] + v[2 * i + 1]; }
  for (int m = 32; m >= 1; m >>= 1) s += __shfl_xor(s, m);
  const float mean = s * (1.f / D); float q = 0;
  for (int i = 0; i < 10; i++) { float d = v[i] - mean; q += d * d; }
  for (int m = 32; m >= 1; m >>= 1) q += __shfl_xor(q, m);
  if (l == 0) { f32x2 o = {mean, 1.0f / sqrtf(q * (1.f / D) + 1e-5f)}; *(f32x2*)(ws + ST_OFF + row * 8) = o; }
}

DI int krow_wo(int ks, int rho) { int sg = 4 * ks + (rho >> 3); int p = sg / 5, i = sg - 5 * p; return 40 * p + 4 * i + 20 * ((rho >> 2) & 1) + (rho & 3); }
DI int krow_w2(int ks, int rho) { int sg = 4 * ks + (rho >> 3); int W = sg / 6, i = sg - 6 * W;
  return 48 * W + (i < 4 ? 4 * i + 16 * ((rho >> 2) & 1) + (rho & 3) : 32 + 8 * (i - 4) + (rho & 7)); }
constexpr int NU_WO = 200, NU_W1 = 480, NU_W2 = 480, NU = NU_WO + NU_W1 + NU_W2;
struct PackInfo { int dst; int tstride; };
DI PackInfo pack_issue(int u, const void* wo, const void* w1, const void* w2, LAS char* stg, int l) {
  int mat, ks, cb, rowb; const void* base; PackInfo pi;
  if (u < NU_WO) { mat = 0; ks = u / 10; cb = u - ks * 10; rowb = D * 2; base = wo; pi.dst = WOP_OFF + (cb * 4 * 20 + ks) * 1024; pi.tstride = 20 * 1024; }
  else if (u < NU_WO + NU_W1) { int v = u - NU_WO; mat = 1; ks = v / 24; cb = v - ks * 24; rowb = F * 2; base = w1; pi.dst = W1P_OFF + (cb * 4 * 20 + ks) * 1024; pi.tstride = 20 * 1024; }
  else { int v = u - NU_WO - NU_W1; mat = 2; ks = v / 10; cb = v - ks * 10; rowb = D * 2; base = w2; pi.dst = W2P_OFF + (cb * 4 * 48 + ks) * 1024; pi.tstride = 48 * 1024; }
  rsrc_t rs = mkrs(base, mat == 0 ? D * D * 2 : D * F * 2);
#pragma unroll
  for (int i = 0; i < 4; i++) {
    const int rho = 8 * i + (l >> 3);
    const int kr = mat == 0 ? krow_wo(ks, rho) : (mat == 1 ? 32 * ks + rho : krow_w2(ks, rho));
    const int f = ((rho >> 1) & 1) | (((rho >> 3) & 1) << 1);
    const int ch = (l & 7) ^ (f << 1);
    glds(rs, stg + i * 1024, kr * rowb + cb * 128 + ch * 16, 0);
  }
  return pi;
}
DI void pack_finish(PackInfo pi, rsrc_t wsr, LAS char* stg, int l) {
  const int g = l >> 4, q = (l >> 2) & 3, p = l & 3;
#pragma unroll
  for (int t = 0; t < 4; t++) {
    v2u h[2];
#pragma unroll
    for (int hf = 0; hf < 2; hf++) {
      const int rho = 8 * g + 4 * hf + q;
      const int f = ((rho >> 1) & 1) | (((rho >> 3) & 1) << 1);
      h[hf] = trread(stg + rho * 128 + ((t ^ f) * 32) + p * 8);
    }
    stb(v4u{h[0].x, h[0].y, h[1].x, h[1].y}, wsr, pi.dst + t * pi.tstride + l * 16, 0);
  }
}

constexpr int P1_SLOT = 32768, P1_NS = 4, P1_STG = P1_NS * P1_SLOT, P1_TAB = P1_STG + 8 * 4096;
template <int WN, bool ISQ>
DI void p1_gemm(const void* x, const void* wqkv, char* ws, LAS char* L, int rb, int ct, int rot, int punit, const void* wo, const void* w1, const void* w2) {
  constexpr int NW = 2 * WN, NTL = 8 / WN, GPW = 16 / NW;
  const int tid = threadIdx.x, l = tid & 63, w = __builtin_amdgcn_readfirstlane(tid >> 6), wr = w / WN, wc = w % WN;
  const int c = l & 15, g = l >> 4;
  const rsrc_t rx = mkrs(x, S * D * 2), rw = mkrs(wqkv, D * 1920 * 2), rws = mkrs(ws, WS_END);
  int va[GPW], vb[GPW];
#pragma unroll
  for (int u = 0; u < GPW; u++) {
    const int ra = (w * GPW + u) * 8 + (l >> 3);
    va[u] = (rb * 128 + ra) * 1280 + (((l & 7) ^ ((ra >> 1) & 7)) * 16);
    const int rk = (w * GPW + u) * 4 + (l >> 4);
    const int ch = (l & 15) ^ ((((rk & 3) | (((rk >> 3) & 1) << 2))) << 1);
    const int colb = ISQ ? ct * 256 + ch * 16 : 1280 + (ch >> 3) * 1280 + (ct - 5) * 128 + (ch & 7) * 16;
    vb[u] = rk * 3840 + colb;
  }
  auto issue = [&](int s) {
    int st = s + rot; st = st >= 10 ? st - 10 : st;
    LAS char* slot = L + (s & 3) * P1_SLOT;
#pragma unroll
    for (int u = 0; u < GPW; u++) glds(rx, slot + (w * GPW + u) * 1024, va[u], st * 128);
#pragma unroll
    for (int u = 0; u < GPW; u++) glds(rw, slot + 16384 + (w * GPW + u) * 1024, vb[u], st * 64 * 3840);
  };
  issue(0); issue(1); issue(2);
  f32x4 acc[4][NTL], cs[NTL];
#pragma unroll
  for (int n = 0; n < NTL; n++) { cs[n] = f32x4{0, 0, 0, 0};
#pragma unroll
    for (int m = 0; m < 4; m++) acc[m][n] = f32x4{0, 0, 0, 0}; }
  const v4u ones = {0x3f803f80u, 0x3f803f80u, 0x3f803f80u, 0x3f803f80u};
  int aoff[2], boff[NTL];
#pragma unroll
  for (int kk = 0; kk < 2; kk++) aoff[kk] = (wr * 64 + c) * 128 + (((4 * kk + g) ^ ((c >> 1) & 7)) * 16);
#pragma unroll
  for (int n = 0; n < NTL; n++) {
    const int tile = ISQ ? wc * NTL + n : (n / (NTL / 2)) * 4 + wc * (NTL / 2) + (n % (NTL / 2));
    const int q = (l >> 2) & 3, p = l & 3;
    const int f = q | ((g & 1) << 2);
    boff[n] = 16384 + (8 * g + q) * 256 + ((tile ^ f) * 32) + p * 8;
  }
  PackInfo pi; pi.dst = 0; pi.tstride = 0;
#pragma unroll 1
  for (int s = 0; s < 10; s++) {
    if (s <= 7) { if constexpr (GPW == 4) WAITV(16); else WAITV(8); }
    else if (s == 8) { if constexpr (GPW == 4) WAITV(12); else WAITV(8); }
    else WAITV(4);
    BAR();
    if (s < 7) issue(s + 3);
    else if (s == 7) pi = pack_issue(punit, wo, w1, w2, L + P1_STG + w * 4096, l);
    LAS char* slot = L + (s & 3) * P1_SLOT;
#pragma unroll
    for (int kk = 0; kk < 2; kk++) {
      v4u a[4], b[NTL];
#pragma unroll
      for (int m = 0; m < 4; m++) a[m] = *(LAS v4u*)(slot + aoff[kk] + m * 2048);
#pragma unroll
      for (int n = 0; n < NTL; n++) {
        v2u lo = trread(slot + boff[n] + kk * 8192), hi = trread(slot + boff[n] + kk * 8192 + 1024);
        b[n] = v4u{lo.x, lo.y, hi.x, hi.y};
      }
#pragma unroll
      for (int n = 0; n < NTL; n++) {
#pragma unroll
        for (int m = 0; m < 4; m++) acc[m][n] = ISQ ? mfma(b[n], a[m], acc[m][n]) : mfma(a[m], b[n], acc[m][n]);
        cs[n] = ISQ ? mfma(b[n], ones, cs[n]) : mfma(ones, b[n], cs[n]);
      }
    }
  }
  const int row0 = rb * 128 + wr * 64;
  if constexpr (ISQ) {
    f32x2 st[4];
#pragma unroll
    for (int m = 0; m < 4; m++) st[m] = *(const f32x2*)(ws + ST_OFF + (row0 + 16 * m + c) * 8);
#pragma unroll
    for (int n = 0; n < NTL; n++) {
      const int q = (ct * 128 + (wc * NTL + n) * 16) / 4 + g;
#pragma unroll
      for (int u = 0; u < 2; u++) {
        v4u o;
#pragma unroll
        for (int hh = 0; hh < 2; hh++) {
          const int m = 2 * u + hh; float v[4];
#pragma unroll
          for (int j = 0; j < 4; j++) v[j] = phi(st[m].y * (acc[m][n][j] - st[m].x * cs[n][j]));
          o[2 * hh] = pk2(v[0], v[1]); o[2 * hh + 1] = pk2(v[2], v[3]);
        }
        stb(o, rws, PQ_OFF + ((q * 64 + (row0 >> 5) + u) * 16 + c) * 16, 0);
      }
    }
  } else {
    f32x4 st[4][2];
#pragma unroll
    for (int m = 0; m < 4; m++) { const f32x4* p = (const f32x4*)(ws + ST_OFF + (row0 + 16 * m + 4 * g) * 8); st[m][0] = p[0]; st[m][1] = p[1]; }
#pragma unroll
    for (int n = 0; n < NTL; n++) {
      const bool isk = n < NTL / 2;
      const int f = (ct - 5) * 64 + (wc * (NTL / 2) + (n % (NTL / 2))) * 16 + c;
      const int h = f / 20, d = f - 20 * h;
#pragma unroll
      for (int u = 0; u < 2; u++) {
        v4u o;
#pragma unroll
        for (int hh = 0; hh < 2; hh++) {
          const int m = 2 * u + hh; float v[4];
#pragma unroll
          for (int j = 0; j < 4; j++) {
            const float mean = st[m][j >> 1][(j & 1) * 2], rs = st[m][j >> 1][(j & 1) * 2 + 1];
            const float t = rs * (acc[m][n][j] - mean * cs[n][0]);
            v[j] = isk ? phi(t) : t;
          }
          o[2 * hh] = pk2(v[0], v[1]); o[2 * hh + 1] = pk2(v[2], v[3]);
        }
        stb(o, rws, (isk ? PK_OFF : PV_OFF) + ((((h * 64 + (row0 >> 5) + u) * 4 + g) * 20) + d) * 16, 0);
      }
    }
  }
  WAITV(0);
  pack_finish(pi, rws, L + P1_STG + w * 4096, l);
}

template <int WN>
__global__ __launch_bounds__(128 * WN) void k_p1(const void* x, const void* wqkv, const void* wo, const void* w1, const void* w2, char* ws) {
  extern __shared__ __attribute__((aligned(16))) char smem[];
  LAS char* L = (LAS char*)smem;
  constexpr int NW = 2 * WN;
  const int xg = blockIdx.x & 7, i = blockIdx.x >> 3;
  const int w = __builtin_amdgcn_readfirstlane(threadIdx.x >> 6), l = threadIdx.x & 63;
  if (i < 30) {
    const int rb = xg * 2 + (i & 1), ct = i >> 1;
    const int wg = xg * 30 + i;
    int pu = wg * NW + w; pu = pu < NU ? pu : NU - 1;
    const int rot = (ct + 5 * (i & 1)) % 10;
    if (ct < 5) p1_gemm<WN, true>(x, wqkv, ws, L, rb, ct, rot, pu, wo, w1, w2);
    else p1_gemm<WN, false>(x, wqkv, ws, L, rb, ct, rot, pu, wo, w1, w2);
  } else {
    const rsrc_t rws = mkrs(ws, WS_END);
    const int wi = (xg * 2 + (i - 30)) * NW + w;
    for (int u = 240 * NW + wi; u < NU; u += 16 * NW) {
      PackInfo pi = pack_issue(u, wo, w1, w2, L + w * 4096, l);
      WAITV(0);
      pack_finish(pi, rws, L + w * 4096, l);
      asm volatile("s_waitcnt lgkmcnt(0)" ::: "memory");
    }
  }
}

DI void kv_stage(char* ws, int xg, int h) {
  const int l = threadIdx.x & 63, w = __builtin_amdgcn_readfirstlane(threadIdx.x >> 6), c = l & 15, g = l >> 4;
  const int dt = w >> 1, et = w & 1;
  const rsrc_t rws = mkrs(ws, WS_END);
  const int dd = dt ? 16 + (c & 3) : c, ee = et ? 16 + (c & 3) : c;
  const int ak = PK_OFF + (((h * 64 + xg * 8) * 4 + g) * 20 + dd) * 16, av = PV_OFF + (((h * 64 + xg * 8) * 4 + g) * 20 + ee) * 16;
  v4u a[8], b[8];
#pragma unroll
  for (int sb = 0; sb < 8; sb++) { a[sb] = ldb(rws, ak, sb * 1280); b[sb] = ldb(rws, av, sb * 1280); }
  f32x4 acc = {0, 0, 0, 0};
  const bool one = et && c >= 4;
#pragma unroll
  for (int sb = 0; sb < 8; sb++) {
    v4u bb = b[sb];
    if (one) bb = v4u{0x3f803f80u, 0x3f803f80u, 0x3f803f80u, 0x3f803f80u};
    acc = mfma(a[sb], bb, acc);
  }
  stb(__builtin_bit_cast(v4u, acc), rws, KVP_OFF + ((h * 8 + xg) * 4 + w) * 1024 + l * 16, 0);
}
__global__ __launch_bounds__(256) void k_kv(char* ws) { kv_stage(ws, blockIdx.x & 7, blockIdx.x >> 3); }

DI void o_stage(char* ws, LAS char* L, int rb, int hp) {
  const int l = threadIdx.x & 63, w = __builtin_amdgcn_readfirstlane(threadIdx.x >> 6), c = l & 15, g = l >> 4;
  const rsrc_t rws = mkrs(ws, WS_END);
  const int sb = rb * 4 + w;
  v4u q1[2], q2[2];
#pragma unroll
  for (int hh = 0; hh < 2; hh++) {
    const int h = 2 * hp + hh;
    q1[hh] = ldb(rws, PQ_OFF + (((5 * h + g) * 64 + sb) * 16 + c) * 16, 0);
    q2[hh] = ldb(rws, PQ_OFF + (((5 * h + 4) * 64 + sb) * 16 + c) * 16, 0);
  }
  {
    const int h = 2 * hp + (w >> 1), et = w & 1;
    f32x4 p0[8], p1[8];
#pragma unroll
    for (int x = 0; x < 8; x++) {
      p0[x] = __builtin_bit_cast(f32x4, ldb(rws, KVP_OFF + (h * 8 * 4 + et) * 1024 + l * 16, x * 4096));
      p1[x] = __builtin_bit_cast(f32x4, ldb(rws, KVP_OFF + (h * 8 * 4 + 2 + et) * 1024 + c * 16, x * 4096));
    }
    f32x4 s0 = p0[0], s1 = p1[0];
#pragma unroll
    for (int x = 1; x < 8; x++) { s0 += p0[x]; s1 += p1[x]; }
    v4u fr = {pk2(s0[0], s0[1]), pk2(s0[2], s0[3]), g == 0 ? pk2(s1[0], s1[1]) : 0u, g == 0 ? pk2(s1[2], s1[3]) : 0u};
    *(LAS v4u*)(L + w * 1024 + l * 16) = fr;
  }
  __syncthreads();
  v4u kf[4];
#pragma unroll
  for (int i = 0; i < 4; i++) kf[i] = *(LAS v4u*)(L + i * 1024 + l * 16);
#pragma unroll
  for (int t = 0; t < 2; t++) {
    unsigned o0[4], o1[4];
#pragma unroll
    for (int hh = 0; hh < 2; hh++) {
      const v4u bq = {q1[hh][2 * t], q1[hh][2 * t + 1], q2[hh][2 * t], q2[hh][2 * t + 1]};
      const f32x4 z = {0, 0, 0, 0};
      f32x4 n0 = mfma(kf[2 * hh], bq, z), n1 = mfma(kf[2 * hh + 1], bq, z);
      auto sw = __builtin_amdgcn_permlane16_swap(__float_as_uint(n1[0]), __float_as_uint(n1[0]), false, false);
      const float den = __uint_as_float(sw[1]) + 1e-6f;
      const float r = 1.0f / den;
      o0[2 * hh] = pk2(n0[0] * r, n0[1] * r); o0[2 * hh + 1] = pk2(n0[2] * r, n0[3] * r);
      o1[2 * hh] = pk2(n1[0] * r, n1[1] * r); o1[2 * hh + 1] = pk2(n1[2] * r, n1[3] * r);
    }
    const int base = OF_OFF + (sb * 2 + t) * 20480 + 5 * hp * 256;
    stb(v4u{o0[0], o0[1], o0[2], o0[3]}, rws, base + l * 16, 0);
    if (g == 0) stb(v4u{o1[0], o1[1], o1[2], o1[3]}, rws, base + 1024 + l * 16, 0);
  }
}
__global__ __launch_bounds__(256) void k_o(char* ws) {
  __shared__ __attribute__((aligned(16))) char sm[4096];
  const int xg = blockIdx.x & 7, i = blockIdx.x >> 3;
  o_stage(ws, (LAS char*)sm, xg * 2 + (i & 1), i >> 1);
}

constexpr int RED_ROW = 336, RED_W = 64 * RED_ROW;
template <int KSW>
DI void gemm80(rsrc_t rws, LAS char* L, int aoff, int woff, int grp, int j) {
  const int l = threadIdx.x & 63, w = __builtin_amdgcn_readfirstlane(threadIdx.x >> 6), c = l & 15, g = l >> 4;
  constexpr int KS = 4 * KSW;
  v4u a[4][KSW], b[5][KSW];
#pragma unroll
  for (int n = 0; n < 5; n++)
#pragma unroll
    for (int k = 0; k < KSW; k++) b[n][k] = ldb(rws, l * 16, woff + ((5 * j + n) * KS + w * KSW + k) * 1024);
#pragma unroll
  for (int m = 0; m < 4; m++)
#pragma unroll
    for (int k = 0; k < KSW; k++) a[m][k] = ldb(rws, l * 16, aoff + ((grp * 4 + m) * KS + w * KSW + k) * 1024);
  __builtin_amdgcn_sched_barrier(0);
#pragma unroll
  for (int m = 0; m < 4; m++) {
    f32x4 acc[5];
#pragma unroll
    for (int n = 0; n < 5; n++) acc[n] = f32x4{0, 0, 0, 0};
#pragma unroll
    for (int k = 0; k < KSW; k++)
#pragma unroll
      for (int n = 0; n < 5; n++) acc[n] = mfma(b[n][k], a[m][k], acc[n]);
#pragma unroll
    for (int n = 0; n < 5; n++) *(LAS f32x4*)(L + w * RED_W + (16 * m + c) * RED_ROW + n * 64 + g * 16) = acc[n];
  }
  __syncthreads();
}
DI void red8(LAS char* L, int tok, int sl, float (&v)[8]) {
  f32x4 s0 = {0, 0, 0, 0}, s1 = {0, 0, 0, 0};
#pragma unroll
  for (int ww = 0; ww < 4; ww++) { LAS f32x4* p = (LAS f32x4*)(L + ww * RED_W + tok * RED_ROW + sl * 32); s0 += p[0]; s1 += p[1]; }
#pragma unroll
  for (int i = 0; i < 4; i++) { v[i] = s0[i]; v[4 + i] = s1[i]; }
}
__global__ __launch_bounds__(256) void k_p2(const void* x, char* ws) {
  extern __shared__ __attribute__((aligned(16))) char smem[];
  LAS char* L = (LAS char*)smem;
  const int xg = blockIdx.x & 7, i = blockIdx.x >> 3, grp = xg * 4 + (i >> 3), j = i & 7;
  const rsrc_t rws = mkrs(ws, WS_END), rx = mkrs(x, S * D * 2);
  v4u xr[3];
#pragma unroll
  for (int r = 0; r < 3; r++) { const int it = threadIdx.x + 256 * r; const int rt = it / 160, e = it - rt * 160, sl = e >> 4, tok = rt * 16 + (e & 15);
    xr[r] = ldb(rx, it < 640 ? (grp * 64 + tok) * 1280 + j * 160 + sl * 16 : 0, 0); }
  gemm80<5>(rws, L, OF_OFF, WOP_OFF, grp, j);
#pragma unroll
  for (int r = 0; r < 3; r++) { const int it = threadIdx.x + 256 * r; const int rt = it / 160, e = it - rt * 160, sl = e >> 4, tok = rt * 16 + (e & 15);
    if (it < 640) {
      float v[8]; red8(L, tok, sl, v);
      v4u o;
#pragma unroll
      for (int q = 0; q < 4; q++) o[q] = pk2(v[2 * q] + bflo(xr[r][q]), v[2 * q + 1] + bfhi(xr[r][q]));
      stb(o, rws, HF_OFF + (grp * 4 + rt) * 20480 + (10 * j) * 256 + e * 16, 0);
    }
  }
}
__global__ __launch_bounds__(256) void k_p4(const void* b2, void* out, char* ws) {
  extern __shared__ __attribute__((aligned(16))) char smem[];
  LAS char* L = (LAS char*)smem;
  const int xg = blockIdx.x & 7, i = blockIdx.x >> 3, grp = xg * 4 + (i >> 3), j = i & 7;
  const rsrc_t rws = mkrs(ws, WS_END), ro = mkrs(out, S * D * 2), rb2 = mkrs(b2, D * 2);
  v4u hr[3], br[3];
#pragma unroll
  for (int r = 0; r < 3; r++) { const int it = threadIdx.x + 256 * r; const int tok = it / 10, sl = it - tok * 10;
    hr[r] = ldb(rws, it < 640 ? HF_OFF + (grp * 4 + (tok >> 4)) * 20480 + (10 * j + sl) * 256 + (tok & 15) * 16 : 0, 0);
    br[r] = ldb(rb2, j * 160 + sl * 16, 0); }
  gemm80<12>(rws, L, GF_OFF, W2P_OFF, grp, j);
#pragma unroll
  for (int r = 0; r < 3; r++) { const int it = threadIdx.x + 256 * r; const int tok = it / 10, sl = it - tok * 10;
    if (it < 640) {
      float v[8]; red8(L, tok, sl, v);
      v4u o;
#pragma unroll
      for (int q = 0; q < 4; q++) o[q] = pk2(v[2 * q] + bflo(br[r][q]) + bflo(hr[r][q]), v[2 * q + 1] + bfhi(br[r][q]) + bfhi(hr[r][q]));
      stb(o, ro, (grp * 64 + tok) * 1280 + j * 160 + sl * 16, 0);
    }
  }
}

DI float dot2(unsigned a, unsigned b, float c) { return __builtin_amdgcn_fdot2_f32_bf16(__builtin_bit_cast(bf16x2, a), __builtin_bit_cast(bf16x2, b), c, false); }
DI float gelu(float v) {
  const float u = v * (0.7978845608028654f * 2.f * LOG2E) * __builtin_fmaf(v * v, 0.044715f, 1.0f);
  const float e = __builtin_amdgcn_exp2f(-u);
  return v * __builtin_amdgcn_rcpf(1.0f + e);
}
constexpr int P3_A = 0, P3_ST = 81920;
__global__ __launch_bounds__(256) void k_p3(const void* b1, char* ws) {
  extern __shared__ __attribute__((aligned(16))) char smem[];
  LAS char* L = (LAS char*)smem;
  const int xg = blockIdx.x & 7, i = blockIdx.x >> 3, grp = xg * 4 + (i >> 3), j = i & 7;
  const int l = threadIdx.x & 63, w = __builtin_amdgcn_readfirstlane(threadIdx.x >> 6), c = l & 15, g = l >> 4;
  const rsrc_t rws = mkrs(ws, WS_END), rb1 = mkrs(b1, F * 2);
  v4u a[4][5], b[3][20];
#pragma unroll
  for (int m = 0; m < 4; m++)
#pragma unroll
    for (int k = 0; k < 5; k++) a[m][k] = ldb(rws, l * 16, HF_OFF + ((grp * 4 + m) * 20 + w * 5 + k) * 1024);
#pragma unroll
  for (int k = 0; k < 20; k++)
#pragma unroll
    for (int n = 0; n < 3; n++) b[n][k] = ldb(rws, l * 16, W1P_OFF + ((12 * j + 3 * w + n) * 20 + k) * 1024);
  v2u bias[3];
#pragma unroll
  for (int n = 0; n < 3; n++) bias[n] = __builtin_amdgcn_raw_buffer_load_b64(rb1, ((12 * j + 3 * w + n) * 16 + 4 * g) * 2, 0, 0);
  __builtin_amdgcn_sched_barrier(0);
#pragma unroll
  for (int m = 0; m < 4; m++) {
    float s = 0, q = 0;
#pragma unroll
    for (int k = 0; k < 5; k++) {
#pragma unroll
      for (int e = 0; e < 4; e++) { s = dot2(a[m][k][e], 0x3f803f80u, s); q = dot2(a[m][k][e], a[m][k][e], q); }
      *(LAS v4u*)(L + P3_A + (m * 20 + w * 5 + k) * 1024 + l * 16) = a[m][k];
    }
    s += __shfl_xor(s, 16); q += __shfl_xor(q, 16);
    s += __shfl_xor(s, 32); q += __shfl_xor(q, 32);
    if (g == 0) *(LAS f32x2*)(L + P3_ST + ((m * 4 + w) * 16 + c) * 8) = f32x2{s, q};
  }
  const v4u ones = {0x3f803f80u, 0x3f803f80u, 0x3f803f80u, 0x3f803f80u};
  f32x4 cs[3];
#pragma unroll
  for (int n = 0; n < 3; n++) cs[n] = f32x4{0, 0, 0, 0};
#pragma unroll
  for (int k = 0; k < 20; k++)
#pragma unroll
    for (int n = 0; n < 3; n++) cs[n] = mfma(b[n][k], ones, cs[n]);
  __syncthreads();
#pragma unroll
  for (int m = 0; m < 4; m++) {
    f32x4 acc[3];
#pragma unroll
    for (int n = 0; n < 3; n++) acc[n] = f32x4{0, 0, 0, 0};
#pragma unroll
    for (int k = 0; k < 20; k++) {
      const v4u af = *(LAS v4u*)(L + P3_A + (m * 20 + k) * 1024 + l * 16);
#pragma unroll
      for (int n = 0; n < 3; n++) acc[n] = mfma(b[n][k], af, acc[n]);
    }
    float s = 0, q = 0;
#pragma unroll
    for (int ww = 0; ww < 4; ww++) { const f32x2 p = *(LAS f32x2*)(L + P3_ST + ((m * 4 + ww) * 16 + c) * 8); s += p.x; q += p.y; }
    const float mean = s * (1.f / D);
    const float var = fmaxf(q * (1.f / D) - mean * mean, 0.f);
    const float r1 = __builtin_amdgcn_rsqf(var + 1e-5f);
    const float rs = r1 * __builtin_amdgcn_rsqf(var * r1 * r1 + 1e-5f);
    unsigned o[3][2];
#pragma unroll
    for (int n = 0; n < 3; n++) {
      float v[4];
#pragma unroll
      for (int e = 0; e < 4; e++) {
        const float bb = (e & 1) ? bfhi(bias[n][e >> 1]) : bflo(bias[n][e >> 1]);
        v[e] = gelu(__builtin_fmaf(rs, acc[n][e] - mean * cs[n][e], bb));
      }
      o[n][0] = pk2(v[0], v[1]); o[n][1] = pk2(v[2], v[3]);
    }
    const int base = GF_OFF + (grp * 4 + m) * 49152 + 6 * (4 * j + w) * 256;
    stb(v4u{o[0][0], o[0][1], o[1][0], o[1][1]}, rws, base + l * 16, 0);
    auto s0 = __builtin_amdgcn_permlane16_swap(o[2][0], o[2][0], false, false);
    auto s1 = __builtin_amdgcn_permlane16_swap(o[2][1], o[2][1], false, false);
    if (!(g & 1)) stb(v4u{s0[0], s1[0], s0[1], s1[1]}, rws, base + 1024 + (g >> 1) * 256 + c * 16, 0);
  }
}

#define P1_WN 2
extern "C" void kernel_launch(void* const* d_in, const int* in_sizes, int n_in, void* d_out, int out_size, void* d_ws, size_t ws_size, hipStream_t stream) {
  const void *x = d_in[0], *wqkv = d_in[1], *wo = d_in[2], *w1 = d_in[4], *b1 = d_in[5], *w2 = d_in[6], *b2 = d_in[7];
  char* ws = (char*)d_ws;
  (void)hipFuncSetAttribute((const void*)k_p1<P1_WN>, hipFuncAttributeMaxDynamicSharedMemorySize, 160 * 1024);
  (void)hipFuncSetAttribute((const void*)k_p2, hipFuncAttributeMaxDynamicSharedMemorySize, 160 * 1024);
  (void)hipFuncSetAttribute((const void*)k_p3, hipFuncAttributeMaxDynamicSharedMemorySize, 160 * 1024);
  (void)hipFuncSetAttribute((const void*)k_p4, hipFuncAttributeMaxDynamicSharedMemorySize, 160 * 1024);
  k_stats<<<S / 4, 256, 0, stream>>>((const unsigned short*)x, ws);
  k_p1<P1_WN><<<256, 128 * P1_WN, 160 * 1024, stream>>>(x, wqkv, wo, w1, w2, ws);
  k_kv<<<256, 256, 0, stream>>>(ws);
  k_o<<<256, 256, 0, stream>>>(ws);
  k_p2<<<256, 256, 4 * RED_W, stream>>>(x, ws);
  k_p3<<<256, 256, 84 * 1024, stream>>>(b1, ws);
  k_p4<<<256, 256, 4 * RED_W, stream>>>(b2, d_out, ws);
}
```

```cpp
#include <hip/hip_runtime.h>
#include <hip/hip_bf16.h>
#include <stdint.h>

#define LAS __attribute__((address_space(3)))
#define DI __device__ __forceinline__
typedef __attribute__((ext_vector_type(8))) __bf16 bf16x8;
typedef __attribute__((ext_vector_type(2))) __bf16 bf16x2;
typedef __attribute__((ext_vector_type(4))) float f32x4;
typedef __attribute__((ext_vector_type(2))) float f32x2;
typedef __attribute__((ext_vector_type(4))) unsigned v4u;
typedef __attribute__((ext_vector_type(2))) unsigned v2u;
typedef __attribute__((ext_vector_type(4))) short s16x4;
typedef __amdgpu_buffer_rsrc_t rsrc_t;

constexpr int S = 2048, D = 640, F = 1536;
constexpr int ACT = S * D * 2;
constexpr int FL_OFF = 0;
constexpr int EPOCH_OFF = FL_OFF;
constexpr int FA_OFF = FL_OFF + 1024;
constexpr int FP_OFF = FA_OFF + 8 * 128;
constexpr int FQ_OFF = FP_OFF + 256;
constexpr int FB_OFF = FQ_OFF + 1024;
constexpr int FC_OFF = FB_OFF + 16 * 128;
constexpr int FD_OFF = FC_OFF + 16 * 128;
constexpr int FE_OFF = FD_OFF + 32 * 128;
constexpr int PK_OFF = 65536, PV_OFF = PK_OFF + ACT, PQ_OFF = PV_OFF + ACT, OF_OFF = PQ_OFF + ACT, HF_OFF = OF_OFF + ACT;
constexpr int GF_OFF = HF_OFF + ACT, WOP_OFF = GF_OFF + S * F * 2, W1P_OFF = WOP_OFF + D * D * 2, W2P_OFF = W1P_OFF + D * F * 2;
constexpr int KVP_OFF = W2P_OFF + D * F * 2, ST_OFF = KVP_OFF + 32 * 8 * 4096, WS_END = ST_OFF + S * 16;
static_assert(FE_OFF + 32 * 128 <= PK_OFF, "flags");
constexpr int SC1 = 16;
constexpr unsigned ONES2 = 0x3f803f80u;

DI rsrc_t mkrs(const void* p, int bytes) { return __builtin_amdgcn_make_buffer_rsrc((void*)p, 0, bytes, 0x00020000); }
DI v4u mkrs4(const void* p, int bytes) { const uint64_t a = (uint64_t)p; return v4u{(unsigned)a, (unsigned)(a >> 32) & 0xffffu, (unsigned)bytes, 0x00020000u}; }
DI f32x4 mfma(v4u a, v4u b, f32x4 c) {
  return __builtin_amdgcn_mfma_f32_16x16x32_bf16(__builtin_bit_cast(bf16x8, a), __builtin_bit_cast(bf16x8, b), c, 0, 0, 0);
}
DI void mfma_t(v4u a, v4u b, f32x4& c) { asm volatile("v_mfma_f32_16x16x32_bf16 %0, %1, %2, %0" : "+v"(c) : "v"(a), "v"(b)); }
DI unsigned pk2(float a, float b) { f32x2 f = {a, b}; return __builtin_bit_cast(unsigned, __builtin_convertvector(f, bf16x2)); }
DI float bflo(unsigned u) { return __uint_as_float(u << 16); }
DI float bfhi(unsigned u) { return __uint_as_float(u & 0xffff0000u); }
DI v4u ldp(rsrc_t r, int voff, int soff) { return __builtin_amdgcn_raw_buffer_load_b128(r, voff, soff, 0); }
DI v4u lds1(rsrc_t r, int voff, int soff) { return __builtin_amdgcn_raw_buffer_load_b128(r, voff, soff, SC1); }
DI void sts1(v4u v, rsrc_t r, int voff, int soff) { __builtin_amdgcn_raw_buffer_store_b128(v, r, voff, soff, SC1); }
DI v2u trread(LAS char* p) { return __builtin_bit_cast(v2u, __builtin_amdgcn_ds_read_tr16_b64_v4i16((LAS s16x4*)p)); }
#define WAITV(n) asm volatile("s_waitcnt vmcnt(" #n ")" ::: "memory")
#define BAR() do { asm volatile("s_waitcnt lgkmcnt(0)" ::: "memory"); __builtin_amdgcn_s_barrier(); asm volatile("" ::: "memory"); } while (0)
#define SB() __builtin_amdgcn_sched_barrier(0)
constexpr float LOG2E = 1.4426950408889634f;
DI float phi(float t) { return fmaxf(t, 0.f) + __builtin_amdgcn_fmed3f(__builtin_amdgcn_exp2f(t * LOG2E), 0.f, 1.f) + 1e-6f; }
DI float dot2(unsigned a, unsigned b, float c) { return __builtin_amdgcn_fdot2_f32_bf16(__builtin_bit_cast(bf16x2, a), __builtin_bit_cast(bf16x2, b), c, false); }
DI float gelu(float v) {
  const float u = v * (0.7978845608028654f * 2.f * LOG2E) * __builtin_fmaf(v * v, 0.044715f, 1.0f);
  return v * __builtin_amdgcn_rcpf(1.0f + __builtin_amdgcn_exp2f(-u));
}
template <int CTRL> DI float dppadd(float v) { return v + __uint_as_float(__builtin_amdgcn_update_dpp(0u, __float_as_uint(v), CTRL, 0xf, 0xf, true)); }
DI float sum16(float v) { v = dppadd<0xB1>(v); v = dppadd<0x4E>(v); v = dppadd<0x141>(v); return dppadd<0x140>(v); }
DI float add_x16(float v) { auto r = __builtin_amdgcn_permlane16_swap(__float_as_uint(v), __float_as_uint(v), false, false); return __uint_as_float(r[0]) + __uint_as_float(r[1]); }
DI float add_x32(float v) { auto r = __builtin_amdgcn_permlane32_swap(__float_as_uint(v), __float_as_uint(v), false, false); return __uint_as_float(r[0]) + __uint_as_float(r[1]); }

DI void glds4(v4u rs, unsigned dst, int soff, int v0, int v1, int v2, int v3) {
  asm volatile("s_nop 4\n\ts_mov_b32 m0, %1\n\ts_nop 0\n\tbuffer_load_dwordx4 %3, %0, %2 offen lds\n\t"
               "s_add_u32 m0, m0, 0x400\n\ts_nop 0\n\tbuffer_load_dwordx4 %4, %0, %2 offen lds\n\t"
               "s_add_u32 m0, m0, 0x400\n\ts_nop 0\n\tbuffer_load_dwordx4 %5, %0, %2 offen lds\n\t"
               "s_add_u32 m0, m0, 0x400\n\ts_nop 0\n\tbuffer_load_dwordx4 %6, %0, %2 offen lds"
               :: "s"(rs), "s"(dst), "s"(soff), "v"(v0), "v"(v1), "v"(v2), "v"(v3) : "memory", "scc");
}
DI v4u ld_hidden(v4u rs, int voff) { v4u r; asm volatile("s_nop 4\n\tbuffer_load_dwordx4 %0, %1, %2, 0 offen" : "=v"(r) : "v"(voff), "s"(rs) : "memory"); return r; }
DI v4u ld_hidden_sc1(v4u rs, int voff) { v4u r; asm volatile("s_nop 4\n\tbuffer_load_dwordx4 %0, %1, %2, 0 offen sc1" : "=v"(r) : "v"(voff), "s"(rs) : "memory"); return r; }

#define STAMP(id)

DI void wait_flags(rsrc_t rws, int off, int n, unsigned tag, int l) {
  const int vo = off + (l < n ? l : 0) * 4;
#pragma unroll 1
  for (int sp = 0; sp < (1 << 16); sp++) {
    asm volatile("" ::: "memory");
    const unsigned v = __builtin_amdgcn_raw_buffer_load_b32(rws, vo, 0, SC1);
    if (__all(v == tag)) break;
  }
  asm volatile("" ::: "memory");
}
DI void publish(rsrc_t rws, int off, unsigned tag) {
  WAITV(0);
  BAR();
  if (threadIdx.x == 0) __builtin_amdgcn_raw_buffer_store_b32(tag, rws, off, 0, SC1);
  asm volatile("" ::: "memory");
}

DI int krow_wo(int ks, int rho) { int sg = 4 * ks + (rho >> 3); int p = sg / 5, i = sg - 5 * p; return 40 * p + 4 * i + 20 * ((rho >> 2) & 1) + (rho & 3); }
DI int krow_w2(int ks, int rho) { int sg = 4 * ks + (rho >> 3); int W = sg / 6, i = sg - 6 * W;
  return 48 * W + (i < 4 ? 4 * i + 16 * ((rho >> 2) & 1) + (rho & 3) : 32 + 8 * (i - 4) + (rho & 7)); }
template <int MAT> DI int pack_addr(int u, int l, int (&v)[4]) {
  constexpr int NCB = MAT == 1 ? 24 : 10, ROWB = MAT == 1 ? F * 2 : D * 2, KS = MAT == 2 ? 48 : 20;
  const int ks = u / NCB, cb = u - ks * NCB;
#pragma unroll
  for (int i = 0; i < 4; i++) {
    const int rho = 8 * i + (l >> 3);
    const int kr = MAT == 0 ? krow_wo(ks, rho) : (MAT == 1 ? 32 * ks + rho : krow_w2(ks, rho));
    const int f = ((rho >> 1) & 1) | (((rho >> 3) & 1) << 1);
    v[i] = kr * ROWB + cb * 128 + (((l & 7) ^ (f << 1)) * 16);
  }
  return (MAT == 0 ? WOP_OFF : (MAT == 1 ? W1P_OFF : W2P_OFF)) + (cb * 4 * KS + ks) * 1024;
}
DI int pack_issue01(int u, v4u rwo4, v4u rw14, unsigned stg, int l) {
  int v[4], dst;
  if (u < 200) { dst = pack_addr<0>(u, l, v); glds4(rwo4, stg, 0, v[0], v[1], v[2], v[3]); }
  else { dst = pack_addr<1>(u - 200, l, v); glds4(rw14, stg, 0, v[0], v[1], v[2], v[3]); }
  return dst;
}
DI void pack_finish(int dst, int tstride, rsrc_t rws, LAS char* stg, int l) {
  const int g = l >> 4, q = (l >> 2) & 3, p = l & 3;
  const int f = ((q >> 1) & 1) | ((g & 1) << 1);
  v2u h[4][2];
#pragma unroll
  for (int t = 0; t < 4; t++)
#pragma unroll
    for (int hf = 0; hf < 2; hf++) h[t][hf] = trread(stg + (8 * g + 4 * hf + q) * 128 + ((t ^ f) * 32) + p * 8);
#pragma unroll
  for (int t = 0; t < 4; t++) sts1(v4u{h[t][0].x, h[t][0].y, h[t][1].x, h[t][1].y}, rws, l * 16, dst + t * tstride);
}

constexpr int P1_SLOT = 32768, P1_STG = 4 * P1_SLOT;
template <bool ISQ>
DI void p1_gemm(v4u rx4, v4u rw4, v4u rw24, v4u rws4, rsrc_t rws, LAS char* L, int rb, int ct, int punit, int& pdst, unsigned tag,
                const int (&va)[4], const int (&vb)[4], int l, int w) {
  const int wr = w >> 1, wc = w & 1, c = l & 15, g = l >> 4;
  const unsigned Lb = (unsigned)(size_t)L;
  f32x4 acc[4][4], cs[4];
#pragma unroll
  for (int n = 0; n < 4; n++) { cs[n] = f32x4{0, 0, 0, 0};
#pragma unroll
    for (int m = 0; m < 4; m++) acc[m][n] = f32x4{0, 0, 0, 0}; }
  v4u ones = {ONES2, ONES2, ONES2, ONES2};
  asm volatile("" : "+v"(ones));
  int aoff[2], boff[4];
#pragma unroll
  for (int kk = 0; kk < 2; kk++) aoff[kk] = (wr * 64 + c) * 128 + (((4 * kk + g) ^ ((c >> 1) & 7)) * 16);
#pragma unroll
  for (int n = 0; n < 4; n++) {
    const int tile = ISQ ? wc * 4 + n : (n >> 1) * 4 + wc * 2 + (n & 1);
    const int q = (l >> 2) & 3, p = l & 3, f = q | ((g & 1) << 2);
    boff[n] = 16384 + (8 * g + q) * 256 + ((tile ^ f) * 32) + p * 8;
  }
  v4u a0[4], b0[4], a1[4], b1[4];
  auto rd = [&](v4u (&a)[4], v4u (&b)[4], int s, int kk) {
    LAS char* slot = L + (s & 3) * P1_SLOT;
#pragma unroll
    for (int n = 0; n < 4; n++) {
      a[n] = *(LAS v4u*)(slot + aoff[kk] + n * 2048);
      v2u lo = trread(slot + boff[n] + kk * 8192), hi = trread(slot + boff[n] + kk * 8192 + 1024);
      b[n] = v4u{lo.x, lo.y, hi.x, hi.y};
    }
  };
  auto mm = [&](v4u (&a)[4], v4u (&b)[4]) {
    asm volatile("s_nop 1");
#pragma unroll
    for (int n = 0; n < 4; n++) {
#pragma unroll
      for (int m = 0; m < 4; m++) { if (ISQ) mfma_t(b[n], a[m], acc[m][n]); else mfma_t(a[m], b[n], acc[m][n]); }
      if (ISQ) mfma_t(b[n], ones, cs[n]); else mfma_t(ones, b[n], cs[n]);
    }
  };
  const int row0 = rb * 128 + wr * 64;
  int vp[4];
  pdst = pack_addr<2>(punit, l, vp);
  const int vst = ST_OFF + (row0 + l) * 16;
  const unsigned xdst = Lb + P1_STG + w * 5120;
  WAITV(24); BAR();
  rd(a0, b0, 0, 0);
#pragma unroll 1
  for (int s = 0; s < 9; s++) {
    rd(a1, b1, s, 1); SB();
    mm(a0, b0); SB();
#define GL(V, R, SO, X) "s_nop 0\n\tbuffer_load_dwordx4 " V ", " R ", " SO " offen " X "lds\n\ts_add_u32 m0, m0, 0x400\n\t"
    asm volatile(
        "s_cmp_gt_u32 %[s], 6\n\ts_cbranch_scc1 1f\n\ts_waitcnt vmcnt(16) lgkmcnt(0)\n\ts_branch 3f\n"
        "1:\n\ts_cmp_eq_u32 %[s], 7\n\ts_cbranch_scc0 2f\n\ts_waitcnt vmcnt(13) lgkmcnt(0)\n\ts_branch 3f\n"
        "2:\n\ts_waitcnt vmcnt(5) lgkmcnt(0)\n"
        "3:\n\ts_barrier\n\ts_cmp_gt_u32 %[s], 6\n\ts_cbranch_scc1 5f\n\ts_cmp_eq_u32 %[s], 6\n\ts_cbranch_scc1 4f\n\t"
        "s_mov_b32 m0, %[slot]\n\t"
        GL("%[a0]", "%[rx]", "%[sa]", "") GL("%[a1]", "%[rx]", "%[sa]", "") GL("%[a2]", "%[rx]", "%[sa]", "") GL("%[a3]", "%[rx]", "%[sa]", "")
        "s_add_u32 m0, m0, 0x3000\n\t"
        GL("%[b0]", "%[rw]", "%[sb]", "") GL("%[b1]", "%[rw]", "%[sb]", "") GL("%[b2]", "%[rw]", "%[sb]", "") GL("%[b3]", "%[rw]", "%[sb]", "")
        "s_branch 5f\n"
        "4:\n\ts_mov_b32 m0, %[xd]\n\t"
        GL("%[p0]", "%[rp]", "0", "") GL("%[p1]", "%[rp]", "0", "") GL("%[p2]", "%[rp]", "0", "") GL("%[p3]", "%[rp]", "0", "")
        GL("%[st]", "%[rs]", "0", "sc1 ")
        "\n5:"
        :: [s] "s"(s), [slot] "s"(Lb + (s & 3) * P1_SLOT + w * 4096), [xd] "s"(xdst), [sa] "s"((s + 4) * 128), [sb] "s"((s + 4) * 64 * 3840),
           [rx] "s"(rx4), [rw] "s"(rw4), [rp] "s"(rw24), [rs] "s"(rws4),
           [a0] "v"(va[0]), [a1] "v"(va[1]), [a2] "v"(va[2]), [a3] "v"(va[3]), [b0] "v"(vb[0]), [b1] "v"(vb[1]), [b2] "v"(vb[2]), [b3] "v"(vb[3]),
           [p0] "v"(vp[0]), [p1] "v"(vp[1]), [p2] "v"(vp[2]), [p3] "v"(vp[3]), [st] "v"(vst)
        : "memory", "scc");
    rd(a0, b0, s + 1, 0); SB();
    mm(a1, b1); SB();
  }
  rd(a1, b1, 9, 1); SB();
  mm(a0, b0); SB();
  WAITV(0);
  LAS char* tab = L + P1_STG + w * 5120 + 4096;
  {
    v4u stg = *(LAS v4u*)(tab + l * 16);
#pragma unroll 1
    for (int sp = 0; sp < (1 << 16) && !__all(stg.y == tag && stg.w == tag); sp++) { asm volatile("" ::: "memory"); stg = lds1(rws, vst, 0); }
    *(LAS v2u*)(tab + l * 8) = v2u{stg.x, stg.z};
  }
  SB();
  mm(a1, b1);
  asm volatile("s_nop 7");
  SB();
  if constexpr (ISQ) {
    f32x2 st[4];
#pragma unroll
    for (int m = 0; m < 4; m++) st[m] = *(LAS f32x2*)(tab + (16 * m + c) * 8);
#pragma unroll
    for (int n = 0; n < 4; n++) {
      const int q = (ct * 128 + (wc * 4 + n) * 16) / 4 + g;
#pragma unroll
      for (int u = 0; u < 2; u++) {
        v4u o;
#pragma unroll
        for (int hh = 0; hh < 2; hh++) {
          const int m = 2 * u + hh; float v[4];
#pragma unroll
          for (int j = 0; j < 4; j++) v[j] = phi(st[m].y * (acc[m][n][j] - st[m].x * cs[n][j]));
          o[2 * hh] = pk2(v[0], v[1]); o[2 * hh + 1] = pk2(v[2], v[3]);
        }
        sts1(o, rws, PQ_OFF + ((q * 64 + (row0 >> 5) + u) * 16 + c) * 16, 0);
      }
    }
  } else {
    f32x4 st[4][2];
#pragma unroll
    for (int m = 0; m < 4; m++) { LAS f32x4* p = (LAS f32x4*)(tab + (16 * m + 4 * g) * 8); st[m][0] = p[0]; st[m][1] = p[1]; }
#pragma unroll
    for (int n = 0; n < 4; n++) {
      const bool isk = n < 2;
      const int f = ct * 64 + (wc * 2 + (n & 1)) * 16 + c;
      const int h = f / 20, d = f - 20 * h;
#pragma unroll
      for (int u = 0; u < 2; u++) {
        v4u o;
#pragma unroll
        for (int hh = 0; hh < 2; hh++) {
          const int m = 2 * u + hh; float v[4];
#pragma unroll
          for (int j = 0; j < 4; j++) {
            const float mean = st[m][j >> 1][(j & 1) * 2], rs = st[m][j >> 1][(j & 1) * 2 + 1];
            const float t = rs * (acc[m][n][j] - mean * cs[n][0]);
            v[j] = isk ? phi(t) : t;
          }
          o[2 * hh] = pk2(v[0], v[1]); o[2 * hh + 1] = pk2(v[2], v[3]);
        }
        sts1(o, rws, (isk ? PK_OFF : PV_OFF) + ((((h * 64 + (row0 >> 5) + u) * 4 + g) * 20) + d) * 16, 0);
      }
    }
  }
}

constexpr int RED_ROW = 336, RED_W = 64 * RED_ROW;
constexpr int HK_OFF = 149 * 1024;
constexpr int P3_ST = 86016;
DI void red8(LAS char* L, int tok, int sl, float (&v)[8]) {
  f32x4 s0 = {0, 0, 0, 0}, s1 = {0, 0, 0, 0};
#pragma unroll
  for (int ww = 0; ww < 4; ww++) { LAS f32x4* p = (LAS f32x4*)(L + ww * RED_W + tok * RED_ROW + sl * 32); s0 += p[0]; s1 += p[1]; }
#pragma unroll
  for (int i = 0; i < 4; i++) { v[i] = s0[i]; v[4 + i] = s1[i]; }
}
template <int KSW>
DI void mm80(const v4u (&a)[4 * KSW], const v4u (&b)[5 * KSW], LAS char* dst) {
  f32x4 acc[2][5];
#pragma unroll
  for (int m = 0; m < 4; m++) {
#pragma unroll
    for (int k = 0; k < KSW; k++) {
#pragma unroll
      for (int n = 0; n < 5; n++) acc[m & 1][n] = mfma(b[k * 5 + n], a[m * KSW + k], k == 0 ? f32x4{0, 0, 0, 0} : acc[m & 1][n]);
      if (m > 0 && k < 5) *(LAS f32x4*)(dst + (m - 1) * 16 * RED_ROW + k * 64) = acc[(m - 1) & 1][k];
      SB();
    }
  }
#pragma unroll
  for (int n = 0; n < 5; n++) *(LAS f32x4*)(dst + 3 * 16 * RED_ROW + n * 64) = acc[1][n];
}

#define C1 10
#define C2 30
#define C3 45
#define E1 30

__global__ __launch_bounds__(256, 1) void k_fused(const void* x, const void* wqkv, const void* wo, const void* w1, const void* b1p, const void* w2,
                                                  const void* b2p, void* out, char* ws) {
  extern __shared__ __attribute__((aligned(16))) char smem[];
  LAS char* L = (LAS char*)smem;
  const unsigned Lb = (unsigned)(size_t)L;
  STAMP(0);
  const int tid = threadIdx.x, l = tid & 63, w = __builtin_amdgcn_readfirstlane(tid >> 6), c = l & 15, g = l >> 4;
  const int xg = blockIdx.x & 7, i = blockIdx.x >> 3;
  const rsrc_t rws = mkrs(ws, WS_END);
  const v4u rws4 = mkrs4(ws, WS_END), rx4 = mkrs4(x, S * D * 2);
  v4u ep = ld_hidden_sc1(rws4, EPOCH_OFF);
  const int srow = xg * 256 + i * 8 + 2 * w + (l >> 5);
  v4u xs0 = ld_hidden(rx4, srow * 1280 + (l & 31) * 16), xs1 = ld_hidden(rx4, srow * 1280 + 512 + (l & 31) * 16);
  v4u xs2 = ld_hidden(rx4, (l & 16) ? 0x7ffffff0 : srow * 1280 + 1024 + (l & 15) * 16);
  int va[4], vb[4];
  const bool isq = i >= 20;
  const int rb = xg * 2 + (i & 1), ct = isq ? (i - 20) >> 1 : i >> 1;
  const v4u rw4 = mkrs4(wqkv, D * 1920 * 2);
  const v4u rwo4 = mkrs4(wo, D * D * 2), rw14 = mkrs4(w1, D * F * 2);
  const int wi = (xg * 2 + (i - 30)) * 4 + w;
  if (i < 30) {
#pragma unroll
    for (int u = 0; u < 4; u++) {
      const int ra = (w * 4 + u) * 8 + (l >> 3);
      va[u] = (rb * 128 + ra) * 1280 + (((l & 7) ^ ((ra >> 1) & 7)) * 16);
      const int rk = (w * 4 + u) * 4 + (l >> 4);
      const int ch = (l & 15) ^ ((((rk & 3) | (((rk >> 3) & 1) << 2))) << 1);
      vb[u] = rk * 3840 + (isq ? ct * 256 + ch * 16 : 1280 + (ch >> 3) * 1280 + ct * 128 + (ch & 7) * 16);
    }
#pragma unroll
    for (int s = 0; s < 4; s++) {
      const unsigned slot = Lb + s * P1_SLOT + w * 4096;
      glds4(rx4, slot, s * 128, va[0], va[1], va[2], va[3]);
      glds4(rw4, slot + 16384, s * 64 * 3840, vb[0], vb[1], vb[2], vb[3]);
    }
  } else {
#pragma unroll 1
    for (int r = 0; r < 8; r++) (void)pack_issue01(r * 64 + wi, rwo4, rw14, Lb + (w * 8 + r) * 4096, l);
  }
  asm volatile("s_waitcnt vmcnt(32)" : "+v"(ep), "+v"(xs0), "+v"(xs1), "+v"(xs2) :: "memory");
  const unsigned tag = __builtin_amdgcn_readfirstlane(ep.x) + 1u;
  {
    float v[24], s = 0.f;
#pragma unroll
    for (int e = 0; e < 4; e++) { v[2 * e] = bflo(xs0[e]); v[2 * e + 1] = bfhi(xs0[e]); v[8 + 2 * e] = bflo(xs1[e]); v[9 + 2 * e] = bfhi(xs1[e]); v[16 + 2 * e] = bflo(xs2[e]); v[17 + 2 * e] = bfhi(xs2[e]); }
#pragma unroll
    for (int e = 0; e < 24; e++) s += v[e];
    const float mean = add_x16(sum16(s)) * (1.f / D);
    float q = 0.f, q2 = 0.f;
#pragma unroll
    for (int e = 0; e < 16; e++) { const float d = v[e] - mean; q += d * d; }
#pragma unroll
    for (int e = 16; e < 24; e++) { const float d = v[e] - mean; q2 += d * d; }
    q += (l & 16) ? 0.f : q2;
    const float rs = __builtin_amdgcn_rsqf(add_x16(sum16(q)) * (1.f / D) + 1e-5f);
    if ((l & 31) == 0) sts1(v4u{__float_as_uint(mean), tag, __float_as_uint(rs), tag}, rws, ST_OFF + srow * 16, 0);
  }
  STAMP(1);
  if (i < 30) {
    int pdst = 0;
    const int pw = (xg * 30 + i) * 4 + w;
    const int punit = pw >> 1;
    const v4u rw24 = mkrs4(w2, D * F * 2);
    if (isq) p1_gemm<true>(rx4, rw4, rw24, rws4, rws, L, rb, ct, punit, pdst, tag, va, vb, l, w);
    else p1_gemm<false>(rx4, rw4, rw24, rws4, rws, L, rb, ct, punit, pdst, tag, va, vb, l, w);
    STAMP(2);
    publish(rws, FA_OFF + xg * 128 + i * 4, tag);
    STAMP(3);
    if (!(w & 1)) pack_finish(pdst, 48 * 1024, rws, L + P1_STG + w * 5120, l);
  } else {
    auto dst_of = [&](int u) { return u < 200 ? WOP_OFF + ((u % 10) * 80 + u / 10) * 1024 : W1P_OFF + (((u - 200) % 24) * 80 + (u - 200) / 24) * 1024; };
#pragma unroll 1
    for (int r = 0; r < 3; r++) {
      WAITV(28);
      pack_finish(dst_of(r * 64 + wi), 20 * 1024, rws, L + (w * 8 + r) * 4096, l);
      asm volatile("s_waitcnt lgkmcnt(0)" ::: "memory");
      int u = (r + 8) * 64 + wi; u = u < 680 ? u : 679;
      (void)pack_issue01(u, rwo4, rw14, Lb + (w * 8 + r) * 4096, l);
    }
    WAITV(24);
#pragma unroll 1
    for (int r = 3; r < 8; r++) pack_finish(dst_of(r * 64 + wi), 20 * 1024, rws, L + (w * 8 + r) * 4096, l);
    WAITV(20);
#pragma unroll 1
    for (int r = 8; r < 11; r++) { int u = r * 64 + wi; u = u < 680 ? u : 679; pack_finish(dst_of(u), 20 * 1024, rws, L + (w * 8 + r - 8) * 4096, l); }
    publish(rws, FP_OFF + (xg * 2 + (i - 30)) * 4, tag);
    STAMP(3);
  }
  const int grp = xg * 4 + (i >> 3), j = i & 7;
  v4u bo[25], bw1[60], bw2[60];
  auto ld_w1 = [&](int lo, int hi) {
#pragma unroll
    for (int q = lo; q < hi; q++) bw1[q] = lds1(rws, l * 16, W1P_OFF + ((12 * j + 3 * w + q % 3) * 20 + q / 3) * 1024);
    SB();
  };
  auto ld_w2 = [&](int lo, int hi) {
#pragma unroll
    for (int q = lo; q < hi; q++) bw2[q] = lds1(rws, l * 16, W2P_OFF + ((5 * j + q % 5) * 48 + w * 12 + q / 5) * 1024);
    SB();
  };
  wait_flags(rws, FP_OFF, 16, tag, l);
  STAMP(4);
#pragma unroll
  for (int q = 0; q < 25; q++) bo[q] = lds1(rws, l * 16, WOP_OFF + ((5 * j + q % 5) * 20 + w * 5 + q / 5) * 1024);
  SB();
  wait_flags(rws, FA_OFF + xg * 128, 20, tag, l);
  STAMP(5);
  {
    const int h = i, dt = w >> 1, et = w & 1;
    const int dd = dt ? 16 + (c & 3) : c, ee = et ? 16 + (c & 3) : c;
    const int ak = PK_OFF + (((h * 64 + xg * 8) * 4 + g) * 20 + dd) * 16, av = PV_OFF + (((h * 64 + xg * 8) * 4 + g) * 20 + ee) * 16;
    v4u a[8], b[8];
#pragma unroll
    for (int sb = 0; sb < 8; sb++) { a[sb] = lds1(rws, ak, sb * 1280); b[sb] = lds1(rws, av, sb * 1280); }
    SB();
    ld_w1(0, C1);
    f32x4 acc = {0, 0, 0, 0};
    const bool one = et && c >= 4;
#pragma unroll
    for (int sb = 0; sb < 8; sb++) {
      v4u bb = b[sb];
      if (one) bb = v4u{ONES2, ONES2, ONES2, ONES2};
      acc = mfma(a[sb], bb, acc);
    }
    sts1(__builtin_bit_cast(v4u, acc), rws, KVP_OFF + ((h * 8 + xg) * 4 + w) * 1024 + l * 16, 0);
    STAMP(6);
    WAITV(0); BAR();
    if (tid == 0) { __builtin_amdgcn_raw_buffer_store_b32(tag, rws, FB_OFF + (h >> 1) * 128 + ((h & 1) * 8 + xg) * 4, 0, SC1);
                    __builtin_amdgcn_raw_buffer_store_b32(tag, rws, FQ_OFF + blockIdx.x * 4, 0, SC1); }
    asm volatile("" ::: "memory");
    STAMP(7);
  }
  {
    const int hp = i >> 1, sb = rb * 4 + w;
    wait_flags(rws, FA_OFF + xg * 128 + 80, 10, tag, l);
    v4u q1[2], q2[2];
#pragma unroll
    for (int hh = 0; hh < 2; hh++) {
      const int h = 2 * hp + hh;
      q1[hh] = lds1(rws, PQ_OFF + (((5 * h + g) * 64 + sb) * 16 + c) * 16, 0);
      q2[hh] = lds1(rws, PQ_OFF + (((5 * h + 4) * 64 + sb) * 16 + c) * 16, 0);
    }
    SB();
    ld_w1(C1, C2);
    wait_flags(rws, FB_OFF + hp * 128 + (w >> 1) * 32, 8, tag, l);
    STAMP(8);
    {
      const int h = 2 * hp + (w >> 1), et = w & 1;
      f32x4 p0[8], p1[8];
#pragma unroll
      for (int xx = 0; xx < 8; xx++) {
        p0[xx] = __builtin_bit_cast(f32x4, lds1(rws, KVP_OFF + (h * 8 * 4 + et) * 1024 + l * 16, xx * 4096));
        p1[xx] = __builtin_bit_cast(f32x4, lds1(rws, KVP_OFF + (h * 8 * 4 + 2 + et) * 1024 + c * 16, xx * 4096));
      }
      SB();
      f32x4 s0 = p0[0], s1 = p1[0];
#pragma unroll
      for (int xx = 1; xx < 8; xx++) { s0 += p0[xx]; s1 += p1[xx]; }
      v4u fr = {pk2(s0[0], s0[1]), pk2(s0[2], s0[3]), g == 0 ? pk2(s1[0], s1[1]) : 0u, g == 0 ? pk2(s1[2], s1[3]) : 0u};
      *(LAS v4u*)(L + w * 1024 + l * 16) = fr;
    }
    BAR();
    STAMP(9);
    v4u kf[4];
#pragma unroll
    for (int q = 0; q < 4; q++) kf[q] = *(LAS v4u*)(L + q * 1024 + l * 16);
#pragma unroll
    for (int t = 0; t < 2; t++) {
      unsigned o0[4], o1[4];
#pragma unroll
      for (int hh = 0; hh < 2; hh++) {
        const v4u bq = {q1[hh][2 * t], q1[hh][2 * t + 1], q2[hh][2 * t], q2[hh][2 * t + 1]};
        const f32x4 z = {0, 0, 0, 0};
        f32x4 n0 = mfma(kf[2 * hh], bq, z), n1 = mfma(kf[2 * hh + 1], bq, z);
        auto sw = __builtin_amdgcn_permlane16_swap(__float_as_uint(n1[0]), __float_as_uint(n1[0]), false, false);
        const float r = __builtin_amdgcn_rcpf(__uint_as_float(sw[1]) + 1e-6f);
        o0[2 * hh] = pk2(n0[0] * r, n0[1] * r); o0[2 * hh + 1] = pk2(n0[2] * r, n0[3] * r);
        o1[2 * hh] = pk2(n1[0] * r, n1[1] * r); o1[2 * hh + 1] = pk2(n1[2] * r, n1[3] * r);
      }
      const int base = OF_OFF + (sb * 2 + t) * 20480 + 5 * hp * 256;
      sts1(v4u{o0[0], o0[1], o0[2], o0[3]}, rws, base + l * 16, 0);
      if (g == 0) sts1(v4u{o1[0], o1[1], o1[2], o1[3]}, rws, base + 1024 + l * 16, 0);
    }
    STAMP(10);
    publish(rws, FC_OFF + rb * 128 + hp * 4, tag);
    STAMP(11);
  }
  LAS char* rdst = L + w * RED_W + c * RED_ROW + g * 16;
  {
    const rsrc_t rx = mkrs(x, S * D * 2);
    v4u xr[3];
#pragma unroll
    for (int r = 0; r < 3; r++) { const int it = tid + 256 * r; const int rt = it / 160, e = it - rt * 160, sl = e >> 4, tok = rt * 16 + (e & 15);
      xr[r] = ldp(rx, it < 640 ? (grp * 64 + tok) * 1280 + j * 160 + sl * 16 : 0, 0); }
    ld_w1(C2, C3);
    wait_flags(rws, FC_OFF + (grp >> 1) * 128, 16, tag, l);
    STAMP(12);
    v4u a[20];
#pragma unroll
    for (int q = 0; q < 20; q++) a[q] = lds1(rws, l * 16, OF_OFF + ((grp * 4 + q / 5) * 20 + w * 5 + q % 5) * 1024);
    SB();
    mm80<5>(a, bo, rdst);
    SB();
    STAMP(13);
    BAR();
#pragma unroll
    for (int r = 0; r < 3; r++) { const int it = tid + 256 * r; const int rt = it / 160, e = it - rt * 160, sl = e >> 4, tok = rt * 16 + (e & 15);
      if (it < 640) {
        float v[8]; red8(L, tok, sl, v);
        v4u o;
#pragma unroll
        for (int q = 0; q < 4; q++) o[q] = pk2(v[2 * q] + bflo(xr[r][q]), v[2 * q + 1] + bfhi(xr[r][q]));
        sts1(o, rws, HF_OFF + (grp * 4 + rt) * 20480 + (10 * j) * 256 + e * 16, 0);
        *(LAS v4u*)(L + HK_OFF + (tok * 10 + sl) * 16) = o;
      }
    }
    STAMP(14);
    publish(rws, FD_OFF + grp * 128 + j * 4, tag);
    STAMP(15);
  }
  {
    ld_w1(C3, 60);
    const rsrc_t rb1 = mkrs(b1p, F * 2);
    v2u bias[3];
#pragma unroll
    for (int n = 0; n < 3; n++) bias[n] = __builtin_amdgcn_raw_buffer_load_b64(rb1, ((12 * j + 3 * w + n) * 16 + 4 * g) * 2, 0, 0);
    wait_flags(rws, FQ_OFF + w * 256, 64, tag, l);
    const v4u ones = {ONES2, ONES2, ONES2, ONES2};
    f32x4 cs[3];
#pragma unroll
    for (int q = 0; q < 60; q++) cs[q % 3] = mfma(bw1[q], ones, q < 3 ? f32x4{0, 0, 0, 0} : cs[q % 3]);
    SB();
    wait_flags(rws, FD_OFF + grp * 128, 8, tag, l);
    STAMP(16);
    v4u a[20];
#pragma unroll
    for (int q = 0; q < 20; q++) a[q] = lds1(rws, l * 16, HF_OFF + ((grp * 4 + q / 5) * 20 + w * 5 + q % 5) * 1024);
    SB();
    BAR();
    ld_w2(0, E1);
#pragma unroll
    for (int m = 0; m < 4; m++) {
      float s = 0, q = 0;
#pragma unroll
      for (int k = 0; k < 5; k++) {
        *(LAS v4u*)(L + (m * 20 + w * 5 + k) * 1024 + l * 16) = a[m * 5 + k];
#pragma unroll
        for (int e = 0; e < 4; e++) { s = dot2(a[m * 5 + k][e], ONES2, s); q = dot2(a[m * 5 + k][e], a[m * 5 + k][e], q); }
      }
      s = add_x32(add_x16(s)); q = add_x32(add_x16(q));
      if (g == 0) *(LAS f32x2*)(L + P3_ST + ((m * 4 + w) * 16 + c) * 8) = f32x2{s, q};
    }
    BAR();
    STAMP(17);
    constexpr int RD = 4;
    v4u af[RD];
#pragma unroll
    for (int q = 0; q < RD; q++) af[q] = *(LAS v4u*)(L + q * 1024 + l * 16);
#pragma unroll
    for (int m = 0; m < 4; m++) {
      f32x4 acc[3];
#pragma unroll
      for (int k = 0; k < 20; k++) {
        const int st = m * 20 + k;
        const v4u cur = af[st % RD];
#pragma unroll
        for (int n = 0; n < 3; n++) acc[n] = mfma(bw1[k * 3 + n], cur, k == 0 ? f32x4{0, 0, 0, 0} : acc[n]);
        if (st + RD < 80) af[st % RD] = *(LAS v4u*)(L + (st + RD) * 1024 + l * 16);
        SB();
      }
      float s = 0, q = 0;
#pragma unroll
      for (int ww = 0; ww < 4; ww++) { const f32x2 p = *(LAS f32x2*)(L + P3_ST + ((m * 4 + ww) * 16 + c) * 8); s += p.x; q += p.y; }
      const float mean = s * (1.f / D);
      const float var = fmaxf(q * (1.f / D) - mean * mean, 0.f);
      const float r1 = __builtin_amdgcn_rsqf(var + 1e-5f);
      const float rs = r1 * __builtin_amdgcn_rsqf(var * r1 * r1 + 1e-5f);
      unsigned o[3][2];
#pragma unroll
      for (int n = 0; n < 3; n++) {
        float v[4];
#pragma unroll
        for (int e = 0; e < 4; e++) {
          const float bb = (e & 1) ? bfhi(bias[n][e >> 1]) : bflo(bias[n][e >> 1]);
          v[e] = gelu(__builtin_fmaf(rs, acc[n][e] - mean * cs[n][e], bb));
        }
        o[n][0] = pk2(v[0], v[1]); o[n][1] = pk2(v[2], v[3]);
      }
      const int base = GF_OFF + (grp * 4 + m) * 49152 + 6 * (4 * j + w) * 256;
      sts1(v4u{o[0][0], o[0][1], o[1][0], o[1][1]}, rws, base + l * 16, 0);
      auto s0 = __builtin_amdgcn_permlane16_swap(o[2][0], o[2][0], false, false);
      auto s1 = __builtin_amdgcn_permlane16_swap(o[2][1], o[2][1], false, false);
      if (!(g & 1)) sts1(v4u{s0[0], s1[0], s0[1], s1[1]}, rws, base + 1024 + (g >> 1) * 256 + c * 16, 0);
      SB();
    }
    STAMP(18);
    publish(rws, FE_OFF + grp * 128 + j * 4, tag);
    STAMP(19);
  }
  {
    ld_w2(E1, 60);
    const rsrc_t ro = mkrs(out, S * D * 2), rb2 = mkrs(b2p, D * 2);
    v4u br[3];
#pragma unroll
    for (int r = 0; r < 3; r++) { const int it = tid + 256 * r; const int tok = it / 10, sl = it - tok * 10; br[r] = ldp(rb2, j * 160 + sl * 16, 0); }
    wait_flags(rws, FE_OFF + grp * 128, 8, tag, l);
    STAMP(20);
    v4u a[48];
#pragma unroll
    for (int q = 0; q < 48; q++) a[q] = lds1(rws, l * 16, GF_OFF + ((grp * 4 + q / 12) * 48 + w * 12 + q % 12) * 1024);
    SB();
    mm80<12>(a, bw2, rdst);
    SB();
    STAMP(21);
    BAR();
#pragma unroll
    for (int r = 0; r < 3; r++) { const int it = tid + 256 * r; const int tok = it / 10, sl = it - tok * 10;
      if (it < 640) {
        float v[8]; red8(L, tok, sl, v);
        const v4u hr = *(LAS v4u*)(L + HK_OFF + it * 16);
        v4u o;
#pragma unroll
        for (int q = 0; q < 4; q++) o[q] = pk2(v[2 * q] + bflo(br[r][q]) + bflo(hr[q]), v[2 * q + 1] + bfhi(br[r][q]) + bfhi(hr[q]));
        __builtin_amdgcn_raw_buffer_store_b128(o, ro, (grp * 64 + tok) * 1280 + j * 160 + sl * 16, 0, 0);
      }
    }
  }
  if (blockIdx.x == 0 && tid == 0) __builtin_amdgcn_raw_buffer_store_b32(tag, rws, EPOCH_OFF, 0, SC1);
}

extern "C" void kernel_launch(void* const* d_in, const int* in_sizes, int n_in, void* d_out, int out_size, void* d_ws, size_t ws_size, hipStream_t stream) {
  (void)hipFuncSetAttribute((const void*)k_fused, hipFuncAttributeMaxDynamicSharedMemorySize, 160 * 1024);
  k_fused<<<256, 256, 160 * 1024, stream>>>(d_in[0], d_in[1], d_in[2], d_in[4], d_in[5], d_in[6], d_in[7], d_out, (char*)d_ws);
}
```

```cpp
#define PROBE 1
#include <hip/hip_runtime.h>
#include <hip/hip_bf16.h>
#include <stdint.h>

#define LAS __attribute__((address_space(3)))
#define DI __device__ __forceinline__
typedef __attribute__((ext_vector_type(8))) __bf16 bf16x8;
typedef __attribute__((ext_vector_type(2))) __bf16 bf16x2;
typedef __attribute__((ext_vector_type(4))) float f32x4;
typedef __attribute__((ext_vector_type(2))) float f32x2;
typedef __attribute__((ext_vector_type(4))) unsigned v4u;
typedef __attribute__((ext_vector_type(2))) unsigned v2u;
typedef __attribute__((ext_vector_type(4))) short s16x4;
typedef __amdgpu_buffer_rsrc_t rsrc_t;

constexpr int S = 2048, D = 640, F = 1536;
constexpr int ACT = S * D * 2;
constexpr int FL_OFF = 0;
constexpr int EPOCH_OFF = FL_OFF;
constexpr int FA_OFF = FL_OFF + 1024;
constexpr int FP_OFF = FA_OFF + 8 * 128;
constexpr int FQ_OFF = FP_OFF + 256;
constexpr int FB_OFF = FQ_OFF + 1024;
constexpr int FC_OFF = FB_OFF + 16 * 128;
constexpr int FD_OFF = FC_OFF + 16 * 128;
constexpr int FE_OFF = FD_OFF + 32 * 128;
constexpr int PK_OFF = 65536, PV_OFF = PK_OFF + ACT, PQ_OFF = PV_OFF + ACT, OF_OFF = PQ_OFF + ACT, HF_OFF = OF_OFF + ACT;
constexpr int GF_OFF = HF_OFF + ACT, WOP_OFF = GF_OFF + S * F * 2, W1P_OFF = WOP_OFF + D * D * 2, W2P_OFF = W1P_OFF + D * F * 2;
constexpr int KVP_OFF = W2P_OFF + D * F * 2, ST_OFF = KVP_OFF + 32 * 8 * 4096, WS_END = ST_OFF + S * 16;
static_assert(FE_OFF + 32 * 128 <= PK_OFF, "flags");
constexpr int SC1 = 16;
constexpr unsigned ONES2 = 0x3f803f80u;

DI rsrc_t mkrs(const void* p, int bytes) { return __builtin_amdgcn_make_buffer_rsrc((void*)p, 0, bytes, 0x00020000); }
DI v4u mkrs4(const void* p, int bytes) { const uint64_t a = (uint64_t)p; return v4u{(unsigned)a, (unsigned)(a >> 32) & 0xffffu, (unsigned)bytes, 0x00020000u}; }
DI f32x4 mfma(v4u a, v4u b, f32x4 c) {
  return __builtin_amdgcn_mfma_f32_16x16x32_bf16(__builtin_bit_cast(bf16x8, a), __builtin_bit_cast(bf16x8, b), c, 0, 0, 0);
}
DI void mfma_t(v4u a, v4u b, f32x4& c) { asm volatile("v_mfma_f32_16x16x32_bf16 %0, %1, %2, %0" : "+v"(c) : "v"(a), "v"(b)); }
DI unsigned pk2(float a, float b) { f32x2 f = {a, b}; return __builtin_bit_cast(unsigned, __builtin_convertvector(f, bf16x2)); }
DI float bflo(unsigned u) { return __uint_as_float(u << 16); }
DI float bfhi(unsigned u) { return __uint_as_float(u & 0xffff0000u); }
DI v4u ldp(rsrc_t r, int voff, int soff) { return __builtin_amdgcn_raw_buffer_load_b128(r, voff, soff, 0); }
DI v4u lds1(rsrc_t r, int voff, int soff) { return __builtin_amdgcn_raw_buffer_load_b128(r, voff, soff, SC1); }
DI void sts1(v4u v, rsrc_t r, int voff, int soff) { __builtin_amdgcn_raw_buffer_store_b128(v, r, voff, soff, SC1); }
DI v2u trread(LAS char* p) { return __builtin_bit_cast(v2u, __builtin_amdgcn_ds_read_tr16_b64_v4i16((LAS s16x4*)p)); }
#define WAITV(n) asm volatile("s_waitcnt vmcnt(" #n ")" ::: "memory")
#define BAR() do { asm volatile("s_waitcnt lgkmcnt(0)" ::: "memory"); __builtin_amdgcn_s_barrier(); asm volatile("" ::: "memory"); } while (0)
#define SB() __builtin_amdgcn_sched_barrier(0)
constexpr float LOG2E = 1.4426950408889634f;
DI float phi(float t) { return fmaxf(t, 0.f) + __builtin_amdgcn_fmed3f(__builtin_amdgcn_exp2f(t * LOG2E), 0.f, 1.f) + 1e-6f; }
DI float dot2(unsigned a, unsigned b, float c) { return __builtin_amdgcn_fdot2_f32_bf16(__builtin_bit_cast(bf16x2, a), __builtin_bit_cast(bf16x2, b), c, false); }
DI float gelu(float v) {
  const float u = v * (0.7978845608028654f * 2.f * LOG2E) * __builtin_fmaf(v * v, 0.044715f, 1.0f);
  return v * __builtin_amdgcn_rcpf(1.0f + __builtin_amdgcn_exp2f(-u));
}
template <int CTRL> DI float dppadd(float v) { return v + __uint_as_float(__builtin_amdgcn_update_dpp(0u, __float_as_uint(v), CTRL, 0xf, 0xf, true)); }
DI float sum16(float v) { v = dppadd<0xB1>(v); v = dppadd<0x4E>(v); v = dppadd<0x141>(v); return dppadd<0x140>(v); }
DI float add_x16(float v) { auto r = __builtin_amdgcn_permlane16_swap(__float_as_uint(v), __float_as_uint(v), false, false); return __uint_as_float(r[0]) + __uint_as_float(r[1]); }
DI float add_x32(float v) { auto r = __builtin_amdgcn_permlane32_swap(__float_as_uint(v), __float_as_uint(v), false, false); return __uint_as_float(r[0]) + __uint_as_float(r[1]); }

DI void glds4(v4u rs, unsigned dst, int soff, int v0, int v1, int v2, int v3) {
  asm volatile("s_nop 4\n\ts_mov_b32 m0, %1\n\ts_nop 0\n\tbuffer_load_dwordx4 %3, %0, %2 offen lds\n\t"
               "s_add_u32 m0, m0, 0x400\n\ts_nop 0\n\tbuffer_load_dwordx4 %4, %0, %2 offen lds\n\t"
               "s_add_u32 m0, m0, 0x400\n\ts_nop 0\n\tbuffer_load_dwordx4 %5, %0, %2 offen lds\n\t"
               "s_add_u32 m0, m0, 0x400\n\ts_nop 0\n\tbuffer_load_dwordx4 %6, %0, %2 offen lds"
               :: "s"(rs), "s"(dst), "s"(soff), "v"(v0), "v"(v1), "v"(v2), "v"(v3) : "memory", "scc");
}
DI v4u ld_hidden(v4u rs, int voff) { v4u r; asm volatile("s_nop 4\n\tbuffer_load_dwordx4 %0, %1, %2, 0 offen" : "=v"(r) : "v"(voff), "s"(rs) : "memory"); return r; }
DI v4u ld_hidden_sc1(v4u rs, int voff) { v4u r; asm volatile("s_nop 4\n\tbuffer_load_dwordx4 %0, %1, %2, 0 offen sc1" : "=v"(r) : "v"(voff), "s"(rs) : "memory"); return r; }

#define STAMP(id) do { if ((id) == 0) { pt0 = __builtin_amdgcn_s_memrealtime(); pc0 = __builtin_amdgcn_s_memtime(); asm volatile("s_waitcnt lgkmcnt(0)" ::: "memory"); } \
  if ((id) == PROBE) { SB(); pt1 = __builtin_amdgcn_s_memrealtime(); asm volatile("s_waitcnt lgkmcnt(0)" ::: "memory"); SB(); } } while (0)

DI void wait_flags(rsrc_t rws, int off, int n, unsigned tag, int l) {
  const int vo = off + (l < n ? l : 0) * 4;
#pragma unroll 1
  for (int sp = 0; sp < (1 << 16); sp++) {
    asm volatile("" ::: "memory");
    const unsigned v = __builtin_amdgcn_raw_buffer_load_b32(rws, vo, 0, SC1);
    if (__all(v == tag)) break;
  }
  asm volatile("" ::: "memory");
}
DI void publish(rsrc_t rws, int off, unsigned tag) {
  WAITV(0);
  BAR();
  if (threadIdx.x == 0) __builtin_amdgcn_raw_buffer_store_b32(tag, rws, off, 0, SC1);
  asm volatile("" ::: "memory");
}

DI int krow_wo(int ks, int rho) { int sg = 4 * ks + (rho >> 3); int p = sg / 5, i = sg - 5 * p; return 40 * p + 4 * i + 20 * ((rho >> 2) & 1) + (rho & 3); }
DI int krow_w2(int ks, int rho) { int sg = 4 * ks + (rho >> 3); int W = sg / 6, i = sg - 6 * W;
  return 48 * W + (i < 4 ? 4 * i + 16 * ((rho >> 2) & 1) + (rho & 3) : 32 + 8 * (i - 4) + (rho & 7)); }
template <int MAT> DI int pack_addr(int u, int l, int (&v)[4]) {
  constexpr int NCB = MAT == 1 ? 24 : 10, ROWB = MAT == 1 ? F * 2 : D * 2, KS = MAT == 2 ? 48 : 20;
  const int ks = u / NCB, cb = u - ks * NCB;
#pragma unroll
  for (int i = 0; i < 4; i++) {
    const int rho = 8 * i + (l >> 3);
    const int kr = MAT == 0 ? krow_wo(ks, rho) : (MAT == 1 ? 32 * ks + rho : krow_w2(ks, rho));
    const int f = ((rho >> 1) & 1) | (((rho >> 3) & 1) << 1);
    v[i] = kr * ROWB + cb * 128 + (((l & 7) ^ (f << 1)) * 16);
  }
  return (MAT == 0 ? WOP_OFF : (MAT == 1 ? W1P_OFF : W2P_OFF)) + (cb * 4 * KS + ks) * 1024;
}
DI int pack_issue01(int u, v4u rwo4, v4u rw14, unsigned stg, int l) {
  int v[4], dst;
  if (u < 200) { dst = pack_addr<0>(u, l, v); glds4(rwo4, stg, 0, v[0], v[1], v[2], v[3]); }
  else { dst = pack_addr<1>(u - 200, l, v); glds4(rw14, stg, 0, v[0], v[1], v[2], v[3]); }
  return dst;
}
DI void pack_finish(int dst, int tstride, rsrc_t rws, LAS char* stg, int l) {
  const int g = l >> 4, q = (l >> 2) & 3, p = l & 3;
  const int f = ((q >> 1) & 1) | ((g & 1) << 1);
  v2u h[4][2];
#pragma unroll
  for (int t = 0; t < 4; t++)
#pragma unroll
    for (int hf = 0; hf < 2; hf++) h[t][hf] = trread(stg + (8 * g + 4 * hf + q) * 128 + ((t ^ f) * 32) + p * 8);
#pragma unroll
  for (int t = 0; t < 4; t++) sts1(v4u{h[t][0].x, h[t][0].y, h[t][1].x, h[t][1].y}, rws, l * 16, dst + t * tstride);
}

constexpr int P1_SLOT = 32768, P1_STG = 4 * P1_SLOT;
template <bool ISQ>
DI void p1_gemm(v4u rx4, v4u rw4, v4u rw24, v4u rws4, rsrc_t rws, LAS char* L, int rb, int ct, int punit, int& pdst, unsigned tag,
                const int (&va)[4], const int (&vb)[4], int l, int w) {
  const int wr = w >> 1, wc = w & 1, c = l & 15, g = l >> 4;
  const unsigned Lb = (unsigned)(size_t)L;
  f32x4 acc[4][4], cs[4];
#pragma unroll
  for (int n = 0; n < 4; n++) { cs[n] = f32x4{0, 0, 0, 0};
#pragma unroll
    for (int m = 0; m < 4; m++) acc[m][n] = f32x4{0, 0, 0, 0}; }
  v4u ones = {ONES2, ONES2, ONES2, ONES2};
  asm volatile("" : "+v"(ones));
  int aoff[2], boff[4];
#pragma unroll
  for (int kk = 0; kk < 2; kk++) aoff[kk] = (wr * 64 + c) * 128 + (((4 * kk + g) ^ ((c >> 1) & 7)) * 16);
#pragma unroll
  for (int n = 0; n < 4; n++) {
    const int tile = ISQ ? wc * 4 + n : (n >> 1) * 4 + wc * 2 + (n & 1);
    const int q = (l >> 2) & 3, p = l & 3, f = q | ((g & 1) << 2);
    boff[n] = 16384 + (8 * g + q) * 256 + ((tile ^ f) * 32) + p * 8;
  }
  v4u a0[4], b0[4], a1[4], b1[4];
  auto rd = [&](v4u (&a)[4], v4u (&b)[4], int s, int kk) {
    LAS char* slot = L + (s & 3) * P1_SLOT;
#pragma unroll
    for (int n = 0; n < 4; n++) {
      a[n] = *(LAS v4u*)(slot + aoff[kk] + n * 2048);
      v2u lo = trread(slot + boff[n] + kk * 8192), hi = trread(slot + boff[n] + kk * 8192 + 1024);
      b[n] = v4u{lo.x, lo.y, hi.x, hi.y};
    }
  };
  auto mm = [&](v4u (&a)[4], v4u (&b)[4]) {
    asm volatile("s_nop 1");
#pragma unroll
    for (int n = 0; n < 4; n++) {
#pragma unroll
      for (int m = 0; m < 4; m++) { if (ISQ) mfma_t(b[n], a[m], acc[m][n]); else mfma_t(a[m], b[n], acc[m][n]); }
      if (ISQ) mfma_t(b[n], ones, cs[n]); else mfma_t(ones, b[n], cs[n]);
    }
  };
  const int row0 = rb * 128 + wr * 64;
  int vp[4];
  pdst = pack_addr<2>(punit, l, vp);
  const int vst = ST_OFF + (row0 + l) * 16;
  const unsigned xdst = Lb + P1_STG + w * 5120;
  WAITV(24); BAR();
  rd(a0, b0, 0, 0);
#pragma unroll 1
  for (int s = 0; s < 9; s++) {
    rd(a1, b1, s, 1); SB();
    mm(a0, b0); SB();
#define GL(V, R, SO, X) "s_nop 0\n\tbuffer_load_dwordx4 " V ", " R ", " SO " offen " X "lds\n\ts_add_u32 m0, m0, 0x400\n\t"
    asm volatile(
        "s_cmp_gt_u32 %[s], 6\n\ts_cbranch_scc1 1f\n\ts_waitcnt vmcnt(16) lgkmcnt(0)\n\ts_branch 3f\n"
        "1:\n\ts_cmp_eq_u32 %[s], 7\n\ts_cbranch_scc0 2f\n\ts_waitcnt vmcnt(13) lgkmcnt(0)\n\ts_branch 3f\n"
        "2:\n\ts_waitcnt vmcnt(5) lgkmcnt(0)\n"
        "3:\n\ts_barrier\n\ts_cmp_gt_u32 %[s], 6\n\ts_cbranch_scc1 5f\n\ts_cmp_eq_u32 %[s], 6\n\ts_cbranch_scc1 4f\n\t"
        "s_mov_b32 m0, %[slot]\n\t"
        GL("%[a0]", "%[rx]", "%[sa]", "") GL("%[a1]", "%[rx]", "%[sa]", "") GL("%[a2]", "%[rx]", "%[sa]", "") GL("%[a3]", "%[rx]", "%[sa]", "")
        "s_add_u32 m0, m0, 0x3000\n\t"
        GL("%[b0]", "%[rw]", "%[sb]", "") GL("%[b1]", "%[rw]", "%[sb]", "") GL("%[b2]", "%[rw]", "%[sb]", "") GL("%[b3]", "%[rw]", "%[sb]", "")
        "s_branch 5f\n"
        "4:\n\ts_mov_b32 m0, %[xd]\n\t"
        GL("%[p0]", "%[rp]", "0", "") GL("%[p1]", "%[rp]", "0", "") GL("%[p2]", "%[rp]", "0", "") GL("%[p3]", "%[rp]", "0", "")
        GL("%[st]", "%[rs]", "0", "sc1 ")
        "\n5:"
        :: [s] "s"(s), [slot] "s"(Lb + (s & 3) * P1_SLOT + w * 4096), [xd] "s"(xdst), [sa] "s"((s + 4) * 128), [sb] "s"((s + 4) * 64 * 3840),
           [rx] "s"(rx4), [rw] "s"(rw4), [rp] "s"(rw24), [rs] "s"(rws4),
           [a0] "v"(va[0]), [a1] "v"(va[1]), [a2] "v"(va[2]), [a3] "v"(va[3]), [b0] "v"(vb[0]), [b1] "v"(vb[1]), [b2] "v"(vb[2]), [b3] "v"(vb[3]),
           [p0] "v"(vp[0]), [p1] "v"(vp[1]), [p2] "v"(vp[2]), [p3] "v"(vp[3]), [st] "v"(vst)
        : "memory", "scc");
    rd(a0, b0, s + 1, 0); SB();
    mm(a1, b1); SB();
  }
  rd(a1, b1, 9, 1); SB();
  mm(a0, b0); SB();
  WAITV(0);
  LAS char* tab = L + P1_STG + w * 5120 + 4096;
  {
    v4u stg = *(LAS v4u*)(tab + l * 16);
#pragma unroll 1
    for (int sp = 0; sp < (1 << 16) && !__all(stg.y == tag && stg.w == tag); sp++) { asm volatile("" ::: "memory"); stg = lds1(rws, vst, 0); }
    *(LAS v2u*)(tab + l * 8) = v2u{stg.x, stg.z};
  }
  SB();
  mm(a1, b1);
  asm volatile("s_nop 7");
  SB();
  if constexpr (ISQ) {
    f32x2 st[4];
#pragma unroll
    for (int m = 0; m < 4; m++) st[m] = *(LAS f32x2*)(tab + (16 * m + c) * 8);
#pragma unroll
    for (int n = 0; n < 4; n++) {
      const int q = (ct * 128 + (wc * 4 + n) * 16) / 4 + g;
#pragma unroll
      for (int u = 0; u < 2; u++) {
        v4u o;
#pragma unroll
        for (int hh = 0; hh < 2; hh++) {
          const int m = 2 * u + hh; float v[4];
#pragma unroll
          for (int j = 0; j < 4; j++) v[j] = phi(st[m].y * (acc[m][n][j] - st[m].x * cs[n][j]));
          o[2 * hh] = pk2(v[0], v[1]); o[2 * hh + 1] = pk2(v[2], v[3]);
        }
        sts1(o, rws, PQ_OFF + ((q * 64 + (row0 >> 5) + u) * 16 + c) * 16, 0);
      }
    }
  } else {
    f32x4 st[4][2];
#pragma unroll
    for (int m = 0; m < 4; m++) { LAS f32x4* p = (LAS f32x4*)(tab + (16 * m + 4 * g) * 8); st[m][0] = p[0]; st[m][1] = p[1]; }
#pragma unroll
    for (int n = 0; n < 4; n++) {
      const bool isk = n < 2;
      const int f = ct * 64 + (wc * 2 + (n & 1)) * 16 + c;
      const int h = f / 20, d = f - 20 * h;
#pragma unroll
      for (int u = 0; u < 2; u++) {
        v4u o;
#pragma unroll
        for (int hh = 0; hh < 2; hh++) {
          const int m = 2 * u + hh; float v[4];
#pragma unroll
          for (int j = 0; j < 4; j++) {
            const float mean = st[m][j >> 1][(j & 1) * 2], rs = st[m][j >> 1][(j & 1) * 2 + 1];
            const float t = rs * (acc[m][n][j] - mean * cs[n][0]);
            v[j] = isk ? phi(t) : t;
          }
          o[2 * hh] = pk2(v[0], v[1]); o[2 * hh + 1] = pk2(v[2], v[3]);
        }
        sts1(o, rws, (isk ? PK_OFF : PV_OFF) + ((((h * 64 + (row0 >> 5) + u) * 4 + g) * 20) + d) * 16, 0);
      }
    }
  }
}

constexpr int RED_ROW = 336, RED_W = 64 * RED_ROW;
constexpr int HK_OFF = 149 * 1024;
constexpr int P3_ST = 86016;
DI void red8(LAS char* L, int tok, int sl, float (&v)[8]) {
  f32x4 s0 = {0, 0, 0, 0}, s1 = {0, 0, 0, 0};
#pragma unroll
  for (int ww = 0; ww < 4; ww++) { LAS f32x4* p = (LAS f32x4*)(L + ww * RED_W + tok * RED_ROW + sl * 32); s0 += p[0]; s1 += p[1]; }
#pragma unroll
  for (int i = 0; i < 4; i++) { v[i] = s0[i]; v[4 + i] = s1[i]; }
}
template <int KSW>
DI void mm80(const v4u (&a)[4 * KSW], const v4u (&b)[5 * KSW], LAS char* dst) {
  f32x4 acc[2][5];
#pragma unroll
  for (int m = 0; m < 4; m++) {
#pragma unroll
    for (int k = 0; k < KSW; k++) {
#pragma unroll
      for (int n = 0; n < 5; n++) acc[m & 1][n] = mfma(b[k * 5 + n], a[m * KSW + k], k == 0 ? f32x4{0, 0, 0, 0} : acc[m & 1][n]);
      if (m > 0 && k < 5) *(LAS f32x4*)(dst + (m - 1) * 16 * RED_ROW + k * 64) = acc[(m - 1) & 1][k];
      SB();
    }
  }
#pragma unroll
  for (int n = 0; n < 5; n++) *(LAS f32x4*)(dst + 3 * 16 * RED_ROW + n * 64) = acc[1][n];
}

#define C1 10
#define C2 30
#define C3 45
#define E1 30

__global__ __launch_bounds__(256, 1) void k_fused(const void* x, const void* wqkv, const void* wo, const void* w1, const void* b1p, const void* w2,
                                                  const void* b2p, void* out, char* ws) {
  extern __shared__ __attribute__((aligned(16))) char smem[];
  LAS char* L = (LAS char*)smem;
  const unsigned Lb = (unsigned)(size_t)L;
  unsigned long long pt0 = 0, pt1 = 0, pc0 = 0;
  STAMP(0);
  const int tid = threadIdx.x, l = tid & 63, w = __builtin_amdgcn_readfirstlane(tid >> 6), c = l & 15, g = l >> 4;
  const int xg = blockIdx.x & 7, i = blockIdx.x >> 3;
  const rsrc_t rws = mkrs(ws, WS_END);
  const v4u rws4 = mkrs4(ws, WS_END), rx4 = mkrs4(x, S * D * 2);
  v4u ep = ld_hidden_sc1(rws4, EPOCH_OFF);
  const int srow = xg * 256 + i * 8 + 2 * w + (l >> 5);
  v4u xs0 = ld_hidden(rx4, srow * 1280 + (l & 31) * 16), xs1 = ld_hidden(rx4, srow * 1280 + 512 + (l & 31) * 16);
  v4u xs2 = ld_hidden(rx4, (l & 16) ? 0x7ffffff0 : srow * 1280 + 1024 + (l & 15) * 16);
  int va[4], vb[4];
  const bool isq = i >= 20;
  const int rb = xg * 2 + (i & 1), ct = isq ? (i - 20) >> 1 : i >> 1;
  const v4u rw4 = mkrs4(wqkv, D * 1920 * 2);
  const v4u rwo4 = mkrs4(wo, D * D * 2), rw14 = mkrs4(w1, D * F * 2);
  const int wi = (xg * 2 + (i - 30)) * 4 + w;
  if (i < 30) {
#pragma unroll
    for (int u = 0; u < 4; u++) {
      const int ra = (w * 4 + u) * 8 + (l >> 3);
      va[u] = (rb * 128 + ra) * 1280 + (((l & 7) ^ ((ra >> 1) & 7)) * 16);
      const int rk = (w * 4 + u) * 4 + (l >> 4);
      const int ch = (l & 15) ^ ((((rk & 3) | (((rk >> 3) & 1) << 2))) << 1);
      vb[u] = rk * 3840 + (isq ? ct * 256 + ch * 16 : 1280 + (ch >> 3) * 1280 + ct * 128 + (ch & 7) * 16);
    }
#pragma unroll
    for (int s = 0; s < 4; s++) {
      const unsigned slot = Lb + s * P1_SLOT + w * 4096;
      glds4(rx4, slot, s * 128, va[0], va[1], va[2], va[3]);
      glds4(rw4, slot + 16384, s * 64 * 3840, vb[0], vb[1], vb[2], vb[3]);
    }
  } else {
#pragma unroll 1
    for (int r = 0; r < 8; r++) (void)pack_issue01(r * 64 + wi, rwo4, rw14, Lb + (w * 8 + r) * 4096, l);
  }
  asm volatile("s_waitcnt vmcnt(32)" : "+v"(ep), "+v"(xs0), "+v"(xs1), "+v"(xs2) :: "memory");
  const unsigned tag = __builtin_amdgcn_readfirstlane(ep.x) + 1u;
  {
    float v[24], s = 0.f;
#pragma unroll
    for (int e = 0; e < 4; e++) { v[2 * e] = bflo(xs0[e]); v[2 * e + 1] = bfhi(xs0[e]); v[8 + 2 * e] = bflo(xs1[e]); v[9 + 2 * e] = bfhi(xs1[e]); v[16 + 2 * e] = bflo(xs2[e]); v[17 + 2 * e] = bfhi(xs2[e]); }
#pragma unroll
    for (int e = 0; e < 24; e++) s += v[e];
    const float mean = add_x16(sum16(s)) * (1.f / D);
    float q = 0.f, q2 = 0.f;
#pragma unroll
    for (int e = 0; e < 16; e++) { const float d = v[e] - mean; q += d * d; }
#pragma unroll
    for (int e = 16; e < 24; e++) { const float d = v[e] - mean; q2 += d * d; }
    q += (l & 16) ? 0.f : q2;
    const float rs = __builtin_amdgcn_rsqf(add_x16(sum16(q)) * (1.f / D) + 1e-5f);
    if ((l & 31) == 0) sts1(v4u{__float_as_uint(mean), tag, __float_as_uint(rs), tag}, rws, ST_OFF + srow * 16, 0);
  }
  STAMP(1);
  if (i < 30) {
    int pdst = 0;
    const int pw = (xg * 30 + i) * 4 + w;
    const int punit = pw >> 1;
    const v4u rw24 = mkrs4(w2, D * F * 2);
    if (isq) p1_gemm<true>(rx4, rw4, rw24, rws4, rws, L, rb, ct, punit, pdst, tag, va, vb, l, w);
    else p1_gemm<false>(rx4, rw4, rw24, rws4, rws, L, rb, ct, punit, pdst, tag, va, vb, l, w);
    STAMP(2);
    publish(rws, FA_OFF + xg * 128 + i * 4, tag);
    STAMP(3);
    if (!(w & 1)) pack_finish(pdst, 48 * 1024, rws, L + P1_STG + w * 5120, l);
  } else {
    auto dst_of = [&](int u) { return u < 200 ? WOP_OFF + ((u % 10) * 80 + u / 10) * 1024 : W1P_OFF + (((u - 200) % 24) * 80 + (u - 200) / 24) * 1024; };
#pragma unroll 1
    for (int r = 0; r < 3; r++) {
      WAITV(28);
      pack_finish(dst_of(r * 64 + wi), 20 * 1024, rws, L + (w * 8 + r) * 4096, l);
      asm volatile("s_waitcnt lgkmcnt(0)" ::: "memory");
      int u = (r + 8) * 64 + wi; u = u < 680 ? u : 679;
      (void)pack_issue01(u, rwo4, rw14, Lb + (w * 8 + r) * 4096, l);
    }
    WAITV(24);
#pragma unroll 1
    for (int r = 3; r < 8; r++) pack_finish(dst_of(r * 64 + wi), 20 * 1024, rws, L + (w * 8 + r) * 4096, l);
    WAITV(20);
#pragma unroll 1
    for (int r = 8; r < 11; r++) { int u = r * 64 + wi; u = u < 680 ? u : 679; pack_finish(dst_of(u), 20 * 1024, rws, L + (w * 8 + r - 8) * 4096, l); }
    publish(rws, FP_OFF + (xg * 2 + (i - 30)) * 4, tag);
    STAMP(3);
  }
  const int grp = xg * 4 + (i >> 3), j = i & 7;
  v4u bo[25], bw1[60], bw2[60];
  auto ld_w1 = [&](int lo, int hi) {
#pragma unroll
    for (int q = lo; q < hi; q++) bw1[q] = lds1(rws, l * 16, W1P_OFF + ((12 * j + 3 * w + q % 3) * 20 + q / 3) * 1024);
    SB();
  };
  auto ld_w2 = [&](int lo, int hi) {
#pragma unroll
    for (int q = lo; q < hi; q++) bw2[q] = lds1(rws, l * 16, W2P_OFF + ((5 * j + q % 5) * 48 + w * 12 + q / 5) * 1024);
    SB();
  };
  wait_flags(rws, FP_OFF, 16, tag, l);
  STAMP(4);
#pragma unroll
  for (int q = 0; q < 25; q++) bo[q] = lds1(rws, l * 16, WOP_OFF + ((5 * j + q % 5) * 20 + w * 5 + q / 5) * 1024);
  SB();
  wait_flags(rws, FA_OFF + xg * 128, 20, tag, l);
  STAMP(5);
  {
    const int h = i, dt = w >> 1, et = w & 1;
    const int dd = dt ? 16 + (c & 3) : c, ee = et ? 16 + (c & 3) : c;
    const int ak = PK_OFF + (((h * 64 + xg * 8) * 4 + g) * 20 + dd) * 16, av = PV_OFF + (((h * 64 + xg * 8) * 4 + g) * 20 + ee) * 16;
    v4u a[8], b[8];
#pragma unroll
    for (int sb = 0; sb < 8; sb++) { a[sb] = lds1(rws, ak, sb * 1280); b[sb] = lds1(rws, av, sb * 1280); }
    SB();
    ld_w1(0, C1);
    f32x4 acc = {0, 0, 0, 0};
    const bool one = et && c >= 4;
#pragma unroll
    for (int sb = 0; sb < 8; sb++) {
      v4u bb = b[sb];
      if (one) bb = v4u{ONES2, ONES2, ONES2, ONES2};
      acc = mfma(a[sb], bb, acc);
    }
    sts1(__builtin_bit_cast(v4u, acc), rws, KVP_OFF + ((h * 8 + xg) * 4 + w) * 1024 + l * 16, 0);
    STAMP(6);
    WAITV(0); BAR();
    if (tid == 0) { __builtin_amdgcn_raw_buffer_store_b32(tag, rws, FB_OFF + (h >> 1) * 128 + ((h & 1) * 8 + xg) * 4, 0, SC1);
                    __builtin_amdgcn_raw_buffer_store_b32(tag, rws, FQ_OFF + blockIdx.x * 4, 0, SC1); }
    asm volatile("" ::: "memory");
    STAMP(7);
  }
  {
    const int hp = i >> 1, sb = rb * 4 + w;
    wait_flags(rws, FA_OFF + xg * 128 + 80, 10, tag, l);
    v4u q1[2], q2[2];
#pragma unroll
    for (int hh = 0; hh < 2; hh++) {
      const int h = 2 * hp + hh;
      q1[hh] = lds1(rws, PQ_OFF + (((5 * h + g) * 64 + sb) * 16 + c) * 16, 0);
      q2[hh] = lds1(rws, PQ_OFF + (((5 * h + 4) * 64 + sb) * 16 + c) * 16, 0);
    }
    SB();
    ld_w1(C1, C2);
    wait_flags(rws, FB_OFF + hp * 128 + (w >> 1) * 32, 8, tag, l);
    STAMP(8);
    {
      const int h = 2 * hp + (w >> 1), et = w & 1;
      f32x4 p0[8], p1[8];
#pragma unroll
      for (int xx = 0; xx < 8; xx++) {
        p0[xx] = __builtin_bit_cast(f32x4, lds1(rws, KVP_OFF + (h * 8 * 4 + et) * 1024 + l * 16, xx * 4096));
        p1[xx] = __builtin_bit_cast(f32x4, lds1(rws, KVP_OFF + (h * 8 * 4 + 2 + et) * 1024 + c * 16, xx * 4096));
      }
      SB();
      f32x4 s0 = p0[0], s1 = p1[0];
#pragma unroll
      for (int xx = 1; xx < 8; xx++) { s0 += p0[xx]; s1 += p1[xx]; }
      v4u fr = {pk2(s0[0], s0[1]), pk2(s0[2], s0[3]), g == 0 ? pk2(s1[0], s1[1]) : 0u, g == 0 ? pk2(s1[2], s1[3]) : 0u};
      *(LAS v4u*)(L + w * 1024 + l * 16) = fr;
    }
    BAR();
    STAMP(9);
    v4u kf[4];
#pragma unroll
    for (int q = 0; q < 4; q++) kf[q] = *(LAS v4u*)(L + q * 1024 + l * 16);
#pragma unroll
    for (int t = 0; t < 2; t++) {
      unsigned o0[4], o1[4];
#pragma unroll
      for (int hh = 0; hh < 2; hh++) {
        const v4u bq = {q1[hh][2 * t], q1[hh][2 * t + 1], q2[hh][2 * t], q2[hh][2 * t + 1]};
        const f32x4 z = {0, 0, 0, 0};
        f32x4 n0 = mfma(kf[2 * hh], bq, z), n1 = mfma(kf[2 * hh + 1], bq, z);
        auto sw = __builtin_amdgcn_permlane16_swap(__float_as_uint(n1[0]), __float_as_uint(n1[0]), false, false);
        const float r = __builtin_amdgcn_rcpf(__uint_as_float(sw[1]) + 1e-6f);
        o0[2 * hh] = pk2(n0[0] * r, n0[1] * r); o0[2 * hh + 1] = pk2(n0[2] * r, n0[3] * r);
        o1[2 * hh] = pk2(n1[0] * r, n1[1] * r); o1[2 * hh + 1] = pk2(n1[2] * r, n1[3] * r);
      }
      const int base = OF_OFF + (sb * 2 + t) * 20480 + 5 * hp * 256;
      sts1(v4u{o0[0], o0[1], o0[2], o0[3]}, rws, base + l * 16, 0);
      if (g == 0) sts1(v4u{o1[0], o1[1], o1[2], o1[3]}, rws, base + 1024 + l * 16, 0);
    }
    STAMP(10);
    publish(rws, FC_OFF + rb * 128 + hp * 4, tag);
    STAMP(11);
  }
  LAS char* rdst = L + w * RED_W + c * RED_ROW + g * 16;
  {
    const rsrc_t rx = mkrs(x, S * D * 2);
    v4u xr[3];
#pragma unroll
    for (int r = 0; r < 3; r++) { const int it = tid + 256 * r; const int rt = it / 160, e = it - rt * 160, sl = e >> 4, tok = rt * 16 + (e & 15);
      xr[r] = ldp(rx, it < 640 ? (grp * 64 + tok) * 1280 + j * 160 + sl * 16 : 0, 0); }
    ld_w1(C2, C3);
    wait_flags(rws, FC_OFF + (grp >> 1) * 128, 16, tag, l);
    STAMP(12);
    v4u a[20];
#pragma unroll
    for (int q = 0; q < 20; q++) a[q] = lds1(rws, l * 16, OF_OFF + ((grp * 4 + q / 5) * 20 + w * 5 + q % 5) * 1024);
    SB();
    mm80<5>(a, bo, rdst);
    SB();
    STAMP(13);
    BAR();
#pragma unroll
    for (int r = 0; r < 3; r++) { const int it = tid + 256 * r; const int rt = it / 160, e = it - rt * 160, sl = e >> 4, tok = rt * 16 + (e & 15);
      if (it < 640) {
        float v[8]; red8(L, tok, sl, v);
        v4u o;
#pragma unroll
        for (int q = 0; q < 4; q++) o[q] = pk2(v[2 * q] + bflo(xr[r][q]), v[2 * q + 1] + bfhi(xr[r][q]));
        sts1(o, rws, HF_OFF + (grp * 4 + rt) * 20480 + (10 * j) * 256 + e * 16, 0);
        *(LAS v4u*)(L + HK_OFF + (tok * 10 + sl) * 16) = o;
      }
    }
    STAMP(14);
    publish(rws, FD_OFF + grp * 128 + j * 4, tag);
    STAMP(15);
  }
  {
    ld_w1(C3, 60);
    const rsrc_t rb1 = mkrs(b1p, F * 2);
    v2u bias[3];
#pragma unroll
    for (int n = 0; n < 3; n++) bias[n] = __builtin_amdgcn_raw_buffer_load_b64(rb1, ((12 * j + 3 * w + n) * 16 + 4 * g) * 2, 0, 0);
    wait_flags(rws, FQ_OFF + w * 256, 64, tag, l);
    const v4u ones = {ONES2, ONES2, ONES2, ONES2};
    f32x4 cs[3];
#pragma unroll
    for (int q = 0; q < 60; q++) cs[q % 3] = mfma(bw1[q], ones, q < 3 ? f32x4{0, 0, 0, 0} : cs[q % 3]);
    SB();
    wait_flags(rws, FD_OFF + grp * 128, 8, tag, l);
    STAMP(16);
    v4u a[20];
#pragma unroll
    for (int q = 0; q < 20; q++) a[q] = lds1(rws, l * 16, HF_OFF + ((grp * 4 + q / 5) * 20 + w * 5 + q % 5) * 1024);
    SB();
    BAR();
    ld_w2(0, E1);
#pragma unroll
    for (int m = 0; m < 4; m++) {
      float s = 0, q = 0;
#pragma unroll
      for (int k = 0; k < 5; k++) {
        *(LAS v4u*)(L + (m * 20 + w * 5 + k) * 1024 + l * 16) = a[m * 5 + k];
#pragma unroll
        for (int e = 0; e < 4; e++) { s = dot2(a[m * 5 + k][e], ONES2, s); q = dot2(a[m * 5 + k][e], a[m * 5 + k][e], q); }
      }
      s = add_x32(add_x16(s)); q = add_x32(add_x16(q));
      if (g == 0) *(LAS f32x2*)(L + P3_ST + ((m * 4 + w) * 16 + c) * 8) = f32x2{s, q};
    }
    BAR();
    STAMP(17);
    constexpr int RD = 4;
    v4u af[RD];
#pragma unroll
    for (int q = 0; q < RD; q++) af[q] = *(LAS v4u*)(L + q * 1024 + l * 16);
#pragma unroll
    for (int m = 0; m < 4; m++) {
      f32x4 acc[3];
#pragma unroll
      for (int k = 0; k < 20; k++) {
        const int st = m * 20 + k;
        const v4u cur = af[st % RD];
#pragma unroll
        for (int n = 0; n < 3; n++) acc[n] = mfma(bw1[k * 3 + n], cur, k == 0 ? f32x4{0, 0, 0, 0} : acc[n]);
        if (st + RD < 80) af[st % RD] = *(LAS v4u*)(L + (st + RD) * 1024 + l * 16);
        SB();
      }
      float s = 0, q = 0;
#pragma unroll
      for (int ww = 0; ww < 4; ww++) { const f32x2 p = *(LAS f32x2*)(L + P3_ST + ((m * 4 + ww) * 16 + c) * 8); s += p.x; q += p.y; }
      const float mean = s * (1.f / D);
      const float var = fmaxf(q * (1.f / D) - mean * mean, 0.f);
      const float r1 = __builtin_amdgcn_rsqf(var + 1e-5f);
      const float rs = r1 * __builtin_amdgcn_rsqf(var * r1 * r1 + 1e-5f);
      unsigned o[3][2];
#pragma unroll
      for (int n = 0; n < 3; n++) {
        float v[4];
#pragma unroll
        for (int e = 0; e < 4; e++) {
          const float bb = (e & 1) ? bfhi(bias[n][e >> 1]) : bflo(bias[n][e >> 1]);
          v[e] = gelu(__builtin_fmaf(rs, acc[n][e] - mean * cs[n][e], bb));
        }
        o[n][0] = pk2(v[0], v[1]); o[n][1] = pk2(v[2], v[3]);
      }
      const int base = GF_OFF + (grp * 4 + m) * 49152 + 6 * (4 * j + w) * 256;
      sts1(v4u{o[0][0], o[0][1], o[1][0], o[1][1]}, rws, base + l * 16, 0);
      auto s0 = __builtin_amdgcn_permlane16_swap(o[2][0], o[2][0], false, false);
      auto s1 = __builtin_amdgcn_permlane16_swap(o[2][1], o[2][1], false, false);
      if (!(g & 1)) sts1(v4u{s0[0], s1[0], s0[1], s1[1]}, rws, base + 1024 + (g >> 1) * 256 + c * 16, 0);
      SB();
    }
    STAMP(18);
    publish(rws, FE_OFF + grp * 128 + j * 4, tag);
    STAMP(19);
  }
  {
    ld_w2(E1, 60);
    const rsrc_t ro = mkrs(out, S * D * 2), rb2 = mkrs(b2p, D * 2);
    v4u br[3];
#pragma unroll
    for (int r = 0; r < 3; r++) { const int it = tid + 256 * r; const int tok = it / 10, sl = it - tok * 10; br[r] = ldp(rb2, j * 160 + sl * 16, 0); }
    wait_flags(rws, FE_OFF + grp * 128, 8, tag, l);
    STAMP(20);
    v4u a[48];
#pragma unroll
    for (int q = 0; q < 48; q++) a[q] = lds1(rws, l * 16, GF_OFF + ((grp * 4 + q / 12) * 48 + w * 12 + q % 12) * 1024);
    SB();
    mm80<12>(a, bw2, rdst);
    SB();
    STAMP(21);
    BAR();
#pragma unroll
    for (int r = 0; r < 3; r++) { const int it = tid + 256 * r; const int tok = it / 10, sl = it - tok * 10;
      if (it < 640) {
        float v[8]; red8(L, tok, sl, v);
        const v4u hr = *(LAS v4u*)(L + HK_OFF + it * 16);
        v4u o;
#pragma unroll
        for (int q = 0; q < 4; q++) o[q] = pk2(v[2 * q] + bflo(br[r][q]) + bflo(hr[q]), v[2 * q + 1] + bfhi(br[r][q]) + bfhi(hr[q]));
        __builtin_amdgcn_raw_buffer_store_b128(o, ro, (grp * 64 + tok) * 1280 + j * 160 + sl * 16, 0, 0);
      }
    }
  }
  if (blockIdx.x == 0 && tid == 0) __builtin_amdgcn_raw_buffer_store_b32(tag, rws, EPOCH_OFF, 0, SC1);
  WAITV(0);
  STAMP(22);
  { const unsigned long long te = __builtin_amdgcn_s_memrealtime();
    const unsigned long long d = PROBE == 99 ? (__builtin_amdgcn_s_memtime() - pc0) / 4 : (pt1 > pt0 ? 10 * (pt1 - pt0) : 0);
    while (__builtin_amdgcn_s_memrealtime() - te < d) __builtin_amdgcn_s_sleep(2); }
}

extern "C" void kernel_launch(void* const* d_in, const int* in_sizes, int n_in, void* d_out, int out_size, void* d_ws, size_t ws_size, hipStream_t stream) {
  (void)hipFuncSetAttribute((const void*)k_fused, hipFuncAttributeMaxDynamicSharedMemorySize, 160 * 1024);
  k_fused<<<256, 256, 160 * 1024, stream>>>(d_in[0], d_in[1], d_in[2], d_in[4], d_in[5], d_in[6], d_in[7], d_out, (char*)d_ws);
}
```

```cpp
#include <hip/hip_runtime.h>
#include <hip/hip_bf16.h>
#include <stdint.h>

#define LAS __attribute__((address_space(3)))
#define DI __device__ __forceinline__
typedef __attribute__((ext_vector_type(8))) __bf16 bf16x8;
typedef __attribute__((ext_vector_type(2))) __bf16 bf16x2;
typedef __attribute__((ext_vector_type(4))) float f32x4;
typedef __attribute__((ext_vector_type(2))) float f32x2;
typedef __attribute__((ext_vector_type(4))) unsigned v4u;
typedef __attribute__((ext_vector_type(2))) unsigned v2u;
typedef __attribute__((ext_vector_type(4))) short s16x4;
typedef __amdgpu_buffer_rsrc_t rsrc_t;

constexpr int S = 2048, D = 640, F = 1536;
constexpr int ACT = S * D * 2;
constexpr int FL_OFF = 0;
constexpr int EPOCH_OFF = FL_OFF;
constexpr int FA_OFF = FL_OFF + 1024;
constexpr int FP_OFF = FA_OFF + 8 * 128;
constexpr int FQ_OFF = FP_OFF + 256;
constexpr int FB_OFF = FQ_OFF + 1024;
constexpr int FC_OFF = FB_OFF + 16 * 128;
constexpr int FD_OFF = FC_OFF + 16 * 128;
constexpr int FE_OFF = FD_OFF + 32 * 128;
constexpr int PK_OFF = 65536, PV_OFF = PK_OFF + ACT, PQ_OFF = PV_OFF + ACT, OF_OFF = PQ_OFF + ACT, HF_OFF = OF_OFF + ACT;
constexpr int GF_OFF = HF_OFF + ACT, WOP_OFF = GF_OFF + S * F * 2, W1P_OFF = WOP_OFF + D * D * 2, W2P_OFF = W1P_OFF + D * F * 2;
constexpr int KVP_OFF = W2P_OFF + D * F * 2, ST_OFF = KVP_OFF + 32 * 8 * 4096, WS_END = ST_OFF + S * 16;
static_assert(FE_OFF + 32 * 128 <= PK_OFF, "flags");
constexpr int SC1 = 16;
constexpr unsigned ONES2 = 0x3f803f80u;

DI rsrc_t mkrs(const void* p, int bytes) { return __builtin_amdgcn_make_buffer_rsrc((void*)p, 0, bytes, 0x00020000); }
DI v4u mkrs4(const void* p, int bytes) { const uint64_t a = (uint64_t)p; return v4u{(unsigned)a, (unsigned)(a >> 32) & 0xffffu, (unsigned)bytes, 0x00020000u}; }
DI f32x4 mfma(v4u a, v4u b, f32x4 c) {
  return __builtin_amdgcn_mfma_f32_16x16x32_bf16(__builtin_bit_cast(bf16x8, a), __builtin_bit_cast(bf16x8, b), c, 0, 0, 0);
}
DI void mfma_t(v4u a, v4u b, f32x4& c) { asm volatile("v_mfma_f32_16x16x32_bf16 %0, %1, %2, %0" : "+v"(c) : "v"(a), "v"(b)); }
DI unsigned pk2(float a, float b) { f32x2 f = {a, b}; return __builtin_bit_cast(unsigned, __builtin_convertvector(f, bf16x2)); }
DI float bflo(unsigned u) { return __uint_as_float(u << 16); }
DI float bfhi(unsigned u) { return __uint_as_float(u & 0xffff0000u); }
DI v4u ldp(rsrc_t r, int voff, int soff) { return __builtin_amdgcn_raw_buffer_load_b128(r, voff, soff, 0); }
DI v4u lds1(rsrc_t r, int voff, int soff) { return __builtin_amdgcn_raw_buffer_load_b128(r, voff, soff, SC1); }
DI void sts1(v4u v, rsrc_t r, int voff, int soff) { __builtin_amdgcn_raw_buffer_store_b128(v, r, voff, soff, SC1); }
DI v2u trread(LAS char* p) { return __builtin_bit_cast(v2u, __builtin_amdgcn_ds_read_tr16_b64_v4i16((LAS s16x4*)p)); }
#define WAITV(n) asm volatile("s_waitcnt vmcnt(" #n ")" ::: "memory")
#define BAR() do { asm volatile("s_waitcnt lgkmcnt(0)" ::: "memory"); __builtin_amdgcn_s_barrier(); asm volatile("" ::: "memory"); } while (0)
#define SB() __builtin_amdgcn_sched_barrier(0)
constexpr float LOG2E = 1.4426950408889634f;
DI float phi(float t) { return fmaxf(t, 0.f) + __builtin_amdgcn_fmed3f(__builtin_amdgcn_exp2f(t * LOG2E), 0.f, 1.f) + 1e-6f; }
DI float dot2(unsigned a, unsigned b, float c) { return __builtin_amdgcn_fdot2_f32_bf16(__builtin_bit_cast(bf16x2, a), __builtin_bit_cast(bf16x2, b), c, false); }
DI float gelu(float v) {
  const float u = v * (0.7978845608028654f * 2.f * LOG2E) * __builtin_fmaf(v * v, 0.044715f, 1.0f);
  return v * __builtin_amdgcn_rcpf(1.0f + __builtin_amdgcn_exp2f(-u));
}
template <int CTRL> DI float dppadd(float v) { return v + __uint_as_float(__builtin_amdgcn_update_dpp(0u, __float_as_uint(v), CTRL, 0xf, 0xf, true)); }
DI float sum16(float v) { v = dppadd<0xB1>(v); v = dppadd<0x4E>(v); v = dppadd<0x141>(v); return dppadd<0x140>(v); }
DI float add_x16(float v) { auto r = __builtin_amdgcn_permlane16_swap(__float_as_uint(v), __float_as_uint(v), false, false); return __uint_as_float(r[0]) + __uint_as_float(r[1]); }
DI float add_x32(float v) { auto r = __builtin_amdgcn_permlane32_swap(__float_as_uint(v), __float_as_uint(v), false, false); return __uint_as_float(r[0]) + __uint_as_float(r[1]); }

DI void glds4(v4u rs, unsigned dst, int soff, int v0, int v1, int v2, int v3) {
  asm volatile("s_nop 4\n\ts_mov_b32 m0, %1\n\ts_nop 0\n\tbuffer_load_dwordx4 %3, %0, %2 offen lds\n\t"
               "s_add_u32 m0, m0, 0x400\n\ts_nop 0\n\tbuffer_load_dwordx4 %4, %0, %2 offen lds\n\t"
               "s_add_u32 m0, m0, 0x400\n\ts_nop 0\n\tbuffer_load_dwordx4 %5, %0, %2 offen lds\n\t"
               "s_add_u32 m0, m0, 0x400\n\ts_nop 0\n\tbuffer_load_dwordx4 %6, %0, %2 offen lds"
               :: "s"(rs), "s"(dst), "s"(soff), "v"(v0), "v"(v1), "v"(v2), "v"(v3) : "memory", "scc");
}
DI v4u ld_hidden(v4u rs, int voff) { v4u r; asm volatile("s_nop 4\n\tbuffer_load_dwordx4 %0, %1, %2, 0 offen" : "=v"(r) : "v"(voff), "s"(rs) : "memory"); return r; }
DI v4u ld_hidden_sc1(v4u rs, int voff) { v4u r; asm volatile("s_nop 4\n\tbuffer_load_dwordx4 %0, %1, %2, 0 offen sc1" : "=v"(r) : "v"(voff), "s"(rs) : "memory"); return r; }

#define STAMP(id)

DI void wait_flags(rsrc_t rws, int off, int n, unsigned tag, int l) {
  const int vo = off + (l < n ? l : 0) * 4;
#pragma unroll 1
  for (int sp = 0; sp < (1 << 16); sp++) {
    asm volatile("" ::: "memory");
    const unsigned v = __builtin_amdgcn_raw_buffer_load_b32(rws, vo, 0, SC1);
    if (__all(v == tag)) break;
  }
  asm volatile("" ::: "memory");
}
DI void publish(rsrc_t rws, int off, unsigned tag) {
  WAITV(0);
  BAR();
  if (threadIdx.x == 0) __builtin_amdgcn_raw_buffer_store_b32(tag, rws, off, 0, SC1);
  asm volatile("" ::: "memory");
}

DI int krow_wo(int ks, int rho) { int sg = 4 * ks + (rho >> 3); int p = sg / 5, i = sg - 5 * p; return 40 * p + 4 * i + 20 * ((rho >> 2) & 1) + (rho & 3); }
DI int krow_w2(int ks, int rho) { int sg = 4 * ks + (rho >> 3); int W = sg / 6, i = sg - 6 * W;
  return 48 * W + (i < 4 ? 4 * i + 16 * ((rho >> 2) & 1) + (rho & 3) : 32 + 8 * (i - 4) + (rho & 7)); }
template <int MAT> DI int pack_addr(int u, int l, int (&v)[4]) {
  constexpr int NCB = MAT == 1 ? 24 : 10, ROWB = MAT == 1 ? F * 2 : D * 2, KS = MAT == 2 ? 48 : 20;
  const int ks = u / NCB, cb = u - ks * NCB;
#pragma unroll
  for (int i = 0; i < 4; i++) {
    const int rho = 8 * i + (l >> 3);
    const int kr = MAT == 0 ? krow_wo(ks, rho) : (MAT == 1 ? 32 * ks + rho : krow_w2(ks, rho));
    const int f = ((rho >> 1) & 1) | (((rho >> 3) & 1) << 1);
    v[i] = kr * ROWB + cb * 128 + (((l & 7) ^ (f << 1)) * 16);
  }
  return (MAT == 0 ? WOP_OFF : (MAT == 1 ? W1P_OFF : W2P_OFF)) + (cb * 4 * KS + ks) * 1024;
}
DI int pack_issue01(int u, v4u rwo4, v4u rw14, unsigned stg, int l) {
  int v[4], dst;
  if (u < 200) { dst = pack_addr<0>(u, l, v); glds4(rwo4, stg, 0, v[0], v[1], v[2], v[3]); }
  else { dst = pack_addr<1>(u - 200, l, v); glds4(rw14, stg, 0, v[0], v[1], v[2], v[3]); }
  return dst;
}
DI void pack_finish(int dst, int tstride, rsrc_t rws, LAS char* stg, int l) {
  const int g = l >> 4, q = (l >> 2) & 3, p = l & 3;
  const int f = ((q >> 1) & 1) | ((g & 1) << 1);
  v2u h[4][2];
#pragma unroll
  for (int t = 0; t < 4; t++)
#pragma unroll
    for (int hf = 0; hf < 2; hf++) h[t][hf] = trread(stg + (8 * g + 4 * hf + q) * 128 + ((t ^ f) * 32) + p * 8);
#pragma unroll
  for (int t = 0; t < 4; t++) sts1(v4u{h[t][0].x, h[t][0].y, h[t][1].x, h[t][1].y}, rws, l * 16, dst + t * tstride);
}

constexpr int P1_SLOT = 32768, P1_STG = 4 * P1_SLOT;
template <bool ISQ>
DI void p1_gemm(v4u rx4, v4u rw4, v4u rw24, v4u rws4, rsrc_t rws, LAS char* L, int rb, int ct, int punit, int& pdst, unsigned tag,
                const int (&va)[4], const int (&vb)[4], int l, int w) {
  const int wr = w >> 1, wc = w & 1, c = l & 15, g = l >> 4;
  const unsigned Lb = (unsigned)(size_t)L;
  f32x4 acc[4][4], cs[4];
#pragma unroll
  for (int n = 0; n < 4; n++) { cs[n] = f32x4{0, 0, 0, 0};
#pragma unroll
    for (int m = 0; m < 4; m++) acc[m][n] = f32x4{0, 0, 0, 0}; }
  v4u ones = {ONES2, ONES2, ONES2, ONES2};
  asm volatile("" : "+v"(ones));
  int aoff[2], boff[4];
#pragma unroll
  for (int kk = 0; kk < 2; kk++) aoff[kk] = (wr * 64 + c) * 128 + (((4 * kk + g) ^ ((c >> 1) & 7)) * 16);
#pragma unroll
  for (int n = 0; n < 4; n++) {
    const int tile = ISQ ? wc * 4 + n : (n >> 1) * 4 + wc * 2 + (n & 1);
    const int q = (l >> 2) & 3, p = l & 3, f = q | ((g & 1) << 2);
    boff[n] = 16384 + (8 * g + q) * 256 + ((tile ^ f) * 32) + p * 8;
  }
  v4u a0[4], b0[4], a1[4], b1[4];
  auto rd = [&](v4u (&a)[4], v4u (&b)[4], int s, int kk) {
    LAS char* slot = L + (s & 3) * P1_SLOT;
#pragma unroll
    for (int n = 0; n < 4; n++) {
      a[n] = *(LAS v4u*)(slot + aoff[kk] + n * 2048);
      v2u lo = trread(slot + boff[n] + kk * 8192), hi = trread(slot + boff[n] + kk * 8192 + 1024);
      b[n] = v4u{lo.x, lo.y, hi.x, hi.y};
    }
  };
  auto mm = [&](v4u (&a)[4], v4u (&b)[4]) {
    asm volatile("s_nop 1");
#pragma unroll
    for (int n = 0; n < 4; n++) {
#pragma unroll
      for (int m = 0; m < 4; m++) { if (ISQ) mfma_t(b[n], a[m], acc[m][n]); else mfma_t(a[m], b[n], acc[m][n]); }
      if (ISQ) mfma_t(b[n], ones, cs[n]); else mfma_t(ones, b[n], cs[n]);
    }
  };
  const int row0 = rb * 128 + wr * 64;
  int vp[4];
  pdst = pack_addr<2>(punit, l, vp);
  const int vst = ST_OFF + (row0 + l) * 16;
  const unsigned xdst = Lb + P1_STG + w * 5120;
  WAITV(24); BAR();
  rd(a0, b0, 0, 0);
#pragma unroll 1
  for (int s = 0; s < 9; s++) {
    rd(a1, b1, s, 1); SB();
    mm(a0, b0); SB();
#define GL(V, R, SO, X) "s_nop 0\n\tbuffer_load_dwordx4 " V ", " R ", " SO " offen " X "lds\n\ts_add_u32 m0, m0, 0x400\n\t"
    asm volatile(
        "s_cmp_gt_u32 %[s], 6\n\ts_cbranch_scc1 1f\n\ts_waitcnt vmcnt(16) lgkmcnt(0)\n\ts_branch 3f\n"
        "1:\n\ts_cmp_eq_u32 %[s], 7\n\ts_cbranch_scc0 2f\n\ts_waitcnt vmcnt(13) lgkmcnt(0)\n\ts_branch 3f\n"
        "2:\n\ts_waitcnt vmcnt(5) lgkmcnt(0)\n"
        "3:\n\ts_barrier\n\ts_cmp_gt_u32 %[s], 6\n\ts_cbranch_scc1 5f\n\ts_cmp_eq_u32 %[s], 6\n\ts_cbranch_scc1 4f\n\t"
        "s_mov_b32 m0, %[slot]\n\t"
        GL("%[a0]", "%[rx]", "%[sa]", "") GL("%[a1]", "%[rx]", "%[sa]", "") GL("%[a2]", "%[rx]", "%[sa]", "") GL("%[a3]", "%[rx]", "%[sa]", "")
        "s_add_u32 m0, m0, 0x3000\n\t"
        GL("%[b0]", "%[rw]", "%[sb]", "") GL("%[b1]", "%[rw]", "%[sb]", "") GL("%[b2]", "%[rw]", "%[sb]", "") GL("%[b3]", "%[rw]", "%[sb]", "")
        "s_branch 5f\n"
        "4:\n\ts_mov_b32 m0, %[xd]\n\t"
        GL("%[p0]", "%[rp]", "0", "") GL("%[p1]", "%[rp]", "0", "") GL("%[p2]", "%[rp]", "0", "") GL("%[p3]", "%[rp]", "0", "")
        GL("%[st]", "%[rs]", "0", "sc1 ")
        "\n5:"
        :: [s] "s"(s), [slot] "s"(Lb + (s & 3) * P1_SLOT + w * 4096), [xd] "s"(xdst), [sa] "s"((s + 4) * 128), [sb] "s"((s + 4) * 64 * 3840),
           [rx] "s"(rx4), [rw] "s"(rw4), [rp] "s"(rw24), [rs] "s"(rws4),
           [a0] "v"(va[0]), [a1] "v"(va[1]), [a2] "v"(va[2]), [a3] "v"(va[3]), [b0] "v"(vb[0]), [b1] "v"(vb[1]), [b2] "v"(vb[2]), [b3] "v"(vb[3]),
           [p0] "v"(vp[0]), [p1] "v"(vp[1]), [p2] "v"(vp[2]), [p3] "v"(vp[3]), [st] "v"(vst)
        : "memory", "scc");
    rd(a0, b0, s + 1, 0); SB();
    mm(a1, b1); SB();
  }
  rd(a1, b1, 9, 1); SB();
  mm(a0, b0); SB();
  WAITV(0);
  LAS char* tab = L + P1_STG + w * 5120 + 4096;
  {
    v4u stg = *(LAS v4u*)(tab + l * 16);
#pragma unroll 1
    for (int sp = 0; sp < (1 << 16) && !__all(stg.y == tag && stg.w == tag); sp++) { asm volatile("" ::: "memory"); stg = lds1(rws, vst, 0); }
    *(LAS v2u*)(tab + l * 8) = v2u{stg.x, stg.z};
  }
  SB();
  mm(a1, b1);
  asm volatile("s_nop 7");
  SB();
  if constexpr (ISQ) {
    f32x2 st[4];
#pragma unroll
    for (int m = 0; m < 4; m++) st[m] = *(LAS f32x2*)(tab + (16 * m + c) * 8);
#pragma unroll
    for (int n = 0; n < 4; n++) {
      const int q = (ct * 128 + (wc * 4 + n) * 16) / 4 + g;
#pragma unroll
      for (int u = 0; u < 2; u++) {
        v4u o;
#pragma unroll
        for (int hh = 0; hh < 2; hh++) {
          const int m = 2 * u + hh; float v[4];
#pragma unroll
          for (int j = 0; j < 4; j++) v[j] = phi(st[m].y * (acc[m][n][j] - st[m].x * cs[n][j]));
          o[2 * hh] = pk2(v[0], v[1]); o[2 * hh + 1] = pk2(v[2], v[3]);
        }
        sts1(o, rws, PQ_OFF + ((q * 64 + (row0 >> 5) + u) * 16 + c) * 16, 0);
      }
    }
  } else {
    f32x4 st[4][2];
#pragma unroll
    for (int m = 0; m < 4; m++) { LAS f32x4* p = (LAS f32x4*)(tab + (16 * m + 4 * g) * 8); st[m][0] = p[0]; st[m][1] = p[1]; }
#pragma unroll
    for (int n = 0; n < 4; n++) {
      const bool isk = n < 2;
      const int f = ct * 64 + (wc * 2 + (n & 1)) * 16 + c;
      const int h = f / 20, d = f - 20 * h;
#pragma unroll
      for (int u = 0; u < 2; u++) {
        v4u o;
#pragma unroll
        for (int hh = 0; hh < 2; hh++) {
          const int m = 2 * u + hh; float v[4];
#pragma unroll
          for (int j = 0; j < 4; j++) {
            const float mean = st[m][j >> 1][(j & 1) * 2], rs = st[m][j >> 1][(j & 1) * 2 + 1];
            const float t = rs * (acc[m][n][j] - mean * cs[n][0]);
            v[j] = isk ? phi(t) : t;
          }
          o[2 * hh] = pk2(v[0], v[1]); o[2 * hh + 1] = pk2(v[2], v[3]);
        }
        sts1(o, rws, (isk ? PK_OFF : PV_OFF) + ((((h * 64 + (row0 >> 5) + u) * 4 + g) * 20) + d) * 16, 0);
      }
    }
  }
}

constexpr int RED_ROW = 336, RED_W = 64 * RED_ROW;
constexpr int HK_OFF = 149 * 1024;
constexpr int P3_ST = 86016;
DI void red8(LAS char* L, int tok, int sl, float (&v)[8]) {
  f32x4 s0 = {0, 0, 0, 0}, s1 = {0, 0, 0, 0};
#pragma unroll
  for (int ww = 0; ww < 4; ww++) { LAS f32x4* p = (LAS f32x4*)(L + ww * RED_W + tok * RED_ROW + sl * 32); s0 += p[0]; s1 += p[1]; }
#pragma unroll
  for (int i = 0; i < 4; i++) { v[i] = s0[i]; v[4 + i] = s1[i]; }
}
template <int KSW>
DI void mm80(const v4u (&a)[4 * KSW], const v4u (&b)[5 * KSW], LAS char* dst) {
  f32x4 acc[2][5];
#pragma unroll
  for (int m = 0; m < 4; m++) {
#pragma unroll
    for (int k = 0; k < KSW; k++) {
#pragma unroll
      for (int n = 0; n < 5; n++) acc[m & 1][n] = mfma(b[k * 5 + n], a[m * KSW + k], k == 0 ? f32x4{0, 0, 0, 0} : acc[m & 1][n]);
      if (m > 0 && k < 5) *(LAS f32x4*)(dst + (m - 1) * 16 * RED_ROW + k * 64) = acc[(m - 1) & 1][k];
      SB();
    }
  }
#pragma unroll
  for (int n = 0; n < 5; n++) *(LAS f32x4*)(dst + 3 * 16 * RED_ROW + n * 64) = acc[1][n];
}

#define C1 10
#define C2 30
#define C3 45
#define E1 30

__global__ __launch_bounds__(256, 1) void k_fused(const void* x, const void* wqkv, const void* wo, const void* w1, const void* b1p, const void* w2,
                                                  const void* b2p, void* out, char* ws) {
  extern __shared__ __attribute__((aligned(16))) char smem[];
  LAS char* L = (LAS char*)smem;
  const unsigned Lb = (unsigned)(size_t)L;
  STAMP(0);
  const int tid = threadIdx.x, l = tid & 63, w = __builtin_amdgcn_readfirstlane(tid >> 6), c = l & 15, g = l >> 4;
  const int xg = blockIdx.x & 7, i = blockIdx.x >> 3;
  const rsrc_t rws = mkrs(ws, WS_END);
  const v4u rws4 = mkrs4(ws, WS_END), rx4 = mkrs4(x, S * D * 2);
  v4u ep = ld_hidden_sc1(rws4, EPOCH_OFF);
  const int srow = xg * 256 + i * 8 + 2 * w + (l >> 5);
  v4u xs0 = ld_hidden(rx4, srow * 1280 + (l & 31) * 16), xs1 = ld_hidden(rx4, srow * 1280 + 512 + (l & 31) * 16);
  v4u xs2 = ld_hidden(rx4, (l & 16) ? 0x7ffffff0 : srow * 1280 + 1024 + (l & 15) * 16);
  int va[4], vb[4];
  const bool isq = i >= 20;
  const int rb = xg * 2 + (i & 1), ct = isq ? (i - 20) >> 1 : i >> 1;
  const v4u rw4 = mkrs4(wqkv, D * 1920 * 2);
  const v4u rwo4 = mkrs4(wo, D * D * 2), rw14 = mkrs4(w1, D * F * 2);
  const int wi = (xg * 2 + (i - 30)) * 4 + w;
  if (i < 30) {
#pragma unroll
    for (int u = 0; u < 4; u++) {
      const int ra = (w * 4 + u) * 8 + (l >> 3);
      va[u] = (rb * 128 + ra) * 1280 + (((l & 7) ^ ((ra >> 1) & 7)) * 16);
      const int rk = (w * 4 + u) * 4 + (l >> 4);
      const int ch = (l & 15) ^ ((((rk & 3) | (((rk >> 3) & 1) << 2))) << 1);
      vb[u] = rk * 3840 + (isq ? ct * 256 + ch * 16 : 1280 + (ch >> 3) * 1280 + ct * 128 + (ch & 7) * 16);
    }
#pragma unroll
    for (int s = 0; s < 4; s++) {
      const unsigned slot = Lb + s * P1_SLOT + w * 4096;
      glds4(rx4, slot, s * 128, va[0], va[1], va[2], va[3]);
      glds4(rw4, slot + 16384, s * 64 * 3840, vb[0], vb[1], vb[2], vb[3]);
    }
  } else {
#pragma unroll 1
    for (int r = 0; r < 8; r++) (void)pack_issue01(r * 64 + wi, rwo4, rw14, Lb + (w * 8 + r) * 4096, l);
  }
  asm volatile("s_waitcnt vmcnt(32)" : "+v"(ep), "+v"(xs0), "+v"(xs1), "+v"(xs2) :: "memory");
  const unsigned tag = __builtin_amdgcn_readfirstlane(ep.x) + 1u;
  {
    float v[24], s = 0.f;
#pragma unroll
    for (int e = 0; e < 4; e++) { v[2 * e] = bflo(xs0[e]); v[2 * e + 1] = bfhi(xs0[e]); v[8 + 2 * e] = bflo(xs1[e]); v[9 + 2 * e] = bfhi(xs1[e]); v[16 + 2 * e] = bflo(xs2[e]); v[17 + 2 * e] = bfhi(xs2[e]); }
#pragma unroll
    for (int e = 0; e < 24; e++) s += v[e];
    const float mean = add_x16(sum16(s)) * (1.f / D);
    float q = 0.f, q2 = 0.f;
#pragma unroll
    for (int e = 0; e < 16; e++) { const float d = v[e] - mean; q += d * d; }
#pragma unroll
    for (int e = 16; e < 24; e++) { const float d = v[e] - mean; q2 += d * d; }
    q += (l & 16) ? 0.f : q2;
    const float rs = __builtin_amdgcn_rsqf(add_x16(sum16(q)) * (1.f / D) + 1e-5f);
    if ((l & 31) == 0) sts1(v4u{__float_as_uint(mean), tag, __float_as_uint(rs), tag}, rws, ST_OFF + srow * 16, 0);
  }
  STAMP(1);
  if (i < 30) {
    int pdst = 0;
    const int pw = (xg * 30 + i) * 4 + w;
    const int punit = pw >> 1;
    const v4u rw24 = mkrs4(w2, D * F * 2);
    if (isq) p1_gemm<true>(rx4, rw4, rw24, rws4, rws, L, rb, ct, punit, pdst, tag, va, vb, l, w);
    else p1_gemm<false>(rx4, rw4, rw24, rws4, rws, L, rb, ct, punit, pdst, tag, va, vb, l, w);
    STAMP(2);
    publish(rws, FA_OFF + xg * 128 + i * 4, tag);
    STAMP(3);
    if (!(w & 1)) pack_finish(pdst, 48 * 1024, rws, L + P1_STG + w * 5120, l);
  } else {
    auto dst_of = [&](int u) { return u < 200 ? WOP_OFF + ((u % 10) * 80 + u / 10) * 1024 : W1P_OFF + (((u - 200) % 24) * 80 + (u - 200) / 24) * 1024; };
#pragma unroll 1
    for (int r = 0; r < 3; r++) {
      WAITV(28);
      pack_finish(dst_of(r * 64 + wi), 20 * 1024, rws, L + (w * 8 + r) * 4096, l);
      asm volatile("s_waitcnt lgkmcnt(0)" ::: "memory");
      int u = (r + 8) * 64 + wi; u = u < 680 ? u : 679;
      (void)pack_issue01(u, rwo4, rw14, Lb + (w * 8 + r) * 4096, l);
    }
    WAITV(24);
#pragma unroll 1
    for (int r = 3; r < 8; r++) pack_finish(dst_of(r * 64 + wi), 20 * 1024, rws, L + (w * 8 + r) * 4096, l);
    WAITV(20);
#pragma unroll 1
    for (int r = 8; r < 11; r++) { int u = r * 64 + wi; u = u < 680 ? u : 679; pack_finish(dst_of(u), 20 * 1024, rws, L + (w * 8 + r - 8) * 4096, l); }
    publish(rws, FP_OFF + (xg * 2 + (i - 30)) * 4, tag);
    STAMP(3);
  }
  const int grp = xg * 4 + (i >> 3), j = i & 7;
  v4u bo[25], bw1[60], bw2[60];
  auto ld_w1 = [&](int lo, int hi) {
#pragma unroll
    for (int q = lo; q < hi; q++) bw1[q] = lds1(rws, l * 16, W1P_OFF + ((12 * j + 3 * w + q % 3) * 20 + q / 3) * 1024);
    SB();
  };
  auto ld_w2 = [&](int lo, int hi) {
#pragma unroll
    for (int q = lo; q < hi; q++) bw2[q] = lds1(rws, l * 16, W2P_OFF + ((5 * j + q % 5) * 48 + w * 12 + q / 5) * 1024);
    SB();
  };
  wait_flags(rws, FP_OFF, 16, tag, l);
  STAMP(4);
#pragma unroll
  for (int q = 0; q < 25; q++) bo[q] = lds1(rws, l * 16, WOP_OFF + ((5 * j + q % 5) * 20 + w * 5 + q / 5) * 1024);
  SB();
  wait_flags(rws, FA_OFF + xg * 128, 20, tag, l);
  STAMP(5);
  {
    const int h = i, dt = w >> 1, et = w & 1;
    const int dd = dt ? 16 + (c & 3) : c, ee = et ? 16 + (c & 3) : c;
    const int ak = PK_OFF + (((h * 64 + xg * 8) * 4 + g) * 20 + dd) * 16, av = PV_OFF + (((h * 64 + xg * 8) * 4 + g) * 20 + ee) * 16;
    v4u a[8], b[8];
#pragma unroll
    for (int sb = 0; sb < 8; sb++) { a[sb] = lds1(rws, ak, sb * 1280); b[sb] = lds1(rws, av, sb * 1280); }
    SB();
    ld_w1(0, C1);
    f32x4 acc = {0, 0, 0, 0};
    const bool one = et && c >= 4;
#pragma unroll
    for (int sb = 0; sb < 8; sb++) {
      v4u bb = b[sb];
      if (one) bb = v4u{ONES2, ONES2, ONES2, ONES2};
      acc = mfma(a[sb], bb, acc);
    }
    sts1(__builtin_bit_cast(v4u, acc), rws, KVP_OFF + ((h * 8 + xg) * 4 + w) * 1024 + l * 16, 0);
    STAMP(6);
    WAITV(0); BAR();
    if (tid == 0) { __builtin_amdgcn_raw_buffer_store_b32(tag, rws, FB_OFF + (h >> 1) * 128 + ((h & 1) * 8 + xg) * 4, 0, SC1);
                    __builtin_amdgcn_raw_buffer_store_b32(tag, rws, FQ_OFF + blockIdx.x * 4, 0, SC1); }
    asm volatile("" ::: "memory");
    STAMP(7);
  }
  {
    const int hp = i >> 1, sb = rb * 4 + w;
    wait_flags(rws, FA_OFF + xg * 128 + 80, 10, tag, l);
    v4u q1[2], q2[2];
#pragma unroll
    for (int hh = 0; hh < 2; hh++) {
      const int h = 2 * hp + hh;
      q1[hh] = lds1(rws, PQ_OFF + (((5 * h + g) * 64 + sb) * 16 + c) * 16, 0);
      q2[hh] = lds1(rws, PQ_OFF + (((5 * h + 4) * 64 + sb) * 16 + c) * 16, 0);
    }
    SB();
    ld_w1(C1, C2);
    wait_flags(rws, FB_OFF + hp * 128 + (w >> 1) * 32, 8, tag, l);
    STAMP(8);
    {
      const int h = 2 * hp + (w >> 1), et = w & 1;
      f32x4 p0[8], p1[8];
#pragma unroll
      for (int xx = 0; xx < 8; xx++) {
        p0[xx] = __builtin_bit_cast(f32x4, lds1(rws, KVP_OFF + (h * 8 * 4 + et) * 1024 + l * 16, xx * 4096));
        p1[xx] = __builtin_bit_cast(f32x4, lds1(rws, KVP_OFF + (h * 8 * 4 + 2 + et) * 1024 + c * 16, xx * 4096));
      }
      SB();
      f32x4 s0 = p0[0], s1 = p1[0];
#pragma unroll
      for (int xx = 1; xx < 8; xx++) { s0 += p0[xx]; s1 += p1[xx]; }
      v4u fr = {pk2(s0[0], s0[1]), pk2(s0[2], s0[3]), g == 0 ? pk2(s1[0], s1[1]) : 0u, g == 0 ? pk2(s1[2], s1[3]) : 0u};
      *(LAS v4u*)(L + w * 1024 + l * 16) = fr;
    }
    BAR();
    STAMP(9);
    v4u kf[4];
#pragma unroll
    for (int q = 0; q < 4; q++) kf[q] = *(LAS v4u*)(L + q * 1024 + l * 16);
#pragma unroll
    for (int t = 0; t < 2; t++) {
      unsigned o0[4], o1[4];
#pragma unroll
      for (int hh = 0; hh < 2; hh++) {
        const v4u bq = {q1[hh][2 * t], q1[hh][2 * t + 1], q2[hh][2 * t], q2[hh][2 * t + 1]};
        const f32x4 z = {0, 0, 0, 0};
        f32x4 n0 = mfma(kf[2 * hh], bq, z), n1 = mfma(kf[2 * hh + 1], bq, z);
        auto sw = __builtin_amdgcn_permlane16_swap(__float_as_uint(n1[0]), __float_as_uint(n1[0]), false, false);
        const float r = __builtin_amdgcn_rcpf(__uint_as_float(sw[1]) + 1e-6f);
        o0[2 * hh] = pk2(n0[0] * r, n0[1] * r); o0[2 * hh + 1] = pk2(n0[2] * r, n0[3] * r);
        o1[2 * hh] = pk2(n1[0] * r, n1[1] * r); o1[2 * hh + 1] = pk2(n1[2] * r, n1[3] * r);
      }
      const int base = OF_OFF + (sb * 2 + t) * 20480 + 5 * hp * 256;
      sts1(v4u{o0[0], o0[1], o0[2], o0[3]}, rws, base + l * 16, 0);
      if (g == 0) sts1(v4u{o1[0], o1[1], o1[2], o1[3]}, rws, base + 1024 + l * 16, 0);
    }
    STAMP(10);
    publish(rws, FC_OFF + rb * 128 + hp * 4, tag);
    STAMP(11);
  }
  LAS char* rdst = L + w * RED_W + c * RED_ROW + g * 16;
  {
    const rsrc_t rx = mkrs(x, S * D * 2);
    v4u xr[3];
#pragma unroll
    for (int r = 0; r < 3; r++) { const int it = tid + 256 * r; const int rt = it / 160, e = it - rt * 160, sl = e >> 4, tok = rt * 16 + (e & 15);
      xr[r] = ldp(rx, it < 640 ? (grp * 64 + tok) * 1280 + j * 160 + sl * 16 : 0, 0); }
    ld_w1(C2, C3);
    wait_flags(rws, FC_OFF + (grp >> 1) * 128, 16, tag, l);
    STAMP(12);
    v4u a[20];
#pragma unroll
    for (int q = 0; q < 20; q++) a[q] = lds1(rws, l * 16, OF_OFF + ((grp * 4 + q / 5) * 20 + w * 5 + q % 5) * 1024);
    SB();
    mm80<5>(a, bo, rdst);
    SB();
    STAMP(13);
    BAR();
#pragma unroll
    for (int r = 0; r < 3; r++) { const int it = tid + 256 * r; const int rt = it / 160, e = it - rt * 160, sl = e >> 4, tok = rt * 16 + (e & 15);
      if (it < 640) {
        float v[8]; red8(L, tok, sl, v);
        v4u o;
#pragma unroll
        for (int q = 0; q < 4; q++) o[q] = pk2(v[2 * q] + bflo(xr[r][q]), v[2 * q + 1] + bfhi(xr[r][q]));
        sts1(o, rws, HF_OFF + (grp * 4 + rt) * 20480 + (10 * j) * 256 + e * 16, 0);
        *(LAS v4u*)(L + HK_OFF + (tok * 10 + sl) * 16) = o;
      }
    }
    STAMP(14);
    publish(rws, FD_OFF + grp * 128 + j * 4, tag);
    STAMP(15);
  }
  {
    ld_w1(C3, 60);
    const rsrc_t rb1 = mkrs(b1p, F * 2);
    v2u bias[3];
#pragma unroll
    for (int n = 0; n < 3; n++) bias[n] = __builtin_amdgcn_raw_buffer_load_b64(rb1, ((12 * j + 3 * w + n) * 16 + 4 * g) * 2, 0, 0);
    wait_flags(rws, FQ_OFF + w * 256, 64, tag, l);
    const v4u ones = {ONES2, ONES2, ONES2, ONES2};
    f32x4 cs[3];
#pragma unroll
    for (int q = 0; q < 60; q++) cs[q % 3] = mfma(bw1[q], ones, q < 3 ? f32x4{0, 0, 0, 0} : cs[q % 3]);
    SB();
    wait_flags(rws, FD_OFF + grp * 128, 8, tag, l);
    STAMP(16);
    v4u a[20];
#pragma unroll
    for (int q = 0; q < 20; q++) a[q] = lds1(rws, l * 16, HF_OFF + ((grp * 4 + q / 5) * 20 + w * 5 + q % 5) * 1024);
    SB();
    BAR();
    ld_w2(0, E1);
#pragma unroll
    for (int m = 0; m < 4; m++) {
      float s = 0, q = 0;
#pragma unroll
      for (int k = 0; k < 5; k++) {
        *(LAS v4u*)(L + (m * 20 + w * 5 + k) * 1024 + l * 16) = a[m * 5 + k];
#pragma unroll
        for (int e = 0; e < 4; e++) { s = dot2(a[m * 5 + k][e], ONES2, s); q = dot2(a[m * 5 + k][e], a[m * 5 + k][e], q); }
      }
      s = add_x32(add_x16(s)); q = add_x32(add_x16(q));
      if (g == 0) *(LAS f32x2*)(L + P3_ST + ((m * 4 + w) * 16 + c) * 8) = f32x2{s, q};
    }
    BAR();
    STAMP(17);
    constexpr int RD = 4;
    v4u af[RD];
#pragma unroll
    for (int q = 0; q < RD; q++) af[q] = *(LAS v4u*)(L + q * 1024 + l * 16);
#pragma unroll
    for (int m = 0; m < 4; m++) {
      f32x4 acc[3];
#pragma unroll
      for (int k = 0; k < 20; k++) {
        const int st = m * 20 + k;
        const v4u cur = af[st % RD];
#pragma unroll
        for (int n = 0; n < 3; n++) acc[n] = mfma(bw1[k * 3 + n], cur, k == 0 ? f32x4{0, 0, 0, 0} : acc[n]);
        if (st + RD < 80) af[st % RD] = *(LAS v4u*)(L + (st + RD) * 1024 + l * 16);
        SB();
      }
      float s = 0, q = 0;
#pragma unroll
      for (int ww = 0; ww < 4; ww++) { const f32x2 p = *(LAS f32x2*)(L + P3_ST + ((m * 4 + ww) * 16 + c) * 8); s += p.x; q += p.y; }
      const float mean = s * (1.f / D);
      const float var = fmaxf(q * (1.f / D) - mean * mean, 0.f);
      const float r1 = __builtin_amdgcn_rsqf(var + 1e-5f);
      const float rs = r1 * __builtin_amdgcn_rsqf(var * r1 * r1 + 1e-5f);
      unsigned o[3][2];
#pragma unroll
      for (int n = 0; n < 3; n++) {
        float v[4];
#pragma unroll
        for (int e = 0; e < 4; e++) {
          const float bb = (e & 1) ? bfhi(bias[n][e >> 1]) : bflo(bias[n][e >> 1]);
          v[e] = gelu(__builtin_fmaf(rs, acc[n][e] - mean * cs[n][e], bb));
        }
        o[n][0] = pk2(v[0], v[1]); o[n][1] = pk2(v[2], v[3]);
      }
      const int base = GF_OFF + (grp * 4 + m) * 49152 + 6 * (4 * j + w) * 256;
      sts1(v4u{o[0][0], o[0][1], o[1][0], o[1][1]}, rws, base + l * 16, 0);
      auto s0 = __builtin_amdgcn_permlane16_swap(o[2][0], o[2][0], false, false);
      auto s1 = __builtin_amdgcn_permlane16_swap(o[2][1], o[2][1], false, false);
      if (!(g & 1)) sts1(v4u{s0[0], s1[0], s0[1], s1[1]}, rws, base + 1024 + (g >> 1) * 256 + c * 16, 0);
      SB();
    }
    STAMP(18);
    publish(rws, FE_OFF + grp * 128 + j * 4, tag);
    STAMP(19);
  }
  {
    ld_w2(E1, 60);
    const rsrc_t ro = mkrs(out, S * D * 2), rb2 = mkrs(b2p, D * 2);
    v4u br[3];
#pragma unroll
    for (int r = 0; r < 3; r++) { const int it = tid + 256 * r; const int tok = it / 10, sl = it - tok * 10; br[r] = ldp(rb2, j * 160 + sl * 16, 0); }
    wait_flags(rws, FE_OFF + grp * 128, 8, tag, l);
    STAMP(20);
    v4u a[48];
#pragma unroll
    for (int q = 0; q < 48; q++) a[q] = lds1(rws, l * 16, GF_OFF + ((grp * 4 + q / 12) * 48 + w * 12 + q % 12) * 1024);
    SB();
    mm80<12>(a, bw2, rdst);
    SB();
    STAMP(21);
    BAR();
#pragma unroll
    for (int r = 0; r < 3; r++) { const int it = tid + 256 * r; const int tok = it / 10, sl = it - tok * 10;
      if (it < 640) {
        float v[8]; red8(L, tok, sl, v);
        const v4u hr = *(LAS v4u*)(L + HK_OFF + it * 16);
        v4u o;
#pragma unroll
        for (int q = 0; q < 4; q++) o[q] = pk2(v[2 * q] + bflo(br[r][q]) + bflo(hr[q]), v[2 * q + 1] + bfhi(br[r][q]) + bfhi(hr[q]));
        __builtin_amdgcn_raw_buffer_store_b128(o, ro, (grp * 64 + tok) * 1280 + j * 160 + sl * 16, 0, SC1);
      }
    }
  }
  if (blockIdx.x == 0 && tid == 0) __builtin_amdgcn_raw_buffer_store_b32(tag, rws, EPOCH_OFF, 0, SC1);
}

extern "C" void kernel_launch(void* const* d_in, const int* in_sizes, int n_in, void* d_out, int out_size, void* d_ws, size_t ws_size, hipStream_t stream) {
  (void)hipFuncSetAttribute((const void*)k_fused, hipFuncAttributeMaxDynamicSharedMemorySize, 160 * 1024);
  k_fused<<<256, 256, 160 * 1024, stream>>>(d_in[0], d_in[1], d_in[2], d_in[4], d_in[5], d_in[6], d_in[7], d_out, (char*)d_ws);
}
```

```cpp
#include <hip/hip_runtime.h>
#include <hip/hip_bf16.h>
#include <stdint.h>
#include <type_traits>

#define LAS __attribute__((address_space(3)))
#define DI __device__ __forceinline__
typedef __attribute__((ext_vector_type(8))) __bf16 bf16x8;
typedef __attribute__((ext_vector_type(2))) __bf16 bf16x2;
typedef __attribute__((ext_vector_type(4))) float f32x4;
typedef __attribute__((ext_vector_type(2))) float f32x2;
typedef __attribute__((ext_vector_type(4))) unsigned v4u;
typedef __attribute__((ext_vector_type(2))) unsigned v2u;
typedef __attribute__((ext_vector_type(4))) short s16x4;
typedef __amdgpu_buffer_rsrc_t rsrc_t;

constexpr int S = 2048, D = 640, F = 1536;
constexpr int ACT = S * D * 2;
constexpr int EPOCH_OFF = 0;
constexpr int FA = 1;
constexpr int FP = FA + 256;
constexpr int FB = FP + 256;
constexpr int FC = FB + 1024;
constexpr int FD = FC + 1024;
constexpr int FE = FD + 4096;
constexpr int NFLAG = FE + 4096;
constexpr int PQ_OFF = (NFLAG * 256 + 65535) / 65536 * 65536, PK_OFF = PQ_OFF + ACT, PV_OFF = PK_OFF + ACT, OF_OFF = PV_OFF + ACT, HF_OFF = OF_OFF + ACT;
constexpr int GF_OFF = HF_OFF + ACT, WOP_OFF = GF_OFF + S * F * 2, W1P_OFF = WOP_OFF + D * D * 2, W2P_OFF = W1P_OFF + D * F * 2;
constexpr int KVP_OFF = W2P_OFF + D * F * 2, ST_OFF = KVP_OFF + 32 * 8 * 4096, HS_OFF = ST_OFF + S * 16, CS_OFF = HS_OFF + S * 64, WS_END = CS_OFF + 1920 * 16;
constexpr int OOB = 0x7ffffff0;
constexpr int SC1 = 16;
constexpr unsigned ONES2 = 0x3f803f80u;

DI rsrc_t mkrs(const void* p, int bytes) { return __builtin_amdgcn_make_buffer_rsrc((void*)p, 0, bytes, 0x00020000); }
DI v4u mkrs4(const void* p, int bytes) { const uint64_t a = (uint64_t)p; return v4u{(unsigned)a, (unsigned)(a >> 32) & 0xffffu, (unsigned)bytes, 0x00020000u}; }
DI f32x4 mfma(v4u a, v4u b, f32x4 c) {
  return __builtin_amdgcn_mfma_f32_16x16x32_bf16(__builtin_bit_cast(bf16x8, a), __builtin_bit_cast(bf16x8, b), c, 0, 0, 0);
}
DI void mfma_t(v4u a, v4u b, f32x4& c) { asm volatile("v_mfma_f32_16x16x32_bf16 %0, %1, %2, %0" : "+v"(c) : "v"(a), "v"(b)); }
DI void mfma_z(v4u a, v4u b, f32x4& c) { asm volatile("v_mfma_f32_16x16x32_bf16 %0, %1, %2, 0" : "=&v"(c) : "v"(a), "v"(b)); }
template <bool FIRST, bool AG, bool BG = false> DI void mfma_w(v4u a, v4u b, f32x4& c) {
#define MF_(CA, CB) do { if constexpr (FIRST) asm volatile("v_mfma_f32_16x16x32_bf16 %0, %1, %2, 0" : "=&v"(c) : CA(a), CB(b)); \
                         else asm volatile("v_mfma_f32_16x16x32_bf16 %0, %1, %2, %0" : "+v"(c) : CA(a), CB(b)); } while (0)
  if constexpr (AG && BG) MF_("a", "a"); else if constexpr (AG) MF_("a", "v"); else if constexpr (BG) MF_("v", "a"); else MF_("v", "v");
#undef MF_
}
template <bool FIRST> DI void mfma_a(v4u a, v4u b, f32x4& c) { if constexpr (FIRST) mfma_z(a, b, c); else mfma_t(a, b, c); }
DI unsigned pk2(float a, float b) { f32x2 f = {a, b}; return __builtin_bit_cast(unsigned, __builtin_convertvector(f, bf16x2)); }
DI float bflo(unsigned u) { return __uint_as_float(u << 16); }
DI float bfhi(unsigned u) { return __uint_as_float(u & 0xffff0000u); }
DI v4u ldp(rsrc_t r, int voff, int soff) { return __builtin_amdgcn_raw_buffer_load_b128(r, voff, soff, 0); }
DI v4u lds1(rsrc_t r, int voff, int soff) { return __builtin_amdgcn_raw_buffer_load_b128(r, voff, soff, SC1); }
DI void sts1(v4u v, rsrc_t r, int voff, int soff) { __builtin_amdgcn_raw_buffer_store_b128(v, r, voff, soff, SC1); }
DI v2u trread(LAS char* p) { return __builtin_bit_cast(v2u, __builtin_amdgcn_ds_read_tr16_b64_v4i16((LAS s16x4*)p)); }
#define WAITV(n) asm volatile("s_waitcnt vmcnt(" #n ")" ::: "memory")
#define BAR() do { asm volatile("s_waitcnt lgkmcnt(0)" ::: "memory"); __builtin_amdgcn_s_barrier(); asm volatile("" ::: "memory"); } while (0)
#define SB() __builtin_amdgcn_sched_barrier(0)
constexpr float LOG2E = 1.4426950408889634f;
DI float phi(float t) { return fmaxf(t, 0.f) + __builtin_amdgcn_fmed3f(__builtin_amdgcn_exp2f(t * LOG2E), 0.f, 1.f) + 1e-6f; }
DI float dot2(unsigned a, unsigned b, float c) { return __builtin_amdgcn_fdot2_f32_bf16(__builtin_bit_cast(bf16x2, a), __builtin_bit_cast(bf16x2, b), c, false); }
DI float gelu(float v) {
  const float u = v * (0.7978845608028654f * 2.f * LOG2E) * __builtin_fmaf(v * v, 0.044715f, 1.0f);
  return v * __builtin_amdgcn_rcpf(1.0f + __builtin_amdgcn_exp2f(-u));
}
template <class T> DI void pin(T& x) { asm volatile("" : "+v"(x)); }
template <class T> DI T P(T x) { pin(x); return x; }
template <int CTRL> DI float dppadd(float v) { return v + __uint_as_float(__builtin_amdgcn_update_dpp(0u, __float_as_uint(v), CTRL, 0xf, 0xf, true)); }
DI float sum16(float v) { v = dppadd<0xB1>(v); v = dppadd<0x4E>(v); v = dppadd<0x141>(v); return dppadd<0x140>(v); }
DI float add_x16(float v) { auto r = __builtin_amdgcn_permlane16_swap(__float_as_uint(v), __float_as_uint(v), false, false); return __uint_as_float(r[0]) + __uint_as_float(r[1]); }
DI float add_x32(float v) { auto r = __builtin_amdgcn_permlane32_swap(__float_as_uint(v), __float_as_uint(v), false, false); return __uint_as_float(r[0]) + __uint_as_float(r[1]); }

DI void glds4(v4u rs, unsigned dst, int soff, int v0, int v1, int v2, int v3) {
  asm volatile("s_nop 4\n\ts_mov_b32 m0, %1\n\ts_nop 0\n\tbuffer_load_dwordx4 %3, %0, %2 offen lds\n\t"
               "s_add_u32 m0, m0, 0x400\n\ts_nop 0\n\tbuffer_load_dwordx4 %4, %0, %2 offen lds\n\t"
               "s_add_u32 m0, m0, 0x400\n\ts_nop 0\n\tbuffer_load_dwordx4 %5, %0, %2 offen lds\n\t"
               "s_add_u32 m0, m0, 0x400\n\ts_nop 0\n\tbuffer_load_dwordx4 %6, %0, %2 offen lds"
               :: "s"(rs), "s"(dst), "s"(soff), "v"(v0), "v"(v1), "v"(v2), "v"(v3) : "memory", "scc");
}
template <int IMM> DI void glds1(v4u rs, unsigned base, int soff, int v) {
  asm volatile("s_add_u32 m0, %1, %4\n\ts_nop 0\n\tbuffer_load_dwordx4 %3, %0, %2 offen lds" :: "s"(rs), "s"(base), "s"(soff), "v"(v), "i"(IMM) : "memory", "scc");
}
template <int IMM> DI void glds1_sc1(v4u rs, unsigned base, int soff, int v) {
  asm volatile("s_add_u32 m0, %1, %4\n\ts_nop 0\n\tbuffer_load_dwordx4 %3, %0, %2 offen sc1 lds" :: "s"(rs), "s"(base), "s"(soff), "v"(v), "i"(IMM) : "memory", "scc");
}
template <int N> using IC = std::integral_constant<int, N>;
template <int N> DI void waitv() { asm volatile("s_waitcnt vmcnt(%0)" :: "i"(N) : "memory"); }
template <int I, int N, class Fn> DI void static_for(Fn&& f) { if constexpr (I < N) { f(std::integral_constant<int, I>{}); static_for<I + 1, N>(f); } }
#define PIN(x) pin(x)
#define STAMP(id)
#define STAMP_ARG
#define STAMP_PASS
#define CSTAMP(id)

#define SLP 0
DI unsigned flag_ld(rsrc_t rws, int f, int n, int l) { asm volatile("" ::: "memory"); return __builtin_amdgcn_raw_buffer_load_b32(rws, (l < n ? l : 0) * 256, f * 256, SC1); }
DI void flag_wait(unsigned v, rsrc_t rws, int f, int n, unsigned tag, int l) {
  if (!__all(v == tag)) {
#pragma unroll 1
    for (int sp = 0; sp < (1 << 16); sp++) { if (__all(flag_ld(rws, f, n, l) == tag)) break; }
  }
  asm volatile("" ::: "memory");
}
DI void wait_flags(rsrc_t rws, int f, int n, unsigned tag, int l) { flag_wait(flag_ld(rws, f, n, l), rws, f, n, tag, l); }
DI void flag_st(rsrc_t rws, int f, unsigned tag, bool me, int l) {
  asm volatile("" ::: "memory"); sts1(v4u{tag, tag, tag, tag}, rws, (me && l < 8) ? l * 16 : OOB, f * 256); asm volatile("" ::: "memory");
}

DI int krow_wo(int ks, int rho) { int sg = 4 * ks + (rho >> 3); int p = sg / 5, i = sg - 5 * p; return 40 * p + 4 * i + 20 * ((rho >> 2) & 1) + (rho & 3); }
DI int krow_w2(int ks, int rho) { int sg = 4 * ks + (rho >> 3); int W = sg / 6, i = sg - 6 * W;
  return 48 * W + (i < 4 ? 4 * i + 16 * ((rho >> 2) & 1) + (rho & 3) : 32 + 8 * (i - 4) + (rho & 7)); }
template <int MAT> DI void pack_src(int ks, int cb, int l, int (&v)[4]) {
  constexpr int ROWB = MAT == 1 ? F * 2 : D * 2;
#pragma unroll
  for (int i = 0; i < 4; i++) {
    const int rho = 8 * i + (l >> 3);
    const int kr = MAT == 0 ? krow_wo(ks, rho) : (MAT == 1 ? 32 * ks + rho : krow_w2(ks, rho));
    const int f = ((rho >> 1) & 1) | (((rho >> 3) & 1) << 1);
    v[i] = kr * ROWB + cb * 128 + (((l & 7) ^ (f << 1)) * 16);
  }
}
DI void pack_issue(int u, int l, const void* wo, const void* w1, const void* w2, unsigned stg, int& dst, int& tstride) {
  int v[4];
  if (u < 288) { const int ks = 8 + u / 24, cb = u % 24; pack_src<1>(ks, cb, l, v); dst = W1P_OFF + (cb * 80 + ks) * 1024; tstride = 20480;
    glds4(mkrs4(w1, D * F * 2), stg, 0, v[0], v[1], v[2], v[3]); }
  else if (u < 768) { const int ks = (u - 288) / 10, cb = (u - 288) % 10; pack_src<2>(ks, cb, l, v); dst = W2P_OFF + (cb * 192 + ks) * 1024; tstride = 49152;
    glds4(mkrs4(w2, D * F * 2), stg, 0, v[0], v[1], v[2], v[3]); }
  else { const int ks = (u - 768) / 10, cb = (u - 768) % 10; pack_src<0>(ks, cb, l, v); dst = WOP_OFF + (cb * 80 + ks) * 1024; tstride = 20480;
    glds4(mkrs4(wo, D * D * 2), stg, 0, v[0], v[1], v[2], v[3]); }
}
DI int unit_lane(int t, int l) { const int g = l >> 4, q = (l >> 2) & 3, p = l & 3, f = ((q >> 1) & 1) | ((g & 1) << 1); return (8 * g + q) * 128 + ((t ^ f) * 32) + p * 8; }
DI v4u unit_read(LAS char* p) { const v2u a = trread(p), b = trread(p + 512); return v4u{a.x, a.y, b.x, b.y}; }

#define ABL 0
constexpr int P1_SLOT = 32768, XT_OFF = 4 * P1_SLOT, XT_W = 8192, WARM_OFF = 3 * P1_SLOT;
constexpr int RED_ROW = 336, RED_W = 16 * RED_ROW, RED_BUF = 4 * RED_W, A3_0 = 2 * RED_BUF, W2L_OFF = 65536, OS_OFF = 32768;
constexpr int NL2 = 24;

#define M1 12
#define M2 16
#define N2 6
#define N3 10
#define N4 26
#define N5 34
#define N6 40
#define N1 0
#define N7 51

__global__ __launch_bounds__(256, 1) void k_fused(const void* x, const void* wqkv, const void* wo, const void* w1, const void* b1p, const void* w2,
                                                  const void* b2p, void* out, char* ws) {
  extern __shared__ __attribute__((aligned(16))) char smem[];
  LAS char* L = (LAS char*)smem;
  const unsigned Lb = (unsigned)(size_t)L;
  STAMP(0);
  const int tid = threadIdx.x, l = tid & 63, w = __builtin_amdgcn_readfirstlane(tid >> 6), c = l & 15, g = l >> 4;
  const int xg = blockIdx.x & 7, i = blockIdx.x >> 3;
  const rsrc_t rws = mkrs(ws, WS_END);
  const v4u rws4 = mkrs4(ws, WS_END), rx4 = mkrs4(x, S * D * 2), rw4 = mkrs4(wqkv, D * 1920 * 2);
  const unsigned Lw = Lb + w * 4096, Lx = Lb + XT_OFF + w * XT_W;
  LAS char* xt = L + XT_OFF + w * XT_W;
  const v4u rw14 = mkrs4(w1, D * F * 2);
  const int grp = xg * 4 + (i >> 3), j = i & 7;
  const int srow = xg * 256 + i * 8 + 2 * w;
  asm volatile("s_nop 4");
  glds1_sc1<7168>(rws4, Lx, EPOCH_OFF, l == 0 ? 0 : OOB);
  glds1<4096>(rx4, Lx, srow * 1280, l * 16);
  glds1<5120>(rx4, Lx, srow * 1280 + 1024, l * 16);
  glds1<6144>(rx4, Lx, srow * 1280 + 2048, l < 32 ? l * 16 : OOB);
  int va[4], vb[4];
  const bool isq = i >= 20;
  const int rb = xg * 2 + (i & 1), ct = isq ? (i - 20) >> 1 : i >> 1;
#pragma unroll
  for (int u = 0; u < 4; u++) {
    const int ra = (w * 4 + u) * 8 + (l >> 3);
    va[u] = (rb * 128 + ra) * 1280 + (((l & 7) ^ ((ra >> 1) & 7)) * 16);
    const int rk = (w * 4 + u) * 4 + (l >> 4);
    const int ch = (l & 15) ^ ((((rk & 3) | (((rk >> 3) & 1) << 2))) << 1);
    vb[u] = rk * 3840 + (isq ? ct * 256 + ch * 16 : 1280 + (ch >> 3) * 1280 + ct * 128 + (ch & 7) * 16);
  }
  auto stage = [&](int s) { glds4(rx4, Lw + s * P1_SLOT, s * 128, va[0], va[1], va[2], va[3]); glds4(rw4, Lw + s * P1_SLOT + 16384, s * 64 * 3840, vb[0], vb[1], vb[2], vb[3]); };
  if (i < 30) stage(0);
  {
    uint64_t pc; unsigned len;
    asm volatile("s_getpc_b64 %0\n.Lk_here:\n\ts_mov_b32 %1, .Lk_end-.Lk_here" : "=s"(pc), "=s"(len));
    const unsigned Ls = Lw;
    const int q = blockIdx.x * 4 + w;
    glds1<WARM_OFF>(mkrs4((const void*)pc, len & ~15u), Ls, (i * 4 + w) * 1024, l * 16);
    glds1<WARM_OFF + 1024>(rw4, Ls, (352 + q) * 1024, l * 16);
    glds1<WARM_OFF>(rw4, Ls, (1376 + q) * 1024, l * 16);
    glds1<WARM_OFF + 1024>(rw14, Ls, q < 768 ? q * 1024 : OOB, l * 16);
  }
  unsigned tag = 0;
  auto row_stats = [&]() {
    tag = __builtin_amdgcn_readfirstlane(*(LAS unsigned*)(xt + 7168)) + 1u;
    LAS char* xr = xt + 4096 + (l >> 5) * 1280;
    const v4u xs0 = *(LAS v4u*)(xr + (l & 31) * 16), xs1 = *(LAS v4u*)(xr + 512 + (l & 31) * 16);
    v4u xs2 = *(LAS v4u*)(xr + 1024 + (l & 15) * 16);
    if (l & 16) xs2 = v4u{0, 0, 0, 0};
    float v[24], s = 0.f;
#pragma unroll
    for (int e = 0; e < 4; e++) { v[2 * e] = bflo(xs0[e]); v[2 * e + 1] = bfhi(xs0[e]); v[8 + 2 * e] = bflo(xs1[e]); v[9 + 2 * e] = bfhi(xs1[e]); v[16 + 2 * e] = bflo(xs2[e]); v[17 + 2 * e] = bfhi(xs2[e]); }
#pragma unroll
    for (int e = 0; e < 24; e++) s += v[e];
    const float mean = add_x16(sum16(s)) * (1.f / D);
    float q = 0.f, q2 = 0.f;
#pragma unroll
    for (int e = 0; e < 16; e++) { const float d = v[e] - mean; q += d * d; }
#pragma unroll
    for (int e = 16; e < 24; e++) { const float d = v[e] - mean; q2 += d * d; }
    q += (l & 16) ? 0.f : q2;
    const float rs = __builtin_amdgcn_rsqf(add_x16(sum16(q)) * (1.f / D) + 1e-5f);
    sts1(v4u{__float_as_uint(mean), tag, __float_as_uint(rs), tag}, rws, (l & 31) == 0 ? ST_OFF + (srow + (l >> 5)) * 16 : OOB, 0);
  };
  int vn[2];
#pragma unroll
  for (int u = 0; u < 2; u++) { const int rho = 8 * u + (l >> 3); vn[u] = rho * 3072 + (((l & 7) ^ ((((rho >> 1) & 1) | (u << 1)) << 1)) * 16); }
  const int sn = w * 32 * 3072 + j * 384;
  auto w1nat = [&](auto Pc, auto Uc) { constexpr int p = Pc, u = Uc;
    glds1<((65536 + p * 16384) & 131071) + u * 1024>(rw14, Lw, sn + ((p & 1) * 128 + (u >> 1) * 16) * 3072 + (p >> 1) * 128, vn[u & 1]); };
  if (i < 30) {
    stage(1);
    int pdst, pstride;
    pack_issue((xg * 30 + i) * 4 + w, l, wo, w1, w2, Lx, pdst, pstride);
    const int wr = w >> 1, wc = w & 1;
    f32x4 acc[4][4];
    LAS char* ap[2][2]; LAS char* bp[2][4];
#pragma unroll
    for (int hf = 0; hf < 2; hf++) {
#pragma unroll
      for (int kk = 0; kk < 2; kk++) ap[hf][kk] = L + hf * 65536 + (wr * 64 + c) * 128 + (((4 * kk + g) ^ ((c >> 1) & 7)) * 16);
#pragma unroll
      for (int n = 0; n < 4; n++) {
        const int tile = (n >> 1) * 4 + wc * 2 + (n & 1);
        const int q = (l >> 2) & 3, p = l & 3, f = q | ((g & 1) << 2);
        bp[hf][n] = L + hf * 65536 + 16384 + (8 * g + q) * 256 + ((tile ^ f) * 32) + p * 8;
      }
    }
    const int row0 = rb * 128 + wr * 64;
    const int vst = ST_OFF + (row0 + l) * 16;
    const int vcs = CS_OFF + ((isq ? ct * 128 + (g >> 1) * 64 : 640 + (g >> 1) * 640 + ct * 64) + (wc * 2 + (g & 1)) * 16 + c) * 16;
    v4u fa[2][4], pk[4]; v2u fb[2][4][2];
    auto rd = [&](auto Hc, auto Qc) {
      constexpr int H = Hc, Q = Qc, s = H >> 1, kk = H & 1, hf = (s >> 1) & 1, so = (s & 1) * P1_SLOT, n = Q / 3, r = Q % 3;
      if constexpr (r == 1) fa[kk][n] = *(LAS v4u*)(ap[hf][kk] + so + n * 2048);
      else fb[kk][n][r >> 1] = trread(bp[hf][n] + so + kk * 8192 + (r >> 1) * 1024);
    };
    auto stld = [&](auto Sc, auto Uc) { constexpr int st = Sc, u = Uc;
      if constexpr (u < 4) glds1<(st & 3) * P1_SLOT + u * 1024>(rx4, Lw, ((ABL & 8) ? st & 1 : st) * 128, va[u]);
      else glds1<(st & 3) * P1_SLOT + 16384 + (u - 4) * 1024>(rw4, Lw, ((ABL & 16) ? st & 1 : st) * 64 * 3840, vb[u - 4]); };
    waitv<16>(); BAR();
    STAMP(1);
    static_for<0, 12>([&](auto Q) { rd(IC<0>{}, Q); });
    SB();
    static_for<0, 20>([&](auto Hc) {
      constexpr int H = Hc, s = H >> 1, kk = H & 1;
      if constexpr (H == 3) { row_stats(); SB(); }
      if constexpr (kk == 1 && s < 9) { waitv<(s == 1 || s == 2 ? 13 : s < 7 ? 12 : 16)>(); BAR(); }
      if constexpr (H == 5) STAMP(23);
      if constexpr (H == 13) STAMP(24);
      if constexpr (H == 18) {
        v4u stg = *(LAS v4u*)(xt + 4096 + l * 16), csg = *(LAS v4u*)(xt + 5120 + l * 16);
        if (!__all(stg.y == tag && stg.w == tag && csg.y == tag && csg.w == tag)) {
#pragma unroll 1
          for (int sp = 0; sp < (1 << 16); sp++) { asm volatile("" ::: "memory"); stg = lds1(rws, vst, 0); csg = lds1(rws, vcs, 0);
            if (__all(stg.y == tag && stg.w == tag && csg.y == tag && csg.w == tag)) break; }
        }
        *(LAS v2u*)(xt + 4096 + l * 8) = v2u{stg.x, stg.z};
        *(LAS float*)(xt + 5120 + l * 4) = __uint_as_float(csg.x) + __uint_as_float(csg.z);
        SB();
      }
      static_for<0, 16>([&](auto Qc) {
        constexpr int Q = Qc, n = Q / 4, m = Q % 4;
        const v4u b = {fb[kk][n][0].x, fb[kk][n][0].y, fb[kk][n][1].x, fb[kk][n][1].y};
        if constexpr (!(ABL & 32) || H == 0 || (Q & 3) == 0) mfma_a<H == 0>(fa[kk][m], b, acc[m][n]);
        if constexpr (H < 19 && Q < 12) rd(IC<H + 1>{}, Qc);
        if constexpr (H < 2) { if constexpr (Q & 1) stld(IC<H + 2>{}, IC<Q / 2>{}); }
        else if constexpr (m == 1) {
          if constexpr (H < 14) stld(IC<(H + 6) / 2>{}, IC<(H & 1) * 4 + n>{});
          else w1nat(IC<H - 14>{}, IC<n>{});
        }
        if constexpr (H == 13 && Q == 14) glds1_sc1<4096>(rws4, Lx, 0, vst);
        if constexpr (H == 13 && Q == 15) glds1_sc1<5120>(rws4, Lx, 0, vcs);
        if constexpr (H == 12 && Q >= 12) pk[Q - 12] = unit_read(xt + unit_lane(Q - 12, l));
        if constexpr ((H == 14 || H == 15) && (Q == 3 || Q == 11)) sts1(pk[(H - 14) * 2 + Q / 8], rws, l * 16, pdst + ((H - 14) * 2 + Q / 8) * pstride);
        SB();
      });
    });
    waitv<17>(); BAR();
    flag_st(rws, FP + blockIdx.x, tag, w == 0, l);
    STAMP(4);
    f32x4 st[4][2]; float cs[4];
#pragma unroll
    for (int m = 0; m < 4; m++) { LAS f32x4* p = (LAS f32x4*)(xt + 4096 + (16 * m + 4 * g) * 8); st[m][0] = p[0]; st[m][1] = p[1]; cs[m] = *(LAS float*)(xt + 5120 + (16 * m + c) * 4); }
    auto epi = [&](int n, bool dophi) {
      const int f = (isq ? ct * 128 + (n >> 1) * 64 : ct * 64) + (wc * 2 + (n & 1)) * 16 + c;
      const int h = f / 20, d = f - 20 * h;
      const int base = (isq ? PQ_OFF : (n < 2 ? PK_OFF : PV_OFF)) + ((((h * 64 + (row0 >> 5)) * 4 + g) * 20) + d) * 16;
#pragma unroll
      for (int u = 0; u < 2; u++) {
        v4u o;
#pragma unroll
        for (int hh = 0; hh < 2; hh++) {
          const int m = 2 * u + hh; float v[4];
#pragma unroll
          for (int e = 0; e < 4; e++) {
            const float mean = st[m][e >> 1][(e & 1) * 2], rs = st[m][e >> 1][(e & 1) * 2 + 1];
            const float t = rs * (acc[m][n][e] - mean * cs[n]);
            v[e] = dophi ? phi(t) : t;
          }
          o[2 * hh] = pk2(v[0], v[1]); o[2 * hh + 1] = pk2(v[2], v[3]);
        }
        sts1(o, rws, base, u * 1280);
      }
    };
    if (isq) { epi(2, true); epi(3, true); } else { epi(2, false); epi(3, false); }
    epi(0, true); epi(1, true);
  } else {
    const int iw = (xg * 2 + i - 30) * 4 + w;
    if (iw < 60) {
      const rsrc_t rw = mkrs(wqkv, D * 1920 * 2);
      const int vo = ((iw & 1) * 320 + (l >> 3)) * 3840 + (iw >> 1) * 128 + (l & 7) * 16;
      v4u t[40];
#pragma unroll
      for (int q = 0; q < 40; q++) t[q] = ldp(rw, vo, q * 8 * 3840);
      SB();
      waitv<44>();
      row_stats();
      v4u sel;
#pragma unroll
      for (int e = 0; e < 4; e++) sel[e] = c == 2 * e ? 0x00003f80u : (c == 2 * e + 1 ? 0x3f800000u : 0u);
      f32x4 a = {0, 0, 0, 0};
#pragma unroll
      for (int q = 0; q < 40; q++) a = mfma(sel, t[q], a);
      const int co = (c < 8 && g < 2) ? CS_OFF + ((iw >> 1) * 64 + c * 8 + 4 * g) * 16 + (iw & 1) * 8 : OOB;
#pragma unroll
      for (int e = 0; e < 4; e++) __builtin_amdgcn_raw_buffer_store_b64(v2u{__float_as_uint(dppadd<0x128>(a[e])), tag}, rws, co, e * 16, SC1);
    } else {
      waitv<4>();
      row_stats();
#pragma unroll 1
      for (int r = 0; r < 2; r++) {
        int pdst, pstride;
        pack_issue(960 + (iw - 60) * 2 + r, l, wo, w1, w2, Lx, pdst, pstride);
        WAITV(0);
#pragma unroll
        for (int t = 0; t < 4; t++) sts1(unit_read(xt + unit_lane(t, l)), rws, l * 16, pdst + t * pstride);
      }
    }
    STAMP(1);
    static_for<0, 24>([&](auto Qc) { w1nat(IC<Qc / 4>{}, IC<Qc % 4>{}); });
    waitv<24>(); BAR();
    flag_st(rws, FP + blockIdx.x, tag, w == 0, l);
    STAMP(4);
  }
  STAMP(2);
  const unsigned fp = flag_ld(rws, FP + w * 64, 64, l);
  waitv<1>(); BAR();
  flag_st(rws, FA + xg * 32 + i, tag, w == 0, l);
  STAMP(3);
  v4u bw1[60], bwo[25], bw2[60];
#pragma unroll
  for (int n = 0; n < 3; n++) {
    const int T = 3 * w + n;
    LAS char* p = L + ((65536 + (T >> 2) * 32768) & 131071) + unit_lane(T & 3, l);
#pragma unroll
    for (int ks = 0; ks < 8; ks++) bw1[ks * 3 + n] = unit_read(p + ks * 4096);
  }
  flag_wait(fp, rws, FP + w * 64, 64, tag, l);
  BAR();
  STAMP(25);
  const unsigned L2w = Lb + W2L_OFF + w * (NL2 * 1024);
  auto w2off = [&](int q) { return W2P_OFF + ((5 * j + q % 5) * 48 + w * 12 + q / 5) * 1024; };
  auto bulk = [&](auto Qc) { constexpr int q = Qc;
    if constexpr (q < 25) bwo[q] = lds1(rws, l * 16, WOP_OFF + ((5 * j + q % 5) * 20 + w * 5 + q / 5) * 1024);
    else bw1[q - 1] = lds1(rws, l * 16, W1P_OFF + ((12 * j + 3 * w + (q - 25) % 3) * 20 + 8 + (q - 25) / 3) * 1024); };
  auto bulkl = [&](auto Qc) { constexpr int q = Qc; glds1_sc1<q * 1024>(rws4, L2w, w2off(60 - NL2 + q), l * 16); };
  auto ldw2 = [&](auto Qc) { constexpr int q = Qc; bw2[q] = lds1(rws, l * 16, w2off(q)); };
#define W1F(r) bw1[r]
#define WOF(r) bwo[r]
  static_for<0, M1>(bulkl); SB();
  wait_flags(rws, FA + xg * 32, 20, tag, l);
  STAMP(5);
  {
    const int dt = w >> 1, et = w & 1;
    const int hb = ((i * 64 + xg * 8) * 4 + g) * 20;
    const int ak = dt ? (c < 4 ? PK_OFF + (hb + 16 + c) * 16 : OOB) : PK_OFF + (hb + c) * 16;
    const int av = et ? (c < 4 ? PV_OFF + (hb + 16 + c) * 16 : OOB) : PV_OFF + (hb + c) * 16;
    v4u ka[8], kb[8];
#pragma unroll
    for (int sb = 0; sb < 8; sb++) { ka[sb] = lds1(rws, ak, sb * 1280); kb[sb] = lds1(rws, av, sb * 1280); }
    SB();
    static_for<N1, N2>(bulk); SB();
    f32x4 a0 = {0, 0, 0, 0};
    const unsigned om = (et && c >= 4) ? ONES2 : 0u;
#pragma unroll
    for (int sb = 0; sb < 8; sb++) a0 = mfma(ka[sb], kb[sb] | om, a0);
    sts1(__builtin_bit_cast(v4u, a0), rws, (dt && g) ? OOB : l * 16, KVP_OFF + ((i * 8 + xg) * 4 + w) * 1024);
    STAMP(6);
    const unsigned fq = flag_ld(rws, FA + xg * 32 + 20, 10, l);
    static_for<N2, N3>(bulk); SB();
    waitv<N3 - N2 + 1>();
    flag_st(rws, FB + (i * 2 + et) * 16 + xg * 2 + dt, tag, true, l);
    STAMP(7);
    flag_wait(fq, rws, FA + xg * 32 + 20, 10, tag, l);
  }
  {
    const int hp = i >> 1, sb = rb * 4 + w;
    const unsigned Lq = Lb + OS_OFF + 4096 + w * 4096;
    const int qo = PQ_OFF + (2 * hp * 64 + sb) * 1280;
    static_for<0, 4>([&](auto Rc) { constexpr int r = Rc; glds1_sc1<r * 1024>(rws4, Lq, qo + (r >> 1) * 81920, (r & 1) ? (l < 16 ? 1024 + l * 16 : OOB) : l * 16); });
    SB();
    static_for<N3, N4>(bulk); static_for<M1, M2>(bulkl); SB();
    const int h = 2 * hp + (w >> 1), et = w & 1;
    wait_flags(rws, FB + (h * 2 + et) * 16, 16, tag, l);
    STAMP(8);
    f32x4 p0[8], p1[8];
#pragma unroll
    for (int xx = 0; xx < 8; xx++) {
      p0[xx] = __builtin_bit_cast(f32x4, lds1(rws, l * 16, KVP_OFF + (h * 8 * 4 + et) * 1024 + xx * 4096));
      p1[xx] = __builtin_bit_cast(f32x4, lds1(rws, g == 0 ? c * 16 : OOB, KVP_OFF + (h * 8 * 4 + 2 + et) * 1024 + xx * 4096));
    }
    SB();
    static_for<N4, N5>(bulk); SB();
    v4u bq[2][2];
    {
      const int q = (l >> 2) & 3, p = l & 3;
      LAS char* qb = L + OS_OFF + 4096 + w * 4096 + (p * 20 + q) * 16;
#pragma unroll
      for (int hh = 0; hh < 2; hh++)
#pragma unroll
        for (int t = 0; t < 2; t++) {
          const v2u lo = trread(qb + hh * 2048 + t * 8 + g * 64), hi = trread(qb + hh * 2048 + t * 8 + 256);
          bq[hh][t] = v4u{lo.x, lo.y, hi.x, hi.y};
        }
    }
    f32x4 s0 = p0[0], s1 = p1[0];
#pragma unroll
    for (int xx = 1; xx < 8; xx++) { s0 += p0[xx]; s1 += p1[xx]; }
    *(LAS v4u*)(L + OS_OFF + w * 1024 + l * 16) = v4u{pk2(s0[0], s0[1]), pk2(s0[2], s0[3]), pk2(s1[0], s1[1]), pk2(s1[2], s1[3])};
    BAR();
    STAMP(9);
    v4u kf[4];
#pragma unroll
    for (int q = 0; q < 4; q++) kf[q] = *(LAS v4u*)(L + OS_OFF + q * 1024 + l * 16);
#pragma unroll
    for (int t = 0; t < 2; t++) {
      unsigned o0[4], o1[4];
#pragma unroll
      for (int hh = 0; hh < 2; hh++) {
        const f32x4 z = {0, 0, 0, 0};
        f32x4 n0 = mfma(kf[2 * hh], bq[hh][t], z), n1 = mfma(kf[2 * hh + 1], bq[hh][t], z);
        auto sw = __builtin_amdgcn_permlane16_swap(__float_as_uint(n1[0]), __float_as_uint(n1[0]), false, false);
        const float r = __builtin_amdgcn_rcpf(__uint_as_float(sw[1]) + 1e-6f);
        o0[2 * hh] = pk2(n0[0] * r, n0[1] * r); o0[2 * hh + 1] = pk2(n0[2] * r, n0[3] * r);
        o1[2 * hh] = pk2(n1[0] * r, n1[1] * r); o1[2 * hh + 1] = pk2(n1[2] * r, n1[3] * r);
      }
      const int base = OF_OFF + (sb * 2 + t) * 20480 + 5 * hp * 256;
      sts1(v4u{o0[0], o0[1], o0[2], o0[3]}, rws, l * 16, base);
      sts1(v4u{o1[0], o1[1], o1[2], o1[3]}, rws, g == 0 ? l * 16 : OOB, base + 1024);
    }
    STAMP(10);
    SB();
    static_for<N5, N6>(bulk); SB();
    waitv<N6 - N5>();
    flag_st(rws, FC + (rb * 2 + (w >> 1)) * 32 + hp * 2 + (w & 1), tag, true, l);
    BAR();
    STAMP(11);
  }
  LAS char* rdst = L + w * RED_W + c * RED_ROW + g * 16;
  const bool act = c < 10;
  LAS char* rsrc = L + (4 * w + g) * RED_ROW + (act ? c : 9) * 32;
  const int rowo = act ? (grp * 64 + 4 * w + g) * 1280 + j * 160 + c * 16 : OOB;
  auto red_ld = [&](int m, f32x4 (&r)[8]) {
#pragma unroll
    for (int ww = 0; ww < 4; ww++) { LAS f32x4* p = (LAS f32x4*)(rsrc + (m & 1) * RED_BUF + ww * RED_W); r[2 * ww] = p[0]; r[2 * ww + 1] = p[1]; }
  };
  v4u hk[4];
  unsigned fs0;
  v2u bias[3];
  {
    const rsrc_t rx = mkrs(x, S * D * 2);
#pragma unroll
    for (int m = 0; m < 4; m++) hk[m] = ldp(rx, rowo, m * 20480);
    const rsrc_t rb1 = mkrs(b1p, F * 2);
#pragma unroll
    for (int n = 0; n < 3; n++) bias[n] = __builtin_amdgcn_raw_buffer_load_b64(rb1, ((12 * j + 3 * w + n) * 16 + 4 * g) * 2, 0, 0);
    static_for<N6, N7>(bulk); static_for<M2, NL2>(bulkl); SB();
    wait_flags(rws, FC + grp * 32, 32, tag, l);
    STAMP(12);
    v4u a[20];
    auto lda = [&](auto Qc) { constexpr int q = Qc; a[q] = lds1(rws, l * 16, OF_OFF + ((grp * 4 + q / 5) * 20 + w * 5 + q % 5) * 1024); };
    static_for<0, 15>(lda);
    SB();
    f32x4 acc[5], r[8];
    auto fin = [&](int m) {
      const f32x4 s0 = (r[0] + r[2]) + (r[4] + r[6]), s1 = (r[1] + r[3]) + (r[5] + r[7]);
      v4u o;
      o[0] = pk2(s0[0] + bflo(hk[m][0]), s0[1] + bfhi(hk[m][0])); o[1] = pk2(s0[2] + bflo(hk[m][1]), s0[3] + bfhi(hk[m][1]));
      o[2] = pk2(s1[0] + bflo(hk[m][2]), s1[1] + bfhi(hk[m][2])); o[3] = pk2(s1[2] + bflo(hk[m][3]), s1[3] + bfhi(hk[m][3]));
      hk[m] = o;
      sts1(o, rws, rowo, HF_OFF + m * 20480);
      float s = 0.f, q = 0.f;
#pragma unroll
      for (int e = 0; e < 4; e++) { s = dot2(o[e], ONES2, s); q = dot2(o[e], o[e], q); }
      s = sum16(act ? s : 0.f); q = sum16(act ? q : 0.f);
      __builtin_amdgcn_raw_buffer_store_b64(v2u{__float_as_uint(s), __float_as_uint(q)}, rws, c == 0 ? HS_OFF + (grp * 64 + 4 * w + g) * 64 + j * 8 : OOB, m * 1024, SC1);
    };
    const int fdo = FD + grp * 128 + j * 4 + w;
    auto mmt = [&](auto Mc) { constexpr int m = Mc;
      static_for<0, 5>([&](auto Kc) { constexpr int k = Kc;
        static_for<0, 5>([&](auto Nc) { constexpr int n = Nc; mfma_w<k == 0, false>(WOF(k * 5 + n), a[m * 5 + k], acc[n]); });
        if constexpr (m == 0) lda(IC<15 + k>{}); else if constexpr (m < 3) bulk(IC<N7 + (m - 1) * 5 + k>{}); else ldw2(Kc);
        SB(); });
    };
    auto wrt = [&](int m) {
      asm volatile("s_nop 3");
#pragma unroll
      for (int n = 0; n < 5; n++) *(LAS f32x4*)(rdst + (m & 1) * RED_BUF + n * 64) = acc[n];
      BAR(); red_ld(m, r); SB();
    };
    mmt(IC<0>{}); wrt(0); fin(0); SB();
    STAMP(13);
    mmt(IC<1>{}); wrt(1);
    waitv<5>(); flag_st(rws, fdo, tag, true, l);
    mmt(IC<2>{}); fin(1); SB(); wrt(2);
    mmt(IC<3>{});
    waitv<5>(); flag_st(rws, fdo + 32, tag, true, l);
    fin(2); SB(); wrt(3);
    fs0 = flag_ld(rws, FD + grp * 128, 32, l);
    fin(3); SB();
    STAMP(14);
    waitv<3>(); flag_st(rws, fdo + 64, tag, true, l);
    STAMP(15);
  }
  {
    int vh[2];
#pragma unroll
    for (int q = 0; q < 2; q++) { const int rr = ((w + q) & 1) * 8 + (l >> 3); vh[q] = rr * 1280 + (((l & 7) ^ ((rr >> 1) & 7)) * 16); }
    const unsigned La = Lb + w * 5120;
    const int hso = HF_OFF + grp * 64 * 1280;
    v4u hs[2];
    auto ldh = [&](auto Mc, auto Qc) { constexpr int m = Mc, q = Qc;
      if constexpr (q < 5) glds1_sc1<((m & 1) ? 0 : A3_0) + q * 1024>(rws4, La, hso + m * 20480 + ((5 * w + q) >> 1) * 128, vh[q & 1]);
      else hs[m & 1] = lds1(rws, HS_OFF + (grp * 64 + c) * 64 + g * 16, m * 1024); };
    const int fdw = FD + grp * 128, few = FE + grp * 128, feo = few + j * 4 + w;
    flag_wait(fs0, rws, fdw, 32, tag, l);
    STAMP(16);
    static_for<0, 6>([&](auto Q) { ldh(IC<0>{}, Q); });
    waitv<7>(); flag_st(rws, FD + grp * 128 + j * 4 + w + 96, tag, true, l);
    unsigned fs = flag_ld(rws, fdw + 32, 32, l);
    SB();
    v4u ones = {ONES2, ONES2, ONES2, ONES2};
    PIN(ones);
    f32x4 cs[3];
    asm volatile("s_nop 1");
    static_for<0, 60>([&](auto Qc) { constexpr int q = Qc; mfma_w<(q < 3), true>(W1F(q), ones, cs[q % 3]);
      if constexpr (q % 5 == 0) ldw2(IC<5 + q / 5>{});
      SB(); });
    float bf[12];
#pragma unroll
    for (int e = 0; e < 12; e++) bf[e] = P((e & 1) ? bfhi(bias[e >> 2][(e >> 1) & 1]) : bflo(bias[e >> 2][(e >> 1) & 1]));
    SB();
    waitv<15>(); BAR();
    STAMP(17);
    LAS char* ab[2];
#pragma unroll
    for (int kk = 0; kk < 2; kk++) ab[kk] = L + c * 128 + (((4 * kk + g) ^ ((c >> 1) & 7)) * 16);
    auto rda = [&](int t) { return *(LAS v4u*)(ab[t & 1] + (((t / 20) & 1) ? 0 : A3_0) + ((t % 20) >> 1) * 2048); };
    constexpr int RD = 4;
    v4u af[RD];
    f32x4 acc[2][3];
    float nmean[2], rs[2], tv[2], tu[2], go[2];
    unsigned gp[6];
    const float C0 = -0.7978845608028654f * 2.f * LOG2E, C1 = C0 * 0.044715f;
    float c0v = C0, c1v = C1;
    PIN(c0v); PIN(c1v);
    auto stats = [&](int m) {
      const f32x4 p = __builtin_bit_cast(f32x4, hs[m & 1]);
      const float s = add_x32(add_x16(p[0] + p[2])), q = add_x32(add_x16(p[1] + p[3]));
      const float mean = s * (1.f / D);
      const float var = fmaxf(q * (1.f / D) - mean * mean, 0.f);
      const float r1 = __builtin_amdgcn_rsqf(var + 1e-5f);
      rs[m & 1] = P(r1 * __builtin_amdgcn_rsqf(var * r1 * r1 + 1e-5f));
      nmean[m & 1] = P(-mean);
    };
    auto uop = [&](auto Mc, auto Ec, auto Sc) {
      constexpr int m = Mc, e = Ec, st = Sc, z = e & 1;
      if constexpr (st == 0) tv[z] = P(__builtin_fmaf(nmean[m & 1], cs[e >> 2][e & 3], acc[m & 1][e >> 2][e & 3]));
      if constexpr (st == 1) tv[z] = P(__builtin_fmaf(rs[m & 1], tv[z], bf[e]));
      if constexpr (st == 2) tu[z] = P(tv[z] * tv[z]);
      if constexpr (st == 3) tu[z] = P(__builtin_fmaf(tu[z], c1v, c0v));
      if constexpr (st == 4) tu[z] = P(tu[z] * tv[z]);
      if constexpr (st == 5) tu[z] = P(__builtin_amdgcn_exp2f(tu[z]));
      if constexpr (st == 6) tu[z] = P(tu[z] + 1.0f);
      if constexpr (st == 7) tu[z] = P(__builtin_amdgcn_rcpf(tu[z]));
      if constexpr (st == 8) go[z] = P(tv[z] * tu[z]);
    };
    auto gstore = [&](int m) {
      const int base = GF_OFF + (grp * 4 + m) * 49152 + 6 * (4 * j + w) * 256;
      sts1(v4u{gp[0], gp[1], gp[2], gp[3]}, rws, l * 16, base);
      auto s0 = __builtin_amdgcn_permlane16_swap(gp[4], gp[4], false, false);
      auto s1 = __builtin_amdgcn_permlane16_swap(gp[5], gp[5], false, false);
      sts1(v4u{s0[0], s1[0], s0[1], s1[1]}, rws, (g & 1) ? OOB : (g >> 1) * 256 + c * 16, base + 1024);
    };
    auto egap = [&](auto Mc, auto Gc) {
      constexpr int m = Mc, G = Gc - 2;
      if constexpr (G >= 0 && G < 54) uop(Mc, IC<2 * (G / 9)>{}, IC<G % 9>{});
      if constexpr (G >= 1 && G < 55) uop(Mc, IC<2 * ((G - 1) / 9) + 1>{}, IC<(G - 1) % 9>{});
      if constexpr (G >= 10 && G <= 55 && (G - 1) % 9 == 0) gp[(G - 10) / 9] = P(pk2(go[0], go[1]));
      if constexpr (G == 57) gstore(m);
    };
    v4u a4[12];
    auto lda4 = [&](int q) { return lds1(rws, l * 16, GF_OFF + ((grp * 4 + q / 12) * 48 + w * 12 + q % 12) * 1024); };
    const rsrc_t ro = mkrs(out, S * D * 2), rb2 = mkrs(b2p, D * 2);
    v4u br;
#pragma unroll
    for (int q = 0; q < RD; q++) af[q] = rda(q);
    flag_wait(fs, rws, fdw + 32, 32, tag, l);
    SB();
    static_for<0, 80>([&](auto Tc) {
      constexpr int T = Tc, m = T / 20, k = T % 20;
      if constexpr (T == 36) STAMP(30);
      if constexpr (k == 16 && m < 3) { waitv<(m == 0 ? 7 : 6)>(); BAR(); }
      if constexpr (T == 36) STAMP(31);
      if constexpr (k == 0) STAMP(26 + m);
      if constexpr (k == 0 && (m == 1 || m == 2)) flag_wait(fs, rws, fdw + (m + 1) * 32, 32, tag, l);
      if constexpr (T == 68) STAMP(32);
      if constexpr (T == 68) flag_wait(fs, rws, few, 32, tag, l);
      if constexpr (T == 68) STAMP(33);
      const v4u cur = af[T % RD];
      static_for<0, 3>([&](auto Nc) {
        constexpr int n = Nc;
        mfma_w<k == 0, true>(W1F(k * 3 + n), cur, acc[m & 1][n]);
        if constexpr (m > 0) egap(IC<m - 1>{}, IC<k * 3 + n>{});
        if constexpr (n == 2 && T + RD < 80) af[T % RD] = rda(T + RD);
        if constexpr (n == 0) {
          if constexpr (m < 3 && k < 6) ldh(IC<m + 1>{}, IC<k>{});
          if constexpr (m < 2 && k == 7) fs = flag_ld(rws, fdw + (m + 2) * 32, 32, l);
          if constexpr (m < 3 && k >= 8 && (k & 1) == 0) ldw2(IC<17 + m * 6 + (k - 8) / 2>{});
          if constexpr (T == 9) ldw2(IC<35>{});
          if constexpr (m >= 2 && k == 7) { waitv<(m == 2 ? 6 : 2)>(); flag_st(rws, feo + (m - 2) * 32, tag, true, l); }
          if constexpr (T == 60) br = ldp(rb2, act ? j * 160 + c * 16 : OOB, 0);
          if constexpr (T == 62) fs = flag_ld(rws, few, 32, l);
          if constexpr (T >= 68) a4[T - 68] = lda4(T - 68);
        }
        if constexpr (k == 18 && n == 1) stats(m);
        if constexpr (T == 74 && n == 1) fs = flag_ld(rws, few + 32, 32, l);
        SB();
      });
    });
    static_assert(NL2 == 24, "36 register-resident w2 fragments are requested above");
    STAMP(18);
    f32x4 ac4[2][5], r[8];
    float b2f[8], ov[8];
#pragma unroll
    for (int e = 0; e < 8; e++) b2f[e] = P((e & 1) ? bfhi(br[e >> 1]) : bflo(br[e >> 1]));
    auto fop = [&](auto Mc, auto Ec, auto Hc) { constexpr int m = Mc, e = Ec, hf = Hc;
      if constexpr (hf == 0) { const float u = P(r[e >> 2][e & 3] + r[2 + (e >> 2)][e & 3]), v = P(r[4 + (e >> 2)][e & 3] + r[6 + (e >> 2)][e & 3]); ov[e] = P(u + v); }
      else { const float hv = P((e & 1) ? bfhi(hk[m][e >> 1]) : bflo(hk[m][e >> 1])); ov[e] = P(ov[e] + b2f[e]); ov[e] = P(ov[e] + hv); }
    };
    auto ost = [&](int m) { __builtin_amdgcn_raw_buffer_store_b128(v4u{pk2(ov[0], ov[1]), pk2(ov[2], ov[3]), pk2(ov[4], ov[5]), pk2(ov[6], ov[7])}, ro, rowo, m * 20480, SC1); };
    SB();
    static_for<0, 48>([&](auto Tc) {
      constexpr int T = Tc, m = T / 12, k = T % 12;
      if constexpr (k == 0) STAMP(34 + 2 * m);
      if constexpr (k == 0 && m < 3) flag_wait(fs, rws, few + (m + 1) * 32, 32, tag, l);
      if constexpr (k == 0 && m < 3) STAMP(35 + 2 * m);
      if constexpr (T == 6) BAR();
      if constexpr (k == 6 && m >= 1) { BAR(); red_ld(m - 1, r); SB(); }
      const v4u cur = a4[k];
      static_for<0, 5>([&](auto Nc) { constexpr int n = Nc, G = k * 5 + n;
        mfma_w<k == 0, (G >= 60 - NL2), true>(bw2[G], cur, ac4[m & 1][n]);
        if constexpr (m == 0) egap(IC<3>{}, IC<G>{});
        if constexpr (m == 0 && G < NL2) bw2[60 - NL2 + G] = *(LAS v4u*)(L + W2L_OFF + w * (NL2 * 1024) + G * 1024 + l * 16);
        if constexpr (m > 0 && G >= 38 && G < 54) fop(IC<m - 1>{}, IC<(G - 38) / 2>{}, IC<G & 1>{});
        if constexpr (m > 0 && G == 56) ost(m - 1);
        if constexpr (n == 4 && T + 12 < 48) a4[k] = lda4(T + 12);
        if constexpr (n == 1 && m > 0 && k < 5) *(LAS f32x4*)(rdst + ((m - 1) & 1) * RED_BUF + k * 64) = ac4[(m - 1) & 1][k];
        if constexpr (n == 1 && m < 2 && k == 4) { waitv<4>(); flag_st(rws, feo + (m + 2) * 32, tag, true, l); }
        if constexpr (n == 1 && m < 2 && k == 8) fs = flag_ld(rws, few + (m + 2) * 32, 32, l);
        SB();
      });
    });
    STAMP(21);
    asm volatile("s_nop 7");
    SB();
#pragma unroll
    for (int n = 0; n < 5; n++) *(LAS f32x4*)(rdst + RED_BUF + n * 64) = ac4[1][n];
    BAR(); red_ld(3, r);
    static_for<0, 16>([&](auto Qc) { fop(IC<3>{}, IC<Qc % 8>{}, IC<Qc / 8>{}); });
    ost(3);
    flag_st(rws, 0, tag, blockIdx.x == 0 && w == 0, l);
  }
  asm volatile(".Lk_end:");
}

extern "C" void kernel_launch(void* const* d_in, const int* in_sizes, int n_in, void* d_out, int out_size, void* d_ws, size_t ws_size, hipStream_t stream) {
  (void)hipFuncSetAttribute((const void*)k_fused, hipFuncAttributeMaxDynamicSharedMemorySize, 160 * 1024);
  k_fused<<<256, 256, 160 * 1024, stream>>>(d_in[0], d_in[1], d_in[2], d_in[4], d_in[5], d_in[6], d_in[7], d_out, (char*)d_ws);
}
```

```cpp
#include <hip/hip_runtime.h>
#include <hip/hip_bf16.h>
#include <stdint.h>
#include <type_traits>

#define LAS __attribute__((address_space(3)))
#define DI __device__ __forceinline__
typedef __attribute__((ext_vector_type(8))) __bf16 bf16x8;
typedef __attribute__((ext_vector_type(2))) __bf16 bf16x2;
typedef __attribute__((ext_vector_type(4))) float f32x4;
typedef __attribute__((ext_vector_type(2))) float f32x2;
typedef __attribute__((ext_vector_type(4))) unsigned v4u;
typedef __attribute__((ext_vector_type(2))) unsigned v2u;
typedef __attribute__((ext_vector_type(4))) short s16x4;
typedef __amdgpu_buffer_rsrc_t rsrc_t;

constexpr int S = 2048, D = 640, F = 1536;
constexpr int ACT = S * D * 2;
constexpr int EPOCH_OFF = 0;
constexpr int F0 = 1;
constexpr int FP = F0 + 256;
constexpr int FQ = FP + 256;
constexpr int FC = FQ + 256;
constexpr int FD = FC + 1024;
constexpr int FE = FD + 4096;
constexpr int NFLAG = FE + 4096;
constexpr int WQP_OFF = (NFLAG * 256 + 65535) / 65536 * 65536, OF_OFF = WQP_OFF + 32 * 80 * 1024, HF_OFF = OF_OFF + ACT;
constexpr int GF_OFF = HF_OFF + ACT, WOP_OFF = GF_OFF + S * F * 2, W1P_OFF = WOP_OFF + D * D * 2, W2P_OFF = W1P_OFF + D * F * 2;
constexpr int KVP_OFF = W2P_OFF + D * F * 2, KVF_OFF = KVP_OFF + 32 * 32 * 2048, WS_END = KVF_OFF + 32 * 2048;
constexpr int OOB = 0x7ffffff0;
constexpr int SC1 = 16;
constexpr unsigned ONES2 = 0x3f803f80u;

DI rsrc_t mkrs(const void* p, int bytes) { return __builtin_amdgcn_make_buffer_rsrc((void*)p, 0, bytes, 0x00020000); }
DI v4u mkrs4(const void* p, int bytes) { const uint64_t a = (uint64_t)p; return v4u{(unsigned)a, (unsigned)(a >> 32) & 0xffffu, (unsigned)bytes, 0x00020000u}; }
DI f32x4 mfma(v4u a, v4u b, f32x4 c) {
  return __builtin_amdgcn_mfma_f32_16x16x32_bf16(__builtin_bit_cast(bf16x8, a), __builtin_bit_cast(bf16x8, b), c, 0, 0, 0);
}
DI void mfma_t(v4u a, v4u b, f32x4& c) { asm volatile("v_mfma_f32_16x16x32_bf16 %0, %1, %2, %0" : "+v"(c) : "v"(a), "v"(b)); }
DI void mfma_z(v4u a, v4u b, f32x4& c) { asm volatile("v_mfma_f32_16x16x32_bf16 %0, %1, %2, 0" : "=&v"(c) : "v"(a), "v"(b)); }
template <bool FIRST, bool AG, bool BG = false> DI void mfma_w(v4u a, v4u b, f32x4& c) {
#define MF_(CA, CB) do { if constexpr (FIRST) asm volatile("v_mfma_f32_16x16x32_bf16 %0, %1, %2, 0" : "=&v"(c) : CA(a), CB(b)); \
                         else asm volatile("v_mfma_f32_16x16x32_bf16 %0, %1, %2, %0" : "+v"(c) : CA(a), CB(b)); } while (0)
  if constexpr (AG && BG) MF_("a", "a"); else if constexpr (AG) MF_("a", "v"); else if constexpr (BG) MF_("v", "a"); else MF_("v", "v");
#undef MF_
}
template <bool FIRST> DI void mfma_a(v4u a, v4u b, f32x4& c) { if constexpr (FIRST) mfma_z(a, b, c); else mfma_t(a, b, c); }
DI unsigned pk2(float a, float b) { f32x2 f = {a, b}; return __builtin_bit_cast(unsigned, __builtin_convertvector(f, bf16x2)); }
DI float bflo(unsigned u) { return __uint_as_float(u << 16); }
DI float bfhi(unsigned u) { return __uint_as_float(u & 0xffff0000u); }
DI v4u ldp(rsrc_t r, int voff, int soff) { return __builtin_amdgcn_raw_buffer_load_b128(r, voff, soff, 0); }
DI v4u lds1(rsrc_t r, int voff, int soff) { return __builtin_amdgcn_raw_buffer_load_b128(r, voff, soff, SC1); }
DI void sts1(v4u v, rsrc_t r, int voff, int soff) { asm volatile("" : "+v"(v)); __builtin_amdgcn_raw_buffer_store_b128(v, r, voff, soff, SC1); asm volatile("s_nop 1" :: "v"(v) : "memory"); }
DI v2u trread(LAS char* p) { return __builtin_bit_cast(v2u, __builtin_amdgcn_ds_read_tr16_b64_v4i16((LAS s16x4*)p)); }
#define WAITV(n) asm volatile("s_waitcnt vmcnt(" #n ")" ::: "memory")
#define BAR() do { asm volatile("s_waitcnt lgkmcnt(0)" ::: "memory"); __builtin_amdgcn_s_barrier(); asm volatile("" ::: "memory"); } while (0)
#define SB() __builtin_amdgcn_sched_barrier(0)
constexpr float LOG2E = 1.4426950408889634f;
DI float phi(float t) { return fmaxf(t, 0.f) + __builtin_amdgcn_fmed3f(__builtin_amdgcn_exp2f(t * LOG2E), 0.f, 1.f) + 1e-6f; }
DI f32x2 fma2(f32x2 a, f32x2 b, f32x2 c) { return __builtin_elementwise_fma(a, b, c); }
DI f32x2 lo2(f32x4 v) { return f32x2{v[0], v[1]}; }
DI f32x2 hi2(f32x4 v) { return f32x2{v[2], v[3]}; }
DI unsigned pk2v(f32x2 f) { return __builtin_bit_cast(unsigned, __builtin_convertvector(f, bf16x2)); }
DI float maxf(float a, float b) { float r; asm("v_max_f32 %0, %1, %2" : "=v"(r) : "v"(a), "v"(b)); return r; }
DI unsigned phi2(f32x2 u) {
  const f32x2 a = fma2(u, f32x2{LOG2E, LOG2E}, f32x2{-LOG2E, -LOG2E});
  const float e0 = __builtin_amdgcn_fmed3f(__builtin_amdgcn_exp2f(a[0]), 0.f, 1.f), e1 = __builtin_amdgcn_fmed3f(__builtin_amdgcn_exp2f(a[1]), 0.f, 1.f);
  return pk2v(f32x2{__builtin_amdgcn_fmed3f(u[0], e0, 3.0e38f), __builtin_amdgcn_fmed3f(u[1], e1, 3.0e38f)} + f32x2{1e-6f, 1e-6f});
}
DI float dot2(unsigned a, unsigned b, float c) { return __builtin_amdgcn_fdot2_f32_bf16(__builtin_bit_cast(bf16x2, a), __builtin_bit_cast(bf16x2, b), c, false); }
DI float gelu(float v) {
  const float u = v * (0.7978845608028654f * 2.f * LOG2E) * __builtin_fmaf(v * v, 0.044715f, 1.0f);
  return v * __builtin_amdgcn_rcpf(1.0f + __builtin_amdgcn_exp2f(-u));
}
template <class T> DI void pin(T& x) { asm volatile("" : "+v"(x)); }
template <class T> DI T P(T x) { pin(x); return x; }
template <int CTRL> DI float dppadd(float v) { return v + __uint_as_float(__builtin_amdgcn_update_dpp(0u, __float_as_uint(v), CTRL, 0xf, 0xf, true)); }
DI float sum16(float v) { v = dppadd<0xB1>(v); v = dppadd<0x4E>(v); v = dppadd<0x141>(v); return dppadd<0x140>(v); }
DI float add_x16(float v) { auto r = __builtin_amdgcn_permlane16_swap(__float_as_uint(v), __float_as_uint(v), false, false); return __uint_as_float(r[0]) + __uint_as_float(r[1]); }
DI float add_x32(float v) { auto r = __builtin_amdgcn_permlane32_swap(__float_as_uint(v), __float_as_uint(v), false, false); return __uint_as_float(r[0]) + __uint_as_float(r[1]); }

DI void glds4(v4u rs, unsigned dst, int soff, int v0, int v1, int v2, int v3) {
  asm volatile("s_nop 4\n\ts_mov_b32 m0, %1\n\ts_nop 0\n\tbuffer_load_dwordx4 %3, %0, %2 offen lds\n\t"
               "s_add_u32 m0, m0, 0x400\n\ts_nop 0\n\tbuffer_load_dwordx4 %4, %0, %2 offen lds\n\t"
               "s_add_u32 m0, m0, 0x400\n\ts_nop 0\n\tbuffer_load_dwordx4 %5, %0, %2 offen lds\n\t"
               "s_add_u32 m0, m0, 0x400\n\ts_nop 0\n\tbuffer_load_dwordx4 %6, %0, %2 offen lds"
               :: "s"(rs), "s"(dst), "s"(soff), "v"(v0), "v"(v1), "v"(v2), "v"(v3) : "memory", "scc");
}
template <int IMM> DI void glds1(v4u rs, unsigned base, int soff, int v) {
  asm volatile("s_add_u32 m0, %1, %4\n\ts_nop 0\n\tbuffer_load_dwordx4 %3, %0, %2 offen lds" :: "s"(rs), "s"(base), "s"(soff), "v"(v), "i"(IMM) : "memory", "scc");
}
template <int IMM> DI void glds1_sc1(v4u rs, unsigned base, int soff, int v) {
  asm volatile("s_add_u32 m0, %1, %4\n\ts_nop 0\n\tbuffer_load_dwordx4 %3, %0, %2 offen sc1 lds" :: "s"(rs), "s"(base), "s"(soff), "v"(v), "i"(IMM) : "memory", "scc");
}
template <int N> using IC = std::integral_constant<int, N>;
template <int N> DI void waitv() { asm volatile("s_waitcnt vmcnt(%0)" :: "i"(N) : "memory"); }
template <int I, int N, class Fn> DI void static_for(Fn&& f) { if constexpr (I < N) { f(std::integral_constant<int, I>{}); static_for<I + 1, N>(f); } }
#define PIN(x) pin(x)
#define STAMP(id)
#define STAMP_ARG
#define STAMP_PASS
#define CSTAMP(id)

#define SLP 0
DI unsigned flag_ld(rsrc_t rws, int f, int n, int l) { asm volatile("" ::: "memory"); return __builtin_amdgcn_raw_buffer_load_b32(rws, (l < n ? l : 0) * 256, f * 256, SC1); }
DI void flag_wait(unsigned v, rsrc_t rws, int f, int n, unsigned tag, int l) {
  if (!__all(v == tag)) {
#pragma unroll 1
    for (int sp = 0; sp < (1 << 16); sp++) { if (__all(flag_ld(rws, f, n, l) == tag)) break; }
  }
  asm volatile("" ::: "memory");
}
DI void wait_flags(rsrc_t rws, int f, int n, unsigned tag, int l) { flag_wait(flag_ld(rws, f, n, l), rws, f, n, tag, l); }
DI void flag_st(rsrc_t rws, int f, unsigned tag, bool me, int l) {
  asm volatile("" ::: "memory"); sts1(v4u{tag, tag, tag, tag}, rws, (me && l < 8) ? l * 16 : OOB, f * 256); asm volatile("" ::: "memory");
}

template <int MAT> DI int frag_off(int T, int ks) {
  if constexpr (MAT == 0) return WOP_OFF + ((T / 5) * 100 + ks * 5 + T % 5) * 1024;
  else if constexpr (MAT == 1) return W1P_OFF + ((T / 3) * 60 + ks * 3 + T % 3) * 1024;
  else return W2P_OFF + ((T / 5) * 240 + ks * 5 + T % 5) * 1024;
}
template <int MAT> DI void pack_issue(int ks, int cb, int l, v4u rs, unsigned stg) {
  const int r = l >> 3;
  static_for<0, 4>([&](auto Ic) { constexpr int i = Ic;
    const int sw = ((l & 7) ^ ((((r >> 1) & 1) | ((i & 1) << 1)) << 1)) * 16, sg = 4 * ks + i;
    if constexpr (MAT == 0) glds1<i * 1024>(rs, stg, sg * 8 * 1280 + cb * 128, r * 1280 + sw);
    else if constexpr (MAT == 1) glds1<i * 1024>(rs, stg, ((2 * i + ks / 10) * 80 + (ks % 10) * 8) * 3072 + cb * 128, r * 3072 + sw);
    else { const int q = sg % 6; glds1<i * 1024>(rs, stg, (48 * (sg / 6) + (q < 4 ? 4 * q : 8 * q)) * 1280 + cb * 128, (q < 4 ? 16 * (r >> 2) + (r & 3) : r) * 1280 + sw); }
  });
}
DI int unit_lane(int t, int l) { const int g = l >> 4, q = (l >> 2) & 3, p = l & 3, f = ((q >> 1) & 1) | ((g & 1) << 1); return (8 * g + q) * 128 + ((t ^ f) * 32) + p * 8; }
DI v4u unit_read(LAS char* p) { const v2u a = trread(p), b = trread(p + 512); return v4u{a.x, a.y, b.x, b.y}; }
template <int MAT> DI void pack_finish(int ks, int cb, int l, rsrc_t rws, LAS char* stg) {
  v4u f[4];
#pragma unroll
  for (int t = 0; t < 4; t++) f[t] = unit_read(stg + unit_lane(t, l));
#pragma unroll
  for (int t = 0; t < 4; t++) sts1(f[t], rws, l * 16, frag_off<MAT>(cb * 4 + t, ks));
}

DI void unit_issue(int u, int l, const void* wo, const void* w1, unsigned stg) {
  if (u < 200) pack_issue<0>(u / 10, u % 10, l, mkrs4(wo, D * D * 2), stg); else pack_issue<1>((u - 200) / 24, (u - 200) % 24, l, mkrs4(w1, D * F * 2), stg);
}
DI void unit_finish(int u, int l, rsrc_t rws, LAS char* stg) {
  if (u < 200) pack_finish<0>(u / 10, u % 10, l, rws, stg); else pack_finish<1>((u - 200) / 24, (u - 200) % 24, l, rws, stg);
}
constexpr int XT_OFF = 81920, XT_W = 10240, TAB_OFF = XT_OFF + 4 * XT_W, OB_ROW = 176;
constexpr int RED_ROW = 336, RED_W = 16 * RED_ROW, RED_BUF = 4 * RED_W, A3_0 = 2 * RED_BUF, W2L_OFF = 65536;
constexpr int NL2 = 24;

#define N1 48
#define N2 54
#define N3 60
#define N5 75
#define M2 0
#define X1 6
#define X2 8
#define XROT 3
#define B0 30
#define RK 10
#define DLY 4
#define FLK 4

__global__ __launch_bounds__(256, 1) void k_fused(const void* x, const void* wqkv, const void* wo, const void* w1, const void* b1p, const void* w2,
                                                  const void* b2p, void* out, char* ws) {
  extern __shared__ __attribute__((aligned(16))) char smem[];
  LAS char* L = (LAS char*)smem;
  const unsigned Lb = (unsigned)(size_t)L;
  STAMP(0);
  const int tid = threadIdx.x, l = tid & 63, w = __builtin_amdgcn_readfirstlane(tid >> 6), c = l & 15, g = l >> 4;
  const int xg = blockIdx.x & 7, i = blockIdx.x >> 3;
  const unsigned Lx = Lb + XT_OFF + w * XT_W;
  LAS char* xt = L + XT_OFF + w * XT_W;
  {
    uint64_t pc; unsigned len;
    asm volatile("s_getpc_b64 %0\n.Lk_here:\n\ts_mov_b32 %1, .Lk_end-.Lk_here\n\ts_nop 2" : "=s"(pc), "=s"(len));
    glds1<9216>(mkrs4((const void*)pc, len & ~15u), Lx, (i * 4 + w) * 640, l < 40 ? l * 16 : OOB);
  }
  const int grp = xg * 4 + (i >> 3), j = i & 7;
  const bool p0 = j < 5;
  const int pk = (i >> 3) * 5 + j;
  const v4u rx4 = mkrs4(x, S * D * 2), rws4 = mkrs4(ws, WS_END);
  const rsrc_t rws = mkrs(ws, WS_END);
  STAMP(19);
  const int ub = blockIdx.x * 3 + w - 1, uc = blockIdx.x * 3 + ((w - grp - 1) & 3);
  const bool hasb = w > 0 && ub < 680, hasc = uc < 480;
  if (p0) {
    const v4u rw4 = mkrs4(wqkv, D * 1920 * 2);
    const int rr = l / 30, e = l - rr * 30, sec = e / 10;
    const int v0 = l < 60 ? rr * 3840 + sec * 1280 + (e - sec * 10) * 16 : OOB, s0 = (32 * pk + 8 * w) * 3840 + xg * 160;
    static_for<0, 4>([&](auto Uc) { constexpr int u = Uc; glds1<u * 1024>(rw4, Lx, s0 + u * 2 * 3840, v0); });
  }
  glds1_sc1<8192>(rws4, Lx, 0, l == 0 ? EPOCH_OFF : (l == 1 ? (F0 + xg * 32 + pk) * 256 : (l == 2 ? (FP + blockIdx.x) * 256 : OOB)));
  {
    const char* tp = l < 8 ? (const char*)wo + l * 102400 : l < 24 ? (const char*)w1 + (l - 8) * 122880 : l < 40 ? (const char*)w2 + (l - 24) * 122880 : l < 56 ? (const char*)out + (l - 40) * 163840 : l < 60 ? (const char*)b1p : (const char*)b2p;
    asm volatile("s_add_u32 m0, %1, 9216\n\ts_nop 0\n\tglobal_load_lds_dword %0, off" :: "v"(tp), "s"(Lx) : "memory", "scc");
  }
  int va[2];
#pragma unroll
  for (int u = 0; u < 2; u++) va[u] = (grp * 64 + w * 16 + u * 8 + (l >> 3)) * 1280 + (((l & 7) ^ (g | (u << 2))) * 16);
  auto ldx = [&](auto Qc) { constexpr int q = Qc; int ln = (q >> 1) + (p0 ? 0 : (j - 5) * 3); ln = ln >= 10 ? ln - 10 : ln;
    glds1<(q & 1) * 1024>(rx4, Lb + w * 2048 + ln * 8192, ln * 128, va[q & 1]); };
  if (!p0) { static_for<0, 20>(ldx); waitv<20>(); } else waitv<0>();
  STAMP(20);
  const unsigned tag = __builtin_amdgcn_readfirstlane(*(LAS unsigned*)(xt + 8192)) + 1u;
  if (p0) {
    const int q = (l >> 2) & 3, p = l & 3;
    const int col = g == 0 ? 160 + 8 * p : g == 1 ? 320 + 8 * p : g == 2 ? (p == 0 ? 192 : p == 1 ? 352 : 32) : 8 * p;
    LAS char* tp = xt + (q >> 1) * 1024 + (q & 1) * 480 + col;
    v4u f[4];
#pragma unroll
    for (int t = 0; t < 4; t++) { const v2u a = trread(tp + 40 * t), b = trread(tp + 40 * t + 2048); f[t] = v4u{a.x, a.y, b.x, b.y}; }
#pragma unroll
    for (int t = 0; t < 4; t++) sts1(f[t], rws, g * 1024 + c * 16, WQP_OFF + ((xg * 4 + t) * 20 + pk) * 4096 + w * 256);
    STAMP(1);
    static_for<0, X2>(ldx);
    waitv<X2>(); BAR(); flag_st(rws, F0 + xg * 32 + pk, tag, w == 0, l);
    STAMP(2);
    static_for<X2, 20>(ldx);
  }
  LAS char* ap[2];
#pragma unroll
  for (int kk = 0; kk < 2; kk++) ap[kk] = L + c * 128 + (((4 * kk + g) ^ ((c >> 1) & 7)) * 16);
  auto rdx = [&](int ks, int m) { return *(LAS v4u*)(ap[ks & 1] + (ks >> 1) * 8192 + m * 2048); };
  auto ldf = [&](int base, int q) { return lds1(rws, l * 16 + (q & 3) * 1024, base + (q >> 2) * 4096); };
  const int hd = 4 * j + w;
  v4u bq[80];
  auto ldq = [&](auto Qc) { constexpr int q = Qc, f = q < 60 ? (q / 3) * 4 + 2 - q % 3 : (q - 60) * 4 + 3; bq[f] = ldf(WQP_OFF + hd * 81920, f); };
  STAMP(3);
  if (hasb) { unit_issue(ub, l, wo, w1, Lx); waitv<4>(); }
  else if (w == 0) wait_flags(rws, F0 + j * 32, 20, tag, l);
  else waitv<0>();
  BAR();
  STAMP(4);
  static_for<0, B0 - 20>(ldq); SB();
  {
    v4u a[20];
#pragma unroll
    for (int ks = 0; ks < 20; ks++) a[ks] = *(LAS v4u*)(ap[ks & 1] + (ks >> 1) * 8192 + w * 2048);
    SB();
    float s[2] = {0.f, 0.f}, q[2] = {0.f, 0.f};
    static_for<0, 20>([&](auto Kc) { constexpr int ks = Kc;
      ldq(IC<B0 - 20 + ks>{});
#pragma unroll
      for (int e = 0; e < 4; e++) { s[e & 1] = dot2(a[ks][e], ONES2, s[e & 1]); q[e & 1] = dot2(a[ks][e], a[ks][e], q[e & 1]); }
      SB(); });
    const float mean = add_x32(add_x16(s[0] + s[1])) * (1.f / D);
    const float rs = __builtin_amdgcn_rsqf(fmaxf(add_x32(add_x16(q[0] + q[1])) * (1.f / D) - mean * mean, 0.f) + 1e-5f);
    *(LAS float*)(L + TAB_OFF + (g & 1) * 256 + (w * 16 + c) * 4) = (g & 1) ? -mean * rs : rs;
  }
  if (hasb) { waitv<B0>(); unit_finish(ub, l, rws, xt); }
  v4u ones = {ONES2, ONES2, ONES2, ONES2};
  PIN(ones);
  v4u fa[2][4];
  unsigned fp = 0;
  v4u bw1[60], bwo[25], bw2[60];
  const unsigned L2w = Lb + W2L_OFF + w * (NL2 * 1024);
  const int wob = WOP_OFF + hd * 25600, w1b = W1P_OFF + hd * 61440, w2b = W2P_OFF + hd * 61440;
  auto bulk = [&](auto Qc) { constexpr int q = Qc;
    if constexpr (q >= 50 && q < 75) bwo[q - 50] = ldf(wob, q - 50); else { constexpr int r = q < 50 ? q : q - 25; bw1[r] = ldf(w1b, r); } };
  auto bulkl = [&](auto Qc) { constexpr int q = Qc; glds1_sc1<q * 1024>(rws4, L2w, w2b + (60 - NL2 + q) * 1024, l * 16); };
  auto ldw2 = [&](auto Qc) { constexpr int q = Qc; bw2[q] = ldf(w2b, q); };
#define W1F(r) bw1[r]
#define WOF(r) bwo[r]
  {
    f32x4 acc[4][3], cs[3], trs[4], tnm[4];
    LAS char* tabp = L + TAB_OFF + g * 16;
#pragma unroll
    for (int m = 0; m < 4; m++) fa[0][m] = rdx(0, m);
    SB();
    static_for<0, 20>([&](auto Kc) { constexpr int ks = Kc;
      if constexpr (ks == 5) { waitv<15>(); BAR(); flag_st(rws, FP + blockIdx.x, tag, w == 0, l); }
      if constexpr (ks == 16) { flag_wait(fp, rws, FP + w * 64, 64, tag, l); BAR(); }
      if constexpr (ks == 5) STAMP(23);
      static_for<0, 15>([&](auto Qc) { constexpr int Q = Qc, n = Q / 5, m = Q % 5;
        if constexpr (m < 4) mfma_w<ks == 0, false, true>(fa[ks & 1][m], bq[ks * 4 + n], acc[m][n]); else mfma_w<ks == 0, false, true>(ones, bq[ks * 4 + n], cs[n]);
        if constexpr (Q >= 1 && Q < 9 && (Q & 1)) fa[(ks + 1) & 1][3 - Q / 2] = rdx(ks < 19 ? ks + 1 : 0, 3 - Q / 2);
        if constexpr (m == 2 && B0 + ks * 3 + n < 80) ldq(IC<B0 + ks * 3 + n>{});
        if constexpr (m == 2 && B0 + ks * 3 + n >= 80) bulk(IC<B0 + ks * 3 + n - 80>{});
        if constexpr (ks == 12 && Q == 0) fp = flag_ld(rws, FP + w * 64, 64, l);
        if constexpr (ks >= 18 && Q >= 9 && Q < 13) (ks == 18 ? trs : tnm)[Q - 9] = *(LAS f32x4*)(tabp + (ks - 18) * 256 + (Q - 9) * 64);
        SB(); });
    });
    static_assert(B0 == 30, "ten requests of the list ride in the last steps");
    asm volatile("s_nop 7");
#pragma unroll
    for (int n = 0; n < 3; n++) pin(cs[n]);
    STAMP(5);
    v4u fk[2], fv[2], fr[2], fb[2];
    const f32x2 one2 = {1.f, 1.f}, mk = c < 8 ? one2 : f32x2{0.f, 0.f}, ck = c < 8 ? -one2 : one2;
    static_for<0, 8>([&](auto Ec) { constexpr int m = Ec / 2, hf = Ec % 2;
      const f32x2 rs = hf ? hi2(trs[m]) : lo2(trs[m]), nm = hf ? hi2(tnm[m]) : lo2(tnm[m]);
      const f32x2 uk = fma2(rs, hf ? hi2(acc[m][0]) : lo2(acc[m][0]), fma2(nm, lo2(cs[0]), one2));
      const f32x2 vv = fma2(rs, hf ? hi2(acc[m][1]) : lo2(acc[m][1]), nm * lo2(cs[1]));
      const f32x2 ur = fma2(rs, hf ? hi2(acc[m][2]) : lo2(acc[m][2]), fma2(nm, lo2(cs[2]), one2));
      constexpr int e = (m & 1) * 2 + hf;
      fk[m >> 1][e] = phi2(uk); fv[m >> 1][e] = pk2v(vv); fr[m >> 1][e] = phi2(ur); fb[m >> 1][e] = pk2v(fma2(ur, mk, ck));
      bulk(IC<10 + Ec>{});
      if constexpr (hf) SB();
    });
#pragma unroll
    for (int u = 0; u < 2; u++) { pin(fk[u]); pin(fv[u]); pin(fr[u]); pin(fb[u]); }
    asm volatile("s_nop 1");
    f32x4 t00, t01, t10, t11;
    mfma_z(fk[0], fv[0], t00); mfma_z(fk[0], fb[0], t01); mfma_z(fr[0], fv[0], t10); mfma_z(fr[0], fb[0], t11);
    mfma_t(fk[1], fv[1], t00); mfma_t(fk[1], fb[1], t01); mfma_t(fr[1], fv[1], t10); mfma_t(fr[1], fb[1], t11);
#pragma unroll
    for (int u = 0; u < 2; u++) *(LAS v4u*)(xt + u * 1024 + l * 16) = fr[u];
    asm volatile("s_nop 5");
    SB();
    const int kvo = KVP_OFF + (hd * 32 + grp) * 2048;
    const bool mid = c >= 4 && c < 12;
    sts1(v4u{pk2(t00[0], t00[1]), tag, pk2(t00[2], t00[3]), tag}, rws, l * 16, kvo);
    sts1(v4u{pk2(t01[0], t01[1]), tag, pk2(t01[2], t01[3]), tag}, rws, mid ? (12 + g * 8 + c) * 16 : OOB, kvo + 1024);
    sts1(v4u{pk2(t10[0], t10[1]), tag, pk2(t10[2], t10[3]), tag}, rws, g == 0 ? c * 16 : OOB, kvo + 1024);
    sts1(v4u{pk2(t11[0], t11[1]), tag, pk2(t11[2], t11[3]), tag}, rws, (g == 0 && mid) ? (44 + c) * 16 : OOB, kvo + 1024);
    STAMP(6);
  }
  if (w != (grp & 3)) {
    const int f = l < 16 ? FD + grp * 128 + (l >> 2) * 32 + j * 4 + (l & 3) : l < 32 ? FE + grp * 128 + ((l - 16) >> 2) * 32 + j * 4 + (l & 3) : FC + grp * 32 + (l & 3) * 8 + j;
    if (hasc) pack_issue<2>(uc / 10, uc % 10, l, mkrs4(w2, D * F * 2), Lx + 4096);
    if (w == ((grp + 1) & 3)) glds1<9216>(rws4, Lx, 0, l < 36 ? f * 256 : (l == 36 ? (FQ + blockIdx.x) * 256 : OOB));
  }
  const bool act = c < 10;
  v4u hk[4];
  {
    f32x4 acc[4], cs;
    float qrs[4], qnm[4];
    v2u q1[4];
    static_for<0, 20>([&](auto Kc) { constexpr int ks = Kc;
      if constexpr (ks == RK) {
        STAMP(7);
        if (w == (grp & 3)) {
          v4u pp[8];
          const int pv = g * 2048 + ((xg == 7 && c >= 8) ? 7 : c) * 16;
#pragma unroll 1
          for (int sp = 0; sp < (1 << 16); sp++) {
            asm volatile("" ::: "memory");
#pragma unroll
            for (int q = 0; q < 8; q++) pp[q] = lds1(rws, pv, KVP_OFF + (hd * 32 + 4 * q) * 2048 + xg * 256);
            SB();
            unsigned bad = 0;
#pragma unroll
            for (int q = 0; q < 8; q++) bad |= (pp[q].y ^ tag) | (pp[q].w ^ tag);
            if (__all(bad == 0)) break;
          }
          float sm[4] = {0.f, 0.f, 0.f, 0.f};
#pragma unroll
          for (int q = 0; q < 8; q++) { sm[0] += bflo(pp[q].x); sm[1] += bfhi(pp[q].x); sm[2] += bflo(pp[q].z); sm[3] += bfhi(pp[q].z); }
#pragma unroll
          for (int e = 0; e < 4; e++) sm[e] = add_x32(add_x16(sm[e]));
          sts1(v4u{pk2(sm[0], sm[1]), tag, pk2(sm[2], sm[3]), tag}, rws, g == 0 ? c * 16 : OOB, KVF_OFF + hd * 2048 + xg * 256);
        }
        SB();
        STAMP(8);
      }
      static_for<0, 5>([&](auto Qc) { constexpr int m = Qc;
        if constexpr (m < 4) mfma_a<ks == 0>(bq[ks * 4 + 3], fa[ks & 1][m], acc[m]); else mfma_a<ks == 0>(bq[ks * 4 + 3], ones, cs);
        if constexpr (ks < 19 && m < 4) fa[(ks + 1) & 1][3 - m] = rdx(ks + 1, 3 - m);
        if constexpr (m == 4 || (m == 1 && (ks & 1))) bulk(IC<18 + (ks / 2) * 3 + (ks & 1) + (m == 4 && (ks & 1))>{});
        SB(); });
    });
    {
      const int sg = 10 * j + (act ? c : 0), row = 4 * w + g;
      LAS char* hp = L + (sg >> 3) * 8192 + row * 128 + (((sg & 7) ^ ((row >> 1) & 7)) * 16);
      LAS char* qp = xt + (l & 3) * 256 + (12 + ((l >> 2) & 3)) * 16;
#pragma unroll
      for (int m = 0; m < 4; m++) {
        qrs[m] = *(LAS float*)(L + TAB_OFF + c * 4 + m * 64); qnm[m] = *(LAS float*)(L + TAB_OFF + c * 4 + 256 + m * 64);
        q1[m] = trread(qp + (m >> 1) * 1024 + (m & 1) * 8);
        hk[m] = *(LAS v4u*)(hp + m * 2048);
      }
    }
    asm volatile("s_nop 7");
    SB();
    STAMP(9);
    v4u ka0, ka1, kb0, kb1;
    const int c4 = c < 4 ? c : 4;
    auto kv_ld = [&]() { asm volatile("" ::: "memory");
      ka0 = lds1(rws, l * 16, KVF_OFF + hd * 2048); ka1 = lds1(rws, 1024 + c * 16, KVF_OFF + hd * 2048);
      kb0 = lds1(rws, 1024 + (16 + g * 8 + c4) * 16, KVF_OFF + hd * 2048); kb1 = lds1(rws, 1024 + (48 + c4) * 16, KVF_OFF + hd * 2048); };
    auto kv_ok = [&]() { return __all(((ka0.y ^ tag) | (ka0.w ^ tag) | (ka1.y ^ tag) | (ka1.w ^ tag) | (kb0.y ^ tag) | (kb0.w ^ tag) | (kb1.y ^ tag) | (kb1.w ^ tag)) == 0); };
    kv_ld();
    static_for<N1, N2>(bulk); SB();
    v4u pq[4];
#pragma unroll
    for (int m = 0; m < 4; m++) {
      const f32x2 rs = {qrs[m], qrs[m]}, nm = {qnm[m], qnm[m]}, one2 = {1.f, 1.f};
      pq[m] = v4u{phi2(fma2(rs, lo2(acc[m]), fma2(nm, lo2(cs), one2))), phi2(fma2(rs, hi2(acc[m]), fma2(nm, hi2(cs), one2))), q1[m].x, q1[m].y};
      pin(pq[m]);
    }
    BAR();
    STAMP(10);
    if (!kv_ok()) {
#pragma unroll 1
      for (int sp = 0; sp < (1 << 16); sp++) { kv_ld(); if (kv_ok()) break; }
    }
    STAMP(24);
    v4u kf0 = {ka0.x, ka0.z, g == 3 ? ka1.x : 0u, g == 3 ? ka1.z : 0u}, kf1 = {kb0.x, kb0.z, g == 3 ? kb1.x : 0u, g == 3 ? kb1.z : 0u};
    pin(kf0); pin(kf1);
    asm volatile("s_nop 1");
    f32x4 n0[4], n1[4];
    LAS char* ob0 = L + c * OB_ROW + w * 40 + g * 8;
    LAS char* ob1 = L + c * OB_ROW + (g == 0 ? w * 40 + 32 : 160);
#pragma unroll
    for (int m = 0; m < 4; m++) { mfma_z(kf0, pq[m], n0[m]); mfma_z(kf1, pq[m], n1[m]); }
    asm volatile("s_nop 7");
    SB();
#pragma unroll
    for (int m = 0; m < 4; m++) {
      auto sw = __builtin_amdgcn_permlane16_swap(__float_as_uint(n1[m][0]), __float_as_uint(n1[m][0]), false, false);
      const float r = __builtin_amdgcn_rcpf(__uint_as_float(sw[1]) + 1e-6f);
      const f32x2 r2 = {r, r};
      *(LAS v2u*)(ob0 + m * 16 * OB_ROW) = v2u{pk2v(lo2(n0[m]) * r2), pk2v(hi2(n0[m]) * r2)};
      *(LAS v2u*)(ob1 + m * 16 * OB_ROW) = v2u{pk2v(lo2(n1[m]) * r2), pk2v(hi2(n1[m]) * r2)};
    }
    BAR();
    v4u o[3];
#pragma unroll
    for (int q = 0; q < 3; q++) o[q] = *(LAS v4u*)(L + (w * 16 + c) * OB_ROW + (q * 4 + (q == 2 ? g & 1 : g)) * 16);
#pragma unroll
    for (int q = 0; q < 3; q++) sts1(o[q], rws, (q < 2 || g < 2) ? l * 16 + q * 1024 : OOB, OF_OFF + (grp * 4 + w) * 20480 + j * 2560);
    STAMP(25);
    SB();
    static_for<N2, N3>(bulk); SB();
    waitv<N3 - N2>();
    flag_st(rws, FC + grp * 32 + w * 8 + j, tag, true, l);
    if (hasc && w != (grp & 3)) pack_finish<2>(uc / 10, uc % 10, l, rws, xt + 4096);
    STAMP(11);
  }
  LAS char* rdst = L + w * RED_W + c * RED_ROW + g * 16;
  LAS char* rsrc = L + (4 * w + g) * RED_ROW + (act ? c : 9) * 32;
  const int rowo = act ? (grp * 64 + 4 * w + g) * 1280 + j * 160 + c * 16 : OOB;
  auto red_ld = [&](int m, f32x4 (&r)[8]) {
#pragma unroll
    for (int ww = 0; ww < 4; ww++) { LAS f32x4* p = (LAS f32x4*)(rsrc + (m & 1) * RED_BUF + ww * RED_W); r[2 * ww] = p[0]; r[2 * ww + 1] = p[1]; }
  };
  v4u hst[2], hs[2][2];
  const int hvo = act ? (4 * w + g) * 160 + ((c ^ (w >> 1)) * 16) : OOB;
  auto h_flag = [&](int m) { asm volatile("" ::: "memory"); sts1(hst[m & 1], rws, c < 2 ? (c * 4 + g) * 16 : OOB, (FD + grp * 128 + m * 32 + j * 4 + w) * 256); asm volatile("" ::: "memory"); };
  const int hsv = (g * 8 + (c >> 2)) * 256 + (c & 3) * 16;
  auto h_poll = [&](int m) { asm volatile("" ::: "memory");
    hs[m & 1][0] = lds1(rws, hsv, (FD + grp * 128 + m * 32) * 256); hs[m & 1][1] = lds1(rws, hsv + 1024, (FD + grp * 128 + m * 32) * 256); };
  auto h_ok = [&](int m) { return __all(hs[m & 1][0].y == tag && hs[m & 1][0].w == tag && hs[m & 1][1].y == tag && hs[m & 1][1].w == tag); };
  auto h_wait = [&](int m) {
    if (!h_ok(m)) {
#pragma unroll 1
      for (int sp = 0; sp < (1 << 16); sp++) { h_poll(m); if (h_ok(m)) break; }
    }
    asm volatile("" ::: "memory");
  };
  v2u bias[3];
  const unsigned La = Lb + w * 5120;
  const int hso = HF_OFF + grp * 81920 + w * 5120;
  auto ldh = [&](auto Mc, auto Qc) { constexpr int m = Mc, q = Qc; glds1_sc1<((m & 1) ? 0 : A3_0) + q * 1024>(rws4, La, hso + m * 20480 + q * 1024, l * 16); };
  {
    const rsrc_t rb1 = mkrs(b1p, F * 2);
#pragma unroll
    for (int n = 0; n < 3; n++) bias[n] = __builtin_amdgcn_raw_buffer_load_b64(rb1, ((12 * j + 3 * w + n) * 16 + 4 * g) * 2, 0, 0);
    static_for<N3, N5>(bulk); SB();
    wait_flags(rws, FC + grp * 32, 32, tag, l);
    BAR();
    flag_st(rws, FQ + blockIdx.x, tag, w == 0, l);
    STAMP(12);
    v4u a[20];
    auto lda = [&](auto Qc) { constexpr int q = Qc; a[q] = ldf(OF_OFF + ((grp * 4 + q / 5) * 20 + w * 5) * 1024, q % 5); };
    static_for<0, 15>(lda);
    SB();
    f32x4 acc[5], r[8];
    auto fin = [&](int m) {
      const f32x4 s0 = (r[0] + r[2]) + (r[4] + r[6]), s1 = (r[1] + r[3]) + (r[5] + r[7]);
      v4u o;
      o[0] = pk2(s0[0] + bflo(hk[m][0]), s0[1] + bfhi(hk[m][0])); o[1] = pk2(s0[2] + bflo(hk[m][1]), s0[3] + bfhi(hk[m][1]));
      o[2] = pk2(s1[0] + bflo(hk[m][2]), s1[1] + bfhi(hk[m][2])); o[3] = pk2(s1[2] + bflo(hk[m][3]), s1[3] + bfhi(hk[m][3]));
      hk[m] = o;
      sts1(o, rws, hvo, HF_OFF + (grp * 4 + m) * 20480 + j * 2560);
      float s = 0.f, q = 0.f;
#pragma unroll
      for (int e = 0; e < 4; e++) { s = dot2(o[e], ONES2, s); q = dot2(o[e], o[e], q); }
      hst[m & 1] = v4u{__float_as_uint(sum16(act ? s : 0.f)), tag, __float_as_uint(sum16(act ? q : 0.f)), tag};
    };
    auto mmt = [&](auto Mc) { constexpr int m = Mc;
      static_for<0, 5>([&](auto Kc) { constexpr int k = Kc;
        static_for<0, 5>([&](auto Nc) { constexpr int n = Nc; mfma_w<k == 0, false>(WOF(k * 5 + n), a[m * 5 + k], acc[n]); });
        if constexpr (m == 0) lda(IC<15 + k>{}); else { if constexpr (m < 3) bulk(IC<N5 + (m - 1) * 5 + k>{}); else ldw2(Kc); }
        SB(); });
    };
    auto wrt = [&](int m) {
      asm volatile("s_nop 3");
#pragma unroll
      for (int n = 0; n < 5; n++) *(LAS f32x4*)(rdst + (m & 1) * RED_BUF + n * 64) = acc[n];
      BAR(); red_ld(m, r); SB();
    };
    mmt(IC<0>{}); wrt(0); fin(0); SB();
    pin(a[19]); SB();
    STAMP(13);
    mmt(IC<1>{}); wrt(1);
    waitv<5>(); h_flag(0);
    const unsigned fq = flag_ld(rws, FQ + w * 40, 40, l);
    mmt(IC<2>{}); fin(1); SB();
    h_poll(0);
    flag_wait(fq, rws, FQ + w * 40, 40, tag, l);
    wrt(2);
    mmt(IC<3>{});
    waitv<7>(); h_flag(1);
    h_wait(0);
    STAMP(16);
    static_for<0, 5>([&](auto Q) { ldh(IC<0>{}, Q); });
    fin(2); SB(); wrt(3);
    fin(3); SB();
    STAMP(14);
    waitv<1>(); h_flag(2);
    STAMP(15);
  }
  {
    const int few = FE + grp * 128, feo = few + j * 4 + w;
    h_poll(1);
    unsigned fs = 0;
    SB();
    f32x4 cs[3];
    f32x2 bf[6];
#pragma unroll
    for (int e = 0; e < 6; e++) bf[e] = P(f32x2{bflo(bias[e >> 1][e & 1]), bfhi(bias[e >> 1][e & 1])});
    SB();
    BAR();
    STAMP(17);
    LAS char* ab[2];
#pragma unroll
    for (int kk = 0; kk < 2; kk++) ab[kk] = L + g * 5120 + c * 160 + ((kk ^ (c >> 3)) * 16);
    auto rda = [&](int t) { const int k = t % 20; return *(LAS v4u*)(ab[k & 1] + (((t / 20) & 1) ? 0 : A3_0) + (k / 10) * 2560 + ((k % 10) >> 1) * 32); };
    constexpr int RD = 4;
    v4u af[RD];
    f32x4 acc[3];
    float rs = 0.f, nr = 0.f;
    auto stats = [&](int m) {
      const f32x4 p0 = __builtin_bit_cast(f32x4, hs[m & 1][0]), p1 = __builtin_bit_cast(f32x4, hs[m & 1][1]);
      auto x1 = __builtin_amdgcn_permlane16_swap(__float_as_uint(P(p0[0] + p1[0])), __float_as_uint(P(p0[2] + p1[2])), false, false);
      const float u = add_x32(__uint_as_float(x1[0]) + __uint_as_float(x1[1]));
      auto x2 = __builtin_amdgcn_permlane16_swap(__float_as_uint(u), __float_as_uint(u), false, false);
      const float mean = P(__uint_as_float(x2[0]) * (1.f / D));
      const float var = fmaxf(__uint_as_float(x2[1]) * (1.f / D) - mean * mean, 0.f);
      const float r1 = __builtin_amdgcn_rsqf(var + 1e-5f);
      rs = P(r1 * __builtin_amdgcn_rsqf(var * r1 * r1 + 1e-5f));
      nr = P(-mean * rs);
    };
    auto epi = [&](int m) {
      asm volatile("s_nop 7");
      SB();
      constexpr float C0 = -0.7978845608028654f * 2.f * LOG2E, C1 = C0 * 0.044715f;
      const f32x2 rs2 = {rs, rs}, nr2 = {nr, nr};
      unsigned gp[6];
#pragma unroll
      for (int e = 0; e < 6; e++) {
        const f32x2 v = fma2(rs2, (e & 1) ? hi2(acc[e >> 1]) : lo2(acc[e >> 1]), fma2(nr2, (e & 1) ? hi2(cs[e >> 1]) : lo2(cs[e >> 1]), bf[e]));
        const f32x2 u = v * fma2(v * v, f32x2{C1, C1}, f32x2{C0, C0});
        const f32x2 d = f32x2{__builtin_amdgcn_exp2f(u[0]), __builtin_amdgcn_exp2f(u[1])} + f32x2{1.f, 1.f};
        gp[e] = pk2v(v * f32x2{__builtin_amdgcn_rcpf(d[0]), __builtin_amdgcn_rcpf(d[1])});
      }
      const int base = GF_OFF + (grp * 4 + m) * 49152 + 6 * hd * 256;
      sts1(v4u{gp[0], gp[1], gp[2], gp[3]}, rws, l * 16, base);
      auto s0 = __builtin_amdgcn_permlane16_swap(gp[4], gp[4], false, false);
      auto s1 = __builtin_amdgcn_permlane16_swap(gp[5], gp[5], false, false);
      sts1(v4u{s0[0], s1[0], s0[1], s1[1]}, rws, (g & 1) ? OOB : (g >> 1) * 256 + c * 16, base + 1024);
      SB();
    };
    v4u a4[12];
    auto lda4 = [&](int q) { return ldf(GF_OFF + ((grp * 4 + q / 12) * 48 + w * 12) * 1024, q % 12); };
    const rsrc_t ro = mkrs(out, S * D * 2), rb2 = mkrs(b2p, D * 2);
    v4u br;
#pragma unroll
    for (int q = 0; q < RD; q++) af[q] = rda(q);
    SB();
    asm volatile("s_nop 1");
    auto rider = [&](auto Qc) { constexpr int q = Qc; if constexpr (q < 31) ldw2(IC<5 + q>{}); else if constexpr (q < 55) bulkl(IC<q - 31>{}); };
    static_for<0, 80>([&](auto Tc) {
      constexpr int T = Tc, m = T / 20, k = T % 20;
      if constexpr (k == 16 && m < 3) { waitv<(m == 0 ? 18 : m == 1 ? 14 : 9)>(); BAR(); }
      if constexpr (k == 0) STAMP(26 + m);
      if constexpr (T == 2) { waitv<5>(); h_flag(3); }
      if constexpr (T == 3) h_wait(1);
      if constexpr (k == 0 && (m == 1 || m == 2)) h_wait(m + 1);
      if constexpr (T == 66) flag_wait(fs, rws, few, 64, tag, l);
      const v4u cur = af[T % RD];
      static_for<0, 3>([&](auto Nc) {
        constexpr int n = Nc;
        mfma_w<k == 0, true>(W1F(k * 3 + n), cur, acc[n]);
        if constexpr (m == 0) mfma_w<k == 0, true>(W1F(k * 3 + n), ones, cs[n]);
        if constexpr (n == 2 && (T & 1) && T + RD < 80) { af[T % RD] = rda(T + RD); SB(); af[(T - 1) % RD] = rda(T + RD - 1); }
        if constexpr (m == 0) {
          if constexpr (n == 0 && k >= 3 && k < 8) ldh(IC<1>{}, IC<k - 3>{});
          if constexpr (n == 1) rider(IC<k>{});
          if constexpr (n == 0 && k == 8) h_poll(2);
          if constexpr (n == 0 && k >= 9) rider(IC<20 + k - 9>{});
        } else if constexpr (n == 0) {
          if constexpr (m < 3 && k < 5) ldh(IC<m + 1>{}, IC<k>{});
          if constexpr (m < 3 && k >= 5) rider(IC<31 + (m - 1) * 15 + k - 5>{});
          if constexpr (T == 60) { br = ldp(rb2, act ? j * 160 + c * 16 : OOB, 0); fs = flag_ld(rws, few, 64, l); }
          if constexpr (T >= 66 && T < 78) a4[T - 66] = lda4(T - 66);
        }
        if constexpr (m == 1 && n == 1 && k == 8) h_poll(3);
        if constexpr (m >= 1 && n == 1 && k == 9) { waitv<(m == 1 ? 12 : m == 2 ? 10 : 6)>(); flag_st(rws, feo + (m - 1) * 32, tag, true, l); }
        if constexpr (k == 6 && n == 1) stats(m);
        SB();
      });
      if constexpr (k == 19) epi(m);
    });
    STAMP(18);
    f32x4 ac4[2][5], r[8];
    float b2f[8], ov[8], ow[2];
#pragma unroll
    for (int e = 0; e < 8; e++) b2f[e] = (e & 1) ? bfhi(br[e >> 1]) : bflo(br[e >> 1]);
    auto fop = [&](auto Mc, auto Pc, auto Sc) { constexpr int m = Mc, st = Sc;
      static_for<0, 2>([&](auto Zc) { constexpr int z = Zc, e = 2 * Pc + z;
        if constexpr (st == 0) ov[e] = P(r[e >> 2][e & 3] + r[2 + (e >> 2)][e & 3]);
        if constexpr (st == 1) ow[z] = P(r[4 + (e >> 2)][e & 3] + r[6 + (e >> 2)][e & 3]);
        if constexpr (st == 2) ov[e] = P(ov[e] + ow[z]);
        if constexpr (st == 3) ow[z] = P(z ? bfhi(hk[m][e >> 1]) : bflo(hk[m][e >> 1]));
        if constexpr (st == 4) ow[z] = P(ow[z] + b2f[e]);
        if constexpr (st == 5) ov[e] = P(ov[e] + ow[z]);
        SB(); });
    };
    auto ost = [&](int m) { sts1(v4u{pk2(ov[0], ov[1]), pk2(ov[2], ov[3]), pk2(ov[4], ov[5]), pk2(ov[6], ov[7])}, ro, rowo, m * 20480); };
    SB();
    static_for<0, 48>([&](auto Tc) {
      constexpr int T = Tc, m = T / 12, k = T % 12;
      if constexpr (k == 0) STAMP(34 + 2 * m);
      if constexpr (k == 0 && (m == 1 || m == 2)) flag_wait(fs, rws, few + (m + 1) * 32, 32, tag, l);
      if constexpr (k == 4) BAR();
      const v4u cur = a4[k];
      static_for<0, 5>([&](auto Nc) { constexpr int n = Nc, G = k * 5 + n;
        mfma_w<k == 0, (G >= 60 - NL2 || G < 3), true>(bw2[G], cur, ac4[m & 1][n]);
        if constexpr (m == 0 && G < NL2) bw2[60 - NL2 + G] = *(LAS v4u*)(L + W2L_OFF + w * (NL2 * 1024) + G * 1024 + l * 16);
        if constexpr (m > 0 && G >= 3 && G < 18 && G % 3 == 0) *(LAS f32x4*)(rdst + ((m - 1) & 1) * RED_BUF + (G / 3 - 1) * 64) = ac4[(m - 1) & 1][G / 3 - 1];
        if constexpr (m > 0 && G >= 20 && G < 28) { constexpr int q = ((G - 20) & 3) * 2 + ((G - 20) >> 2); r[q] = ((LAS f32x4*)(rsrc + ((m - 1) & 1) * RED_BUF + (q >> 1) * RED_W))[q & 1]; }
        if constexpr (m > 0 && G >= 32 && G < 56) fop(IC<m - 1>{}, IC<(G - 32) / 6>{}, IC<(G - 32) % 6>{});
        if constexpr (m > 0 && G == 57) ost(m - 1);
        if constexpr (m < 3 && n == 4) a4[k] = lda4(m * 12 + k + 12);
        if constexpr (n == 2 && T == 6) { waitv<6>(); flag_st(rws, feo + 96, tag, true, l); }
        if constexpr (n == 2 && m < 2 && k == 8) fs = flag_ld(rws, few + (m + 2) * 32, 32, l);
        SB();
      });
    });
    STAMP(21);
    asm volatile("s_nop 7");
    SB();
#pragma unroll
    for (int n = 0; n < 5; n++) *(LAS f32x4*)(rdst + RED_BUF + n * 64) = ac4[1][n];
    BAR(); red_ld(3, r);
#pragma unroll
    for (int e = 0; e < 8; e++) ov[e] = ((r[e >> 2][e & 3] + r[2 + (e >> 2)][e & 3]) + (r[4 + (e >> 2)][e & 3] + r[6 + (e >> 2)][e & 3])) + (((e & 1) ? bfhi(hk[3][e >> 1]) : bflo(hk[3][e >> 1])) + b2f[e]);
    ost(3);
    flag_st(rws, 0, tag, blockIdx.x == 0 && w == 0, l);
  }
  { WAITV(0); const unsigned long long te = __builtin_amdgcn_s_memrealtime(); while (__builtin_amdgcn_s_memrealtime() - te < 0ull) __builtin_amdgcn_s_sleep(2); }
  asm volatile(".Lk_end:");
}

extern "C" void kernel_launch(void* const* d_in, const int* in_sizes, int n_in, void* d_out, int out_size, void* d_ws, size_t ws_size, hipStream_t stream) {
  (void)hipFuncSetAttribute((const void*)k_fused, hipFuncAttributeMaxDynamicSharedMemorySize, 160 * 1024);
  k_fused<<<256, 256, 160 * 1024, stream>>>(d_in[0], d_in[1], d_in[2], d_in[4], d_in[5], d_in[6], d_in[7], d_out, (char*)d_ws);
}
```

```cpp
#include <hip/hip_runtime.h>
#include <hip/hip_bf16.h>
#include <stdint.h>
#include <type_traits>

#define LAS __attribute__((address_space(3)))
#define DI __device__ __forceinline__
typedef __attribute__((ext_vector_type(8))) __bf16 bf16x8;
typedef __attribute__((ext_vector_type(2))) __bf16 bf16x2;
typedef __attribute__((ext_vector_type(4))) float f32x4;
typedef __attribute__((ext_vector_type(2))) float f32x2;
typedef __attribute__((ext_vector_type(4))) unsigned v4u;
typedef __attribute__((ext_vector_type(2))) unsigned v2u;
typedef __attribute__((ext_vector_type(4))) short s16x4;
typedef __amdgpu_buffer_rsrc_t rsrc_t;

constexpr int S = 2048, D = 640, F = 1536;
constexpr int ACT = S * D * 2;
constexpr int EPOCH_OFF = 0;
constexpr int F0 = 1;
constexpr int FP = F0 + 256;
constexpr int FC = FP + 256;
constexpr int FD = FC + 1024;
constexpr int FE = FD + 1024;
constexpr int NFLAG = FE + 4096;
constexpr int WQP_OFF = (NFLAG * 256 + 65535) / 65536 * 65536, OF_OFF = WQP_OFF + 32 * 80 * 1024, HF_OFF = OF_OFF + ACT;
constexpr int GF_OFF = HF_OFF + ACT, WOP_OFF = GF_OFF + S * F * 2, W1P_OFF = WOP_OFF + D * D * 2, W2P_OFF = W1P_OFF + D * F * 2;
constexpr int KVP_OFF = W2P_OFF + D * F * 2, KVF_OFF = KVP_OFF + 32 * 32 * 2048, WS_END = KVF_OFF + 32 * 2048;
constexpr int OOB = 0x7ffffff0;
constexpr int SC1 = 16;
constexpr unsigned ONES2 = 0x3f803f80u;

DI rsrc_t mkrs(const void* p, int bytes) { return __builtin_amdgcn_make_buffer_rsrc((void*)p, 0, bytes, 0x00020000); }
DI v4u mkrs4(const void* p, int bytes) { const uint64_t a = (uint64_t)p; return v4u{(unsigned)a, (unsigned)(a >> 32) & 0xffffu, (unsigned)bytes, 0x00020000u}; }
DI f32x4 mfma(v4u a, v4u b, f32x4 c) {
  return __builtin_amdgcn_mfma_f32_16x16x32_bf16(__builtin_bit_cast(bf16x8, a), __builtin_bit_cast(bf16x8, b), c, 0, 0, 0);
}
DI void mfma_t(v4u a, v4u b, f32x4& c) { asm volatile("v_mfma_f32_16x16x32_bf16 %0, %1, %2, %0" : "+v"(c) : "v"(a), "v"(b)); }
DI void mfma_z(v4u a, v4u b, f32x4& c) { asm volatile("v_mfma_f32_16x16x32_bf16 %0, %1, %2, 0" : "=&v"(c) : "v"(a), "v"(b)); }
template <bool FIRST, bool AG, bool BG = false> DI void mfma_w(v4u a, v4u b, f32x4& c) {
#define MF_(CA, CB) do { if constexpr (FIRST) asm volatile("v_mfma_f32_16x16x32_bf16 %0, %1, %2, 0" : "=&v"(c) : CA(a), CB(b)); \
                         else asm volatile("v_mfma_f32_16x16x32_bf16 %0, %1, %2, %0" : "+v"(c) : CA(a), CB(b)); } while (0)
  if constexpr (AG && BG) MF_("a", "a"); else if constexpr (AG) MF_("a", "v"); else if constexpr (BG) MF_("v", "a"); else MF_("v", "v");
#undef MF_
}
template <bool FIRST> DI void mfma_a(v4u a, v4u b, f32x4& c) { if constexpr (FIRST) mfma_z(a, b, c); else mfma_t(a, b, c); }
DI unsigned pk2(float a, float b) { f32x2 f = {a, b}; return __builtin_bit_cast(unsigned, __builtin_convertvector(f, bf16x2)); }
DI float bflo(unsigned u) { return __uint_as_float(u << 16); }
DI float bfhi(unsigned u) { return __uint_as_float(u & 0xffff0000u); }
DI v4u ldp(rsrc_t r, int voff, int soff) { return __builtin_amdgcn_raw_buffer_load_b128(r, voff, soff, 0); }
DI v4u lds1(rsrc_t r, int voff, int soff) { return __builtin_amdgcn_raw_buffer_load_b128(r, voff, soff, SC1); }
DI void sts1(v4u v, rsrc_t r, int voff, int soff) { asm volatile("" : "+v"(v)); __builtin_amdgcn_raw_buffer_store_b128(v, r, voff, soff, SC1); asm volatile("s_nop 1" :: "v"(v) : "memory"); }
DI v2u trread(LAS char* p) { return __builtin_bit_cast(v2u, __builtin_amdgcn_ds_read_tr16_b64_v4i16((LAS s16x4*)p)); }
#define WAITV(n) asm volatile("s_waitcnt vmcnt(" #n ")" ::: "memory")
#define BAR() do { asm volatile("s_waitcnt lgkmcnt(0)" ::: "memory"); __builtin_amdgcn_s_barrier(); asm volatile("" ::: "memory"); } while (0)
#define SB() __builtin_amdgcn_sched_barrier(0)
constexpr float LOG2E = 1.4426950408889634f;
DI float phi(float t) { return fmaxf(t, 0.f) + __builtin_amdgcn_fmed3f(__builtin_amdgcn_exp2f(t * LOG2E), 0.f, 1.f) + 1e-6f; }
DI f32x2 fma2(f32x2 a, f32x2 b, f32x2 c) { return __builtin_elementwise_fma(a, b, c); }
DI f32x2 lo2(f32x4 v) { return f32x2{v[0], v[1]}; }
DI f32x2 hi2(f32x4 v) { return f32x2{v[2], v[3]}; }
DI unsigned pk2v(f32x2 f) { return __builtin_bit_cast(unsigned, __builtin_convertvector(f, bf16x2)); }
DI float maxf(float a, float b) { float r; asm("v_max_f32 %0, %1, %2" : "=v"(r) : "v"(a), "v"(b)); return r; }
DI unsigned phi2(f32x2 u) {
  const f32x2 a = fma2(u, f32x2{LOG2E, LOG2E}, f32x2{-LOG2E, -LOG2E});
  const float e0 = __builtin_amdgcn_fmed3f(__builtin_amdgcn_exp2f(a[0]), 0.f, 1.f), e1 = __builtin_amdgcn_fmed3f(__builtin_amdgcn_exp2f(a[1]), 0.f, 1.f);
  return pk2v(f32x2{__builtin_amdgcn_fmed3f(u[0], e0, 3.0e38f), __builtin_amdgcn_fmed3f(u[1], e1, 3.0e38f)} + f32x2{1e-6f, 1e-6f});
}
DI float dot2(unsigned a, unsigned b, float c) { return __builtin_amdgcn_fdot2_f32_bf16(__builtin_bit_cast(bf16x2, a), __builtin_bit_cast(bf16x2, b), c, false); }
DI float gelu(float v) {
  const float u = v * (0.7978845608028654f * 2.f * LOG2E) * __builtin_fmaf(v * v, 0.044715f, 1.0f);
  return v * __builtin_amdgcn_rcpf(1.0f + __builtin_amdgcn_exp2f(-u));
}
template <class T> DI void pin(T& x) { asm volatile("" : "+v"(x)); }
template <class T> DI T P(T x) { pin(x); return x; }
template <int CTRL> DI float dppadd(float v) { return v + __uint_as_float(__builtin_amdgcn_update_dpp(0u, __float_as_uint(v), CTRL, 0xf, 0xf, true)); }
DI float sum16(float v) { v = dppadd<0xB1>(v); v = dppadd<0x4E>(v); v = dppadd<0x141>(v); return dppadd<0x140>(v); }
DI float add_x16(float v) { auto r = __builtin_amdgcn_permlane16_swap(__float_as_uint(v), __float_as_uint(v), false, false); return __uint_as_float(r[0]) + __uint_as_float(r[1]); }
DI float add_x32(float v) { auto r = __builtin_amdgcn_permlane32_swap(__float_as_uint(v), __float_as_uint(v), false, false); return __uint_as_float(r[0]) + __uint_as_float(r[1]); }

DI void glds4(v4u rs, unsigned dst, int soff, int v0, int v1, int v2, int v3) {
  asm volatile("s_nop 4\n\ts_mov_b32 m0, %1\n\ts_nop 0\n\tbuffer_load_dwordx4 %3, %0, %2 offen lds\n\t"
               "s_add_u32 m0, m0, 0x400\n\ts_nop 0\n\tbuffer_load_dwordx4 %4, %0, %2 offen lds\n\t"
               "s_add_u32 m0, m0, 0x400\n\ts_nop 0\n\tbuffer_load_dwordx4 %5, %0, %2 offen lds\n\t"
               "s_add_u32 m0, m0, 0x400\n\ts_nop 0\n\tbuffer_load_dwordx4 %6, %0, %2 offen lds"
               :: "s"(rs), "s"(dst), "s"(soff), "v"(v0), "v"(v1), "v"(v2), "v"(v3) : "memory", "scc");
}
template <int IMM> DI void glds1(v4u rs, unsigned base, int soff, int v) {
  asm volatile("s_add_u32 m0, %1, %4\n\ts_nop 0\n\tbuffer_load_dwordx4 %3, %0, %2 offen lds" :: "s"(rs), "s"(base), "s"(soff), "v"(v), "i"(IMM) : "memory", "scc");
}
template <int IMM> DI void glds1_sc1(v4u rs, unsigned base, int soff, int v) {
  asm volatile("s_add_u32 m0, %1, %4\n\ts_nop 0\n\tbuffer_load_dwordx4 %3, %0, %2 offen sc1 lds" :: "s"(rs), "s"(base), "s"(soff), "v"(v), "i"(IMM) : "memory", "scc");
}
template <int IMM> DI void set_m0(unsigned base) { asm volatile("s_add_u32 m0, %0, %1" :: "s"(base), "i"(IMM) : "memory", "scc"); }
template <int OFF> DI void glds_o(v4u rs, int soff, int v) { asm volatile("buffer_load_dwordx4 %2, %0, %1 offen offset:%3 sc1 lds" :: "s"(rs), "s"(soff), "v"(v), "i"(OFF) : "memory"); }
template <int N> using IC = std::integral_constant<int, N>;
template <int N> DI void waitv() { asm volatile("s_waitcnt vmcnt(%0)" :: "i"(N) : "memory"); }
template <int I, int N, class Fn> DI void static_for(Fn&& f) { if constexpr (I < N) { f(std::integral_constant<int, I>{}); static_for<I + 1, N>(f); } }
#define PIN(x) pin(x)
#define STAMP(id)
#define STAMP_ARG
#define STAMP_PASS
#define CSTAMP(id)

#define SLP 0
DI unsigned flag_ld(rsrc_t rws, int f, int n, int l) { asm volatile("" ::: "memory"); return __builtin_amdgcn_raw_buffer_load_b32(rws, (l < n ? l : 0) * 256, f * 256, SC1); }
DI void flag_wait(unsigned v, rsrc_t rws, int f, int n, unsigned tag, int l) {
  if (__builtin_expect(!__all(v == tag), 0)) {
#pragma unroll 1
    for (int sp = 0; sp < (1 << 16); sp++) { if (__all(flag_ld(rws, f, n, l) == tag)) break; }
  }
  asm volatile("" ::: "memory");
}
DI void wait_flags(rsrc_t rws, int f, int n, unsigned tag, int l) { flag_wait(flag_ld(rws, f, n, l), rws, f, n, tag, l); }
DI void flag_st(rsrc_t rws, int f, unsigned tag, bool me, int l) {
  asm volatile("" ::: "memory"); sts1(v4u{tag, tag, tag, tag}, rws, (me && l < 8) ? l * 16 : OOB, f * 256); asm volatile("" ::: "memory");
}

template <int MAT> DI int frag_off(int T, int ks) {
  if constexpr (MAT == 0) return WOP_OFF + ((T / 5) * 100 + ks * 5 + T % 5) * 1024;
  else if constexpr (MAT == 1) return W1P_OFF + ((T / 3) * 60 + ks * 3 + T % 3) * 1024;
  else return W2P_OFF + ((T / 5) * 240 + ks * 5 + T % 5) * 1024;
}
template <int MAT> DI void pack_issue(int ks, int cb, int l, v4u rs, unsigned stg) {
  const int r = l >> 3;
  static_for<0, 4>([&](auto Ic) { constexpr int i = Ic;
    const int sw = ((l & 7) ^ ((((r >> 1) & 1) | ((i & 1) << 1)) << 1)) * 16, sg = 4 * ks + i;
    if constexpr (MAT == 0) glds1<i * 1024>(rs, stg, sg * 8 * 1280 + cb * 128, r * 1280 + sw);
    else if constexpr (MAT == 1) { const int q = ks % 10;
      glds1<i * 1024>(rs, stg, ((2 * i + ks / 10) * 80 + (q < 8 ? 32 * (q >> 2) + 4 * (q & 3) : 8 * q)) * 3072 + cb * 128, (q < 8 ? 16 * (r >> 2) + (r & 3) : r) * 3072 + sw); }
    else { const int q = sg % 6; glds1<i * 1024>(rs, stg, (48 * (sg / 6) + (q < 4 ? 4 * q : 8 * q)) * 1280 + cb * 128, (q < 4 ? 16 * (r >> 2) + (r & 3) : r) * 1280 + sw); }
  });
}
DI int unit_lane(int t, int l) { const int g = l >> 4, q = (l >> 2) & 3, p = l & 3, f = ((q >> 1) & 1) | ((g & 1) << 1); return (8 * g + q) * 128 + ((t ^ f) * 32) + p * 8; }
DI v4u unit_read(LAS char* p) { const v2u a = trread(p), b = trread(p + 512); return v4u{a.x, a.y, b.x, b.y}; }
template <int MAT> DI void pack_finish(int ks, int cb, int l, rsrc_t rws, LAS char* stg) {
  v4u f[4];
#pragma unroll
  for (int t = 0; t < 4; t++) f[t] = unit_read(stg + unit_lane(t, l));
#pragma unroll
  for (int t = 0; t < 4; t++) sts1(f[t], rws, l * 16, frag_off<MAT>(cb * 4 + t, ks));
}

DI void unit_issue(int u, int l, const void* wo, const void* w1, unsigned stg) {
  if (u < 200) pack_issue<0>(u % 20, u / 20, l, mkrs4(wo, D * D * 2), stg); else pack_issue<1>(u % 20, u / 20 - 10, l, mkrs4(w1, D * F * 2), stg);
}
DI void unit_finish(int u, int l, rsrc_t rws, LAS char* stg) {
  if (u < 200) pack_finish<0>(u % 20, u / 20, l, rws, stg); else pack_finish<1>(u % 20, u / 20 - 10, l, rws, stg);
}
constexpr int TAB_OFF = 81920, SCR_OFF = TAB_OFF + 512, SNK_OFF = SCR_OFF + 2048, XT_OFF = 92160, XT_W = 10240, OB_ROW = 176, WO_L = 61440;
constexpr int RED_ROW = 336, RED_W = 16 * RED_ROW, RED_BUF = 4 * RED_W, A3_0 = 2 * RED_BUF, W2L_OFF = 65536;
constexpr int NL2 = 24;
static_assert(SNK_OFF + 1024 <= WO_L + 28 * 1024 && A3_0 + 20480 <= W2L_OFF, "LDS map");

#define N1 (B0 + 18)
#define N2 42
#define N3 50
#define N5 62
#define N6 80
#define X1 6
#define X2 8
#define XROT 3
#define B0 15
#define ABL 0
#define FPK 5
#define RK 12
#define DLY 4
#define FLK 4

__global__ __launch_bounds__(256, 1) void k_fused(const void* x, const void* wqkv, const void* wo, const void* w1, const void* b1p, const void* w2,
                                                  const void* b2p, void* out, char* ws) {
  extern __shared__ __attribute__((aligned(16))) char smem[];
  LAS char* L = (LAS char*)smem;
  const unsigned Lb = (unsigned)(size_t)L;
  { const unsigned long long te = __builtin_amdgcn_s_memrealtime(); while (__builtin_amdgcn_s_memrealtime() - te < 10ull) __builtin_amdgcn_s_sleep(2); }
  STAMP(0);
  const int tid = threadIdx.x, l = tid & 63, w = __builtin_amdgcn_readfirstlane(tid >> 6), c = l & 15, g = l >> 4;
  const int xg = blockIdx.x & 7, i = blockIdx.x >> 3;
  const unsigned Lx = Lb + XT_OFF + w * XT_W;
  LAS char* xt = L + XT_OFF + w * XT_W;
  {
    uint64_t pc; unsigned len;
    asm volatile("s_getpc_b64 %0\n.Lk_here:\n\ts_mov_b32 %1, .Lk_end-.Lk_here\n\ts_nop 2" : "=s"(pc), "=s"(len));
    glds1<9216>(mkrs4((const void*)pc, len & ~15u), Lx, (i * 4 + w) * 640, l < 40 ? l * 16 : OOB);
  }
  const int grp = xg * 4 + (i >> 3), j = i & 7;
  const bool p0 = j < 5;
  const int pk = (i >> 3) * 5 + j;
  const v4u rx4 = mkrs4(x, S * D * 2), rws4 = mkrs4(ws, WS_END);
  const rsrc_t rws = mkrs(ws, WS_END);
  STAMP(19);
  const int ub = blockIdx.x * 3 + w - 1, uc = (255 - blockIdx.x) * 2 + w - 2;
  const bool hasb = w > 0 && ub < 680, hasc = w > 1 && uc < 480;
  if (p0) {
    const v4u rw4 = mkrs4(wqkv, D * 1920 * 2);
    const int rr = l / 30, e = l - rr * 30, sec = e / 10;
    const int v0 = l < 60 ? rr * 3840 + sec * 1280 + (e - sec * 10) * 16 : OOB, s0 = (32 * pk + 8 * w) * 3840 + xg * 160;
    static_for<0, 4>([&](auto Uc) { constexpr int u = Uc; glds1<u * 1024>(rw4, Lx, s0 + u * 2 * 3840, v0); });
  }
  glds1_sc1<8192>(rws4, Lx, 0, l == 0 ? EPOCH_OFF : (l == 1 ? (F0 + xg * 32 + pk) * 256 : (l == 2 ? (FP + blockIdx.x) * 256 : OOB)));
  {
    const char* tp = l < 8 ? (const char*)wo + l * 102400 : l < 24 ? (const char*)w1 + (l - 8) * 122880 : l < 40 ? (const char*)w2 + (l - 24) * 122880 : l < 56 ? (const char*)out + (l - 40) * 163840 : l < 60 ? (const char*)b1p : (const char*)b2p;
    asm volatile("s_add_u32 m0, %1, 9216\n\ts_nop 0\n\tglobal_load_lds_dword %0, off" :: "v"(tp), "s"(Lx) : "memory", "scc");
  }
  int va[2];
#pragma unroll
  for (int u = 0; u < 2; u++) va[u] = (grp * 64 + w * 16 + u * 8 + (l >> 3)) * 1280 + (((l & 7) ^ (g | (u << 2))) * 16);
  auto ldx = [&](auto Qc) { constexpr int q = Qc; int ln = (q >> 1) + (p0 ? 0 : (j - 5) * 3); ln = ln >= 10 ? ln - 10 : ln;
    glds1<(q & 1) * 1024>(rx4, Lb + w * 2048 + ln * 8192, ln * 128, va[q & 1]); };
  if (!p0) { static_for<0, 20>(ldx); waitv<20>(); } else waitv<0>();
  STAMP(20);
  const unsigned tag = __builtin_amdgcn_readfirstlane(*(LAS unsigned*)(xt + 8192)) + 1u;
  if (p0) {
    const int q = (l >> 2) & 3, p = l & 3;
    const int col = g == 0 ? 160 + 8 * p : g == 1 ? 320 + 8 * p : g == 2 ? (p == 0 ? 192 : p == 1 ? 352 : 32) : 8 * p;
    LAS char* tp = xt + (q >> 1) * 1024 + (q & 1) * 480 + col;
    v4u f[4];
#pragma unroll
    for (int t = 0; t < 4; t++) { const v2u a = trread(tp + 40 * t), b = trread(tp + 40 * t + 2048); f[t] = v4u{a.x, a.y, b.x, b.y}; }
#pragma unroll
    for (int t = 0; t < 4; t++) sts1(f[t], rws, g * 1024 + c * 16, WQP_OFF + ((xg * 4 + t) * 20 + pk) * 4096 + w * 256);
    STAMP(1);
    static_for<0, X2>(ldx);
    waitv<X2>(); BAR(); flag_st(rws, F0 + xg * 32 + pk, tag, w == 0, l);
    STAMP(2);
    static_for<X2, 20>(ldx);
  }
  LAS char* ap[2];
#pragma unroll
  for (int kk = 0; kk < 2; kk++) ap[kk] = L + c * 128 + (((4 * kk + g) ^ ((c >> 1) & 7)) * 16);
  auto rdx = [&](int ks, int m) { return *(LAS v4u*)(ap[ks & 1] + (ks >> 1) * 8192 + m * 2048); };
  auto ldf = [&](int base, int q) { return lds1(rws, l * 16 + (q & 3) * 1024, base + (q >> 2) * 4096); };
  const int hd = 4 * j + w;
  v4u bq[80];
  auto ldq = [&](auto Qc) { constexpr int q = Qc, f = q < 60 ? (q / 3) * 4 + 2 - q % 3 : (q - 60) * 4 + 3; bq[f] = ldf(WQP_OFF + hd * 81920, f); };
  unsigned f0 = 0;
  if (w == 0) f0 = flag_ld(rws, F0 + j * 32, 20, l);
  if (hasb) unit_issue(ub, l, wo, w1, Lx);
  if (hasc) pack_issue<2>(uc % 48, uc / 48, l, mkrs4(w2, D * F * 2), Lx + 4096);
  if (hasb && hasc) waitv<8>(); else if (hasb || hasc) waitv<4>(); else waitv<0>();
  STAMP(3);
  {
    v4u a[20];
#pragma unroll
    for (int ks = 0; ks < 20; ks++) a[ks] = *(LAS v4u*)(ap[ks & 1] + (ks >> 1) * 8192 + w * 2048);
    SB();
    float s[2] = {0.f, 0.f}, q[2] = {0.f, 0.f};
#pragma unroll
    for (int ks = 0; ks < 20; ks++)
#pragma unroll
      for (int e = 0; e < 4; e++) { s[e & 1] = dot2(a[ks][e], ONES2, s[e & 1]); q[e & 1] = dot2(a[ks][e], a[ks][e], q[e & 1]); }
    const float mean = add_x32(add_x16(s[0] + s[1])) * (1.f / D);
    const float rs = __builtin_amdgcn_rsqf(fmaxf(add_x32(add_x16(q[0] + q[1])) * (1.f / D) - mean * mean, 0.f) + 1e-5f);
    *(LAS float*)(L + TAB_OFF + (g & 1) * 256 + (w * 16 + c) * 4) = (g & 1) ? -mean * rs : rs;
  }
  if (w == 0) flag_wait(f0, rws, F0 + j * 32, 20, tag, l);
  BAR();
  STAMP(4);
  static_for<0, B0>(ldq); SB();
  waitv<B0>();
  if (hasb && !(ABL & 1)) unit_finish(ub, l, rws, xt);
  if (hasc && !(ABL & 1)) pack_finish<2>(uc % 48, uc / 48, l, rws, xt + 4096);
  v4u ones = {ONES2, ONES2, ONES2, ONES2};
  PIN(ones);
  v4u fa[2][4];
  unsigned fp = 0;
  const int cb0 = (5 * j) >> 2;
  const int fpa = FP + (w == 0 ? 20 * cb0 / 3 : w == 1 ? (200 + 60 * j) / 3 : 208 - 24 * cb0 + (w - 2) * 24), fpn = w == 0 ? (20 * cb0 + 39) / 3 - 20 * cb0 / 3 + 1 : w == 1 ? 21 : 24;
  v4u bw1[60], bw2[60];
  const int w1b = W1P_OFF + hd * 61440, w2b = W2P_OFF + hd * 61440;
  auto ldw2 = [&](auto Qc) { constexpr int q = Qc; if constexpr (q >= 0 && q < 60) bw2[q] = ldf(w2b, q); };
  auto bulk = [&](auto Qc) { constexpr int q = Qc; if constexpr (q < 60) bw1[q] = ldf(w1b, q); else ldw2(IC<q - 60>{}); };
  auto run = [&](auto Nc, unsigned lds, int mem) { static_for<0, Nc>([&](auto Qc) { constexpr int q = Qc;
    if constexpr (q % 4 == 0) { set_m0<q * 1024>(lds); asm volatile("s_nop 0"); }
    glds_o<(q % 4) * 1024>(rws4, mem + (q / 4) * 4096, l * 16); }); };
#define W1F(r) bw1[r]
  {
    f32x4 acc[4][3], cs[3], trs[4], tnm[4];
    LAS char* tabp = L + TAB_OFF + g * 16;
#pragma unroll
    for (int m = 0; m < 4; m++) fa[0][m] = rdx(0, m);
    SB();
    static_for<0, 20>([&](auto Kc) { constexpr int ks = Kc;
      if constexpr (ks == FPK) { waitv<3 * FPK>(); BAR(); flag_st(rws, FP + blockIdx.x, tag, w == 0, l); }
      if constexpr (ks == 19) { flag_wait(fp, rws, fpa, fpn, tag, l); BAR(); }
      if constexpr (ks == 5) STAMP(23);
      static_for<0, 15>([&](auto Qc) { constexpr int Q = Qc, n = Q / 5, m = Q % 5;
        if constexpr (m < 4) { if constexpr (!(ABL & 4) || m == 0 || ks == 0) mfma_w<ks == 0, false, true>(fa[ks & 1][m], bq[ks * 4 + n], acc[m][n]); } else if constexpr (!(ABL & 2) || ks == 0) mfma_w<ks == 0, false, true>(ones, bq[ks * 4 + n], cs[n]);
        if constexpr (Q >= 1 && Q < 9 && (Q & 1)) fa[(ks + 1) & 1][3 - Q / 2] = rdx(ks < 19 ? ks + 1 : 0, 3 - Q / 2);
        if constexpr (m == 2 && B0 + ks * 3 + n < 80) ldq(IC<B0 + ks * 3 + n>{});
        if constexpr (ks == 15 && Q == 0) fp = flag_ld(rws, fpa, fpn, l);
        if constexpr (ks >= 18 && Q >= 9 && Q < 13) (ks == 18 ? trs : tnm)[Q - 9] = *(LAS f32x4*)(tabp + (ks - 18) * 256 + (Q - 9) * 64);
        SB(); });
    });
    static_assert(B0 >= 12 && B0 <= 20, "");
    asm volatile("s_nop 7");
#pragma unroll
    for (int n = 0; n < 3; n++) pin(cs[n]);
    STAMP(5);
    v4u fk[2], fv[2], fr[2], fb[2];
    const f32x2 one2 = {1.f, 1.f}, mk = c < 8 ? one2 : f32x2{0.f, 0.f}, ck = c < 8 ? -one2 : one2;
    static_for<0, 8>([&](auto Ec) { constexpr int m = Ec / 2, hf = Ec % 2;
      const f32x2 rs = hf ? hi2(trs[m]) : lo2(trs[m]), nm = hf ? hi2(tnm[m]) : lo2(tnm[m]);
      const f32x2 uk = fma2(rs, hf ? hi2(acc[m][0]) : lo2(acc[m][0]), fma2(nm, lo2(cs[0]), one2));
      const f32x2 vv = fma2(rs, hf ? hi2(acc[m][1]) : lo2(acc[m][1]), nm * lo2(cs[1]));
      const f32x2 ur = fma2(rs, hf ? hi2(acc[m][2]) : lo2(acc[m][2]), fma2(nm, lo2(cs[2]), one2));
      constexpr int e = (m & 1) * 2 + hf;
      fk[m >> 1][e] = phi2(uk); fv[m >> 1][e] = pk2v(vv); fr[m >> 1][e] = phi2(ur); fb[m >> 1][e] = pk2v(fma2(ur, mk, ck));
      if constexpr (B0 + 60 + Ec < 80) ldq(IC<B0 + 60 + Ec>{}); else bulk(IC<B0 + Ec - 20>{});
      if constexpr (hf) SB();
    });
#pragma unroll
    for (int u = 0; u < 2; u++) { pin(fk[u]); pin(fv[u]); pin(fr[u]); pin(fb[u]); }
    asm volatile("s_nop 1");
    f32x4 t00, t01, t10, t11;
    mfma_z(fk[0], fv[0], t00); mfma_z(fk[0], fb[0], t01); mfma_z(fr[0], fv[0], t10); mfma_z(fr[0], fb[0], t11);
    mfma_t(fk[1], fv[1], t00); mfma_t(fk[1], fb[1], t01); mfma_t(fr[1], fv[1], t10); mfma_t(fr[1], fb[1], t11);
#pragma unroll
    for (int u = 0; u < 2; u++) *(LAS v4u*)(L + (c >= 12 ? SCR_OFF + w * 512 + u * 256 + g * 64 + (c - 12) * 16 : SNK_OFF + l * 16)) = fr[u];
    asm volatile("s_nop 5");
    SB();
    const int kvo = KVP_OFF + (hd * 32 + grp) * 2048;
    const bool mid = c >= 4 && c < 12;
    sts1(v4u{pk2(t00[0], t00[1]), tag, pk2(t00[2], t00[3]), tag}, rws, l * 16, kvo);
    sts1(v4u{pk2(t01[0], t01[1]), tag, pk2(t01[2], t01[3]), tag}, rws, mid ? (12 + g * 8 + c) * 16 : OOB, kvo + 1024);
    sts1(v4u{pk2(t10[0], t10[1]), tag, pk2(t10[2], t10[3]), tag}, rws, g == 0 ? c * 16 : OOB, kvo + 1024);
    sts1(v4u{pk2(t11[0], t11[1]), tag, pk2(t11[2], t11[3]), tag}, rws, (g == 0 && mid) ? (44 + c) * 16 : OOB, kvo + 1024);
    STAMP(6);
  }
  if (w == ((grp + 1) & 3)) {
    const int f = l < 16 ? FE + grp * 128 + (l >> 2) * 32 + j * 4 + (l & 3) : (l < 20 ? FC : FD) + grp * 32 + (l & 3) * 8 + j;
    glds1<SNK_OFF>(rws4, Lb, 0, l < 28 ? f * 256 + (l >= 24 ? 128 : 0) : OOB);
  }
  const bool act = c < 10;
  v2u xr[5];
  {
    f32x4 acc[4], cs;
    float qrs[4], qnm[4];
    v2u q1[4];
    static_for<0, 21>([&](auto Kc) { constexpr int ks = Kc;
      if constexpr (ks == RK) {
        STAMP(7);
        if (w == (grp & 3)) {
          v4u pp[8];
          const int pv = g * 2048 + ((xg == 7 && c >= 8) ? 7 : c) * 16;
#pragma unroll 1
          for (int sp = 0; sp < (1 << 16); sp++) {
            asm volatile("" ::: "memory");
#pragma unroll
            for (int q = 0; q < 8; q++) pp[q] = lds1(rws, pv, KVP_OFF + (hd * 32 + 4 * q) * 2048 + xg * 256);
            SB();
            unsigned bad = 0;
#pragma unroll
            for (int q = 0; q < 8; q++) bad |= (pp[q].y ^ tag) | (pp[q].w ^ tag);
            if (__all(bad == 0)) break;
          }
          float sm[4] = {0.f, 0.f, 0.f, 0.f};
#pragma unroll
          for (int q = 0; q < 8; q++) { sm[0] += bflo(pp[q].x); sm[1] += bfhi(pp[q].x); sm[2] += bflo(pp[q].z); sm[3] += bfhi(pp[q].z); }
#pragma unroll
          for (int e = 0; e < 4; e++) sm[e] = add_x32(add_x16(sm[e]));
          sts1(v4u{pk2(sm[0], sm[1]), tag, pk2(sm[2], sm[3]), tag}, rws, g == 0 ? c * 16 : OOB, KVF_OFF + hd * 2048 + xg * 256);
        } else { const int f = (28 + ((w - grp - 1) & 3) * 24) * 1024; run(IC<24>{}, Lb + WO_L + f, WOP_OFF + j * 102400 + f); }
        SB();
        STAMP(8);
      }
      if constexpr (ks < 20) static_for<0, 5>([&](auto Qc) { constexpr int m = Qc;
        if constexpr (m < 4) mfma_a<ks == 0>(bq[ks * 4 + 3], fa[ks & 1][m], acc[m]); else mfma_a<ks == 0>(bq[ks * 4 + 3], ones, cs);
        if constexpr (ks < 19 && m < 4) fa[(ks + 1) & 1][3 - m] = rdx(ks + 1, 3 - m);
        if constexpr (m == 4 || (m == 1 && (ks & 1))) bulk(IC<B0 - 12 + (ks / 2) * 3 + (ks & 1) + (m == 4 && (ks & 1))>{});
        SB(); });
    });
    {
      LAS char* qp = L + SCR_OFF + w * 512 + (l & 15) / 4 * 16 + (l & 3) * 64;
#pragma unroll
      for (int m = 0; m < 4; m++) {
        qrs[m] = *(LAS float*)(L + TAB_OFF + c * 4 + m * 64); qnm[m] = *(LAS float*)(L + TAB_OFF + c * 4 + 256 + m * 64);
        q1[m] = trread(qp + (m >> 1) * 256 + (m & 1) * 8);
      }
#pragma unroll
      for (int n = 0; n < 5; n++) { const int sg = 10 * j + 2 * n + (g >> 1);
        xr[n] = *(LAS v2u*)(L + (sg >> 3) * 8192 + (w * 16 + c) * 128 + (((sg & 7) ^ ((c >> 1) & 7)) * 16) + (g & 1) * 8); }
    }
    asm volatile("s_nop 7");
    SB();
    STAMP(9);
    v4u ka0, ka1, kb0, kb1;
    const int c4 = c < 4 ? c : 4;
    auto kv_ld = [&]() { asm volatile("" ::: "memory");
      ka0 = lds1(rws, l * 16, KVF_OFF + hd * 2048); ka1 = lds1(rws, 1024 + c * 16, KVF_OFF + hd * 2048);
      kb0 = lds1(rws, 1024 + (16 + g * 8 + c4) * 16, KVF_OFF + hd * 2048); kb1 = lds1(rws, 1024 + (48 + c4) * 16, KVF_OFF + hd * 2048); };
    auto kv_ok = [&]() { return __all(((ka0.y ^ tag) | (ka0.w ^ tag) | (ka1.y ^ tag) | (ka1.w ^ tag) | (kb0.y ^ tag) | (kb0.w ^ tag) | (kb1.y ^ tag) | (kb1.w ^ tag)) == 0); };
    static_for<N1, N2>(bulk); SB();
    kv_ld();
    v4u pq[4];
#pragma unroll
    for (int m = 0; m < 4; m++) {
      const f32x2 rs = {qrs[m], qrs[m]}, nm = {qnm[m], qnm[m]}, one2 = {1.f, 1.f};
      pq[m] = v4u{phi2(fma2(rs, lo2(acc[m]), fma2(nm, lo2(cs), one2))), phi2(fma2(rs, hi2(acc[m]), fma2(nm, hi2(cs), one2))), q1[m].x, q1[m].y};
      pin(pq[m]);
    }
    BAR();
    STAMP(10);
    if (__builtin_expect(!kv_ok(), 0)) {
#pragma unroll 1
      for (int sp = 0; sp < (1 << 16); sp++) { kv_ld(); if (kv_ok()) break; }
    }
    STAMP(24);
    v4u kf0 = {ka0.x, ka0.z, g == 3 ? ka1.x : 0u, g == 3 ? ka1.z : 0u}, kf1 = {kb0.x, kb0.z, g == 3 ? kb1.x : 0u, g == 3 ? kb1.z : 0u};
    pin(kf0); pin(kf1);
    asm volatile("s_nop 1");
    f32x4 n0[4], n1[4];
    LAS char* ob0 = L + c * OB_ROW + w * 40 + g * 8;
    LAS char* ob1 = L + c * OB_ROW + (g == 0 ? w * 40 + 32 : 160);
#pragma unroll
    for (int m = 0; m < 4; m++) { mfma_z(kf0, pq[m], n0[m]); mfma_z(kf1, pq[m], n1[m]); }
    asm volatile("s_nop 7");
    SB();
#pragma unroll
    for (int m = 0; m < 4; m++) {
      auto sw = __builtin_amdgcn_permlane16_swap(__float_as_uint(n1[m][0]), __float_as_uint(n1[m][0]), false, false);
      const float r = __builtin_amdgcn_rcpf(__uint_as_float(sw[1]) + 1e-6f);
      const f32x2 r2 = {r, r};
      *(LAS v2u*)(ob0 + m * 16 * OB_ROW) = v2u{pk2v(lo2(n0[m]) * r2), pk2v(hi2(n0[m]) * r2)};
      *(LAS v2u*)(ob1 + m * 16 * OB_ROW) = v2u{pk2v(lo2(n1[m]) * r2), pk2v(hi2(n1[m]) * r2)};
    }
    BAR();
    v4u o[3];
#pragma unroll
    for (int q = 0; q < 3; q++) o[q] = *(LAS v4u*)(L + (w * 16 + c) * OB_ROW + (q * 4 + (q == 2 ? g & 1 : g)) * 16);
#pragma unroll
    for (int q = 0; q < 3; q++) sts1(o[q], rws, (q < 2 || g < 2) ? l * 16 + q * 1024 : OOB, OF_OFF + (grp * 4 + w) * 20480 + j * 2560);
    STAMP(25);
    SB();
    static_for<N2, N3>(bulk); SB();
    waitv<N3 - N2>();
    flag_st(rws, FC + grp * 32 + w * 8 + j, tag, true, l);
    run(IC<7>{}, Lb + WO_L + w * 7168, WOP_OFF + j * 102400 + w * 7168);
    STAMP(11);
  }
  LAS char* rdst = L + w * RED_W + c * RED_ROW + g * 16;
  LAS char* rsrc = L + (4 * w + g) * RED_ROW + (act ? c : 9) * 32;
  const int rowo = act ? (grp * 64 + 4 * w + g) * 1280 + j * 160 + c * 16 : OOB;
  auto red_ld = [&](int m, f32x4 (&r)[8]) {
#pragma unroll
    for (int ww = 0; ww < 4; ww++) { LAS f32x4* p = (LAS f32x4*)(rsrc + (m & 1) * RED_BUF + ww * RED_W); r[2 * ww] = p[0]; r[2 * ww + 1] = p[1]; }
  };
  v4u hs[4][2], hk[4];
  float hsa[4], hsb[4];
  const int hsv = g * 512 + c * 16;
  auto h_poll = [&](int m) { asm volatile("" ::: "memory");
    hs[m][0] = lds1(rws, hsv, (FD + grp * 32 + m * 8) * 256); hs[m][1] = lds1(rws, hsv + 256, (FD + grp * 32 + m * 8) * 256); };
  auto h_ok = [&](int m) { return __all(((hs[m][0].y ^ tag) | (hs[m][0].w ^ tag) | (hs[m][1].y ^ tag) | (hs[m][1].w ^ tag)) == 0); };
  auto h_wait = [&](int m) {
    if (__builtin_expect(!h_ok(m), 0)) {
#pragma unroll 1
      for (int sp = 0; sp < (1 << 16); sp++) { h_poll(m); if (h_ok(m)) break; }
    }
    asm volatile("" ::: "memory");
    hsa[m] = P(__uint_as_float(hs[m][0].x) + __uint_as_float(hs[m][1].x)); hsb[m] = P(__uint_as_float(hs[m][0].z) + __uint_as_float(hs[m][1].z));
  };
  v2u bias[3];
  const unsigned La = Lb + w * 5120;
  const int hso = HF_OFF + grp * 81920 + w * 5120;
  auto ldh = [&](auto Mc, auto Qc) { constexpr int m = Mc, q = Qc; glds1_sc1<((m & 1) ? 0 : A3_0) + q * 1024>(rws4, La, hso + m * 20480 + q * 1024, l * 16); };
  f32x4 cs[3];
  {
    const rsrc_t rb1 = mkrs(b1p, F * 2);
#pragma unroll
    for (int n = 0; n < 3; n++) bias[n] = __builtin_amdgcn_raw_buffer_load_b64(rb1, ((12 * j + 3 * w + n) * 16 + 4 * g) * 2, 0, 0);
    static_for<N3, N5>(bulk); SB();
    wait_flags(rws, FC + grp * 32 + w * 8, 8, tag, l);
    STAMP(12);
    v4u a[20], bo[100];
    auto lda = [&](auto Qc) { constexpr int q = Qc; a[q] = ldf(OF_OFF + (grp * 4 + w) * 20480, q); };
    static_for<0, 12>(lda);
    BAR();
    LAS char* wp[2] = {L + WO_L + l * 16, L + WO_L + 51200 + l * 16};
    auto rdo = [&](int f) { return *(LAS v4u*)(wp[f / 50] + (f % 50) * 1024); };
#pragma unroll
    for (int f = 0; f < 10; f++) bo[f] = rdo(f);
    SB();
    f32x4 acc[5];
    static_for<0, 100>([&](auto Fc) { constexpr int f = Fc, k = f / 5, n = f % 5;
      mfma_a<k == 0>(bo[f], a[k], acc[n]);
      if constexpr (k < 18) bo[(k + 2) * 5 + 4 - n] = rdo((k + 2) * 5 + 4 - n);
      if constexpr (n == 2 && k + 12 < 20) lda(IC<k + 12>{});
      SB(); });
    STAMP(13);
    asm volatile("s_nop 7");
    SB();
    unsigned hp[10];
    float s[2] = {0.f, 0.f}, q[2] = {0.f, 0.f};
#pragma unroll
    for (int n = 0; n < 5; n++) {
      hp[2 * n] = pk2v(lo2(acc[n]) + f32x2{bflo(xr[n].x), bfhi(xr[n].x)}); hp[2 * n + 1] = pk2v(hi2(acc[n]) + f32x2{bflo(xr[n].y), bfhi(xr[n].y)});
    }
    const int hb = HF_OFF + (grp * 4 + w) * 20480 + j * 2560;
    sts1(v4u{hp[0], hp[1], hp[2], hp[3]}, rws, l * 16, hb);
    sts1(v4u{hp[4], hp[5], hp[6], hp[7]}, rws, l * 16 + 1024, hb);
    auto s0 = __builtin_amdgcn_permlane16_swap(hp[8], hp[8], false, false);
    auto s1 = __builtin_amdgcn_permlane16_swap(hp[9], hp[9], false, false);
    sts1(v4u{s0[0], s1[0], s0[1], s1[1]}, rws, (g & 1) ? OOB : 2048 + (g >> 1) * 256 + c * 16, hb);
    STAMP(14);
#pragma unroll
    for (int e = 0; e < 10; e++) { s[e & 1] = dot2(hp[e], ONES2, s[e & 1]); q[e & 1] = dot2(hp[e], hp[e], q[e & 1]); }
    const v4u hst = {__float_as_uint(add_x32(add_x16(s[0] + s[1]))), tag, __float_as_uint(add_x32(add_x16(q[0] + q[1]))), tag};
    static_for<N5, N5 + 6>(bulk); SB();
    waitv<6>();
    sts1(hst, rws, g == 0 ? c * 16 : OOB, (FD + grp * 32 + w * 8 + j) * 256);
    BAR();
    STAMP(15);
    static_for<N5 + 6, N6>(bulk); SB();
    h_poll(0); h_poll(1);
    h_wait(0); h_wait(1);
    STAMP(16);
    run(IC<5>{}, La + A3_0, hso); run(IC<5>{}, La, hso + 20480);
    h_poll(2); h_poll(3);
    asm volatile("s_nop 1");
    static_for<0, 60>([&](auto Qc) { constexpr int q = Qc; mfma_w<(q < 3), true>(W1F(q), ones, cs[q % 3]);
      if constexpr (q % 5 == 0 && N6 + q / 5 < 92) bulk(IC<N6 + q / 5>{});
      SB(); });
    static_assert(N6 == 80, "twelve requests ride on the column sums");
    h_wait(2); h_wait(3);
  }
  {
    const int few = FE + grp * 128, feo = few + j * 4 + w;
    unsigned fs = 0;
    f32x2 bf[6];
#pragma unroll
    for (int e = 0; e < 6; e++) bf[e] = P(f32x2{bflo(bias[e >> 1][e & 1]), bfhi(bias[e >> 1][e & 1])});
    SB();
    waitv<12>(); BAR();
    STAMP(17);
    LAS char* ab = L + g * 5120 + c * 16;
    auto rda = [&](int t) { return *(LAS v4u*)(ab + (((t / 20) & 1) ? 0 : A3_0) + (t % 20) * 256); };
    const int cc = act ? c : 0;
    LAS char* hr0 = L + j * 2560 + (4 * w + g) * 16 + (cc < 8 ? ((cc >> 2) * 4 + (cc & 1) * 2) * 256 + ((cc >> 1) & 1) * 8 : cc * 256);
    LAS char* hr1 = hr0 + (cc < 8 ? 256 : 8);
    const unsigned L2w = Lb + W2L_OFF + w * (NL2 * 1024);
    constexpr int RD = 6;
    v4u af[RD];
    f32x4 acc[3];
    f32x2 bp[6], gv[6], gu[6];
    unsigned gp[6];
    float rs = 0.f, nr = 0.f, sa = 0.f, sb = 0.f;
    auto stats = [&](int m, auto Pc) { constexpr int p = Pc;
      if constexpr (p == 1) { auto x = __builtin_amdgcn_permlane16_swap(__float_as_uint(hsa[m]), __float_as_uint(hsb[m]), false, false); sa = P(__uint_as_float(x[0]) + __uint_as_float(x[1])); sb = P(sa); }
      if constexpr (p == 2) { auto x = __builtin_amdgcn_permlane32_swap(__float_as_uint(sa), __float_as_uint(sb), false, false); sa = P(__uint_as_float(x[0]) + __uint_as_float(x[1])); sb = P(sa); }
      if constexpr (p == 3) { auto x = __builtin_amdgcn_permlane16_swap(__float_as_uint(sa), __float_as_uint(sb), false, false);
        sa = P(__uint_as_float(x[0]) * (1.f / D)); sb = P(fmaxf(__uint_as_float(x[1]) * (1.f / D) - sa * sa, 0.f)); }
      if constexpr (p == 4) { const float r1 = __builtin_amdgcn_rsqf(sb + 1e-5f);
        rs = P(r1 * __builtin_amdgcn_rsqf(sb * r1 * r1 + 1e-5f));
        nr = P(-sa * rs); }
    };
    auto epi1 = [&]() {
      asm volatile("s_nop 7");
      SB();
      constexpr float C0 = -0.7978845608028654f * 2.f * LOG2E, C1 = C0 * 0.044715f;
      const f32x2 rs2 = {rs, rs};
#pragma unroll
      for (int e = 0; e < 6; e++) gv[e] = fma2(rs2, (e & 1) ? hi2(acc[e >> 1]) : lo2(acc[e >> 1]), bp[e]);
      SB();
#pragma unroll
      for (int e = 0; e < 6; e++) gu[e] = gv[e] * gv[e];
      SB();
#pragma unroll
      for (int e = 0; e < 6; e++) gu[e] = fma2(gu[e], f32x2{C1, C1}, f32x2{C0, C0});
      SB();
#pragma unroll
      for (int e = 0; e < 6; e++) gu[e] = P(gu[e] * gv[e]);
      SB();
    };
    auto gstore = [&](int m) {
      const int base = GF_OFF + (grp * 4 + m) * 49152 + 6 * hd * 256;
      sts1(v4u{gp[0], gp[1], gp[2], gp[3]}, rws, l * 16, base);
      auto s0 = __builtin_amdgcn_permlane16_swap(gp[4], gp[4], false, false);
      auto s1 = __builtin_amdgcn_permlane16_swap(gp[5], gp[5], false, false);
      sts1(v4u{s0[0], s1[0], s0[1], s1[1]}, rws, (g & 1) ? OOB : (g >> 1) * 256 + c * 16, base + 1024);
    };
    auto egap = [&](auto Mc, auto Gc) { constexpr int G = Gc;
      if constexpr (G % 2 == 0 && G < 24) gu[G / 4][(G / 2) & 1] = P(__builtin_amdgcn_exp2f(gu[G / 4][(G / 2) & 1]));
      if constexpr (G % 2 == 1 && G >= 3 && G < 27) gu[(G - 3) / 4][((G - 3) / 2) & 1] = P(__builtin_amdgcn_rcpf(gu[(G - 3) / 4][((G - 3) / 2) & 1]));
      SB();
      if constexpr (G % 2 == 1 && G < 25) gu[(G - 1) / 4][((G - 1) / 2) & 1] = P(gu[(G - 1) / 4][((G - 1) / 2) & 1] + 1.0f);
      if constexpr (G % 2 == 0 && G >= 4 && G < 28) gv[(G - 4) / 4][((G - 4) / 2) & 1] = P(gv[(G - 4) / 4][((G - 4) / 2) & 1] * gu[(G - 4) / 4][((G - 4) / 2) & 1]);
      if constexpr (G >= 27 && G < 30) { gp[(G - 27) * 2] = P(pk2v(gv[(G - 27) * 2])); gp[(G - 27) * 2 + 1] = P(pk2v(gv[(G - 27) * 2 + 1])); }
      if constexpr (G == 31) gstore(Mc);
    };
    v4u a4[12];
    auto lda4 = [&](int q) { return ldf(GF_OFF + ((grp * 4 + q / 12) * 48 + w * 12) * 1024, q % 12); };
    const rsrc_t ro = mkrs(out, S * D * 2), rb2 = mkrs(b2p, D * 2);
    v4u br;
#pragma unroll
    for (int q = 0; q < RD; q++) af[q] = rda(q);
    SB();
    asm volatile("s_nop 1");
    auto rider = [&](auto Qc) { constexpr int q = Qc; if constexpr (q < NL2) glds1_sc1<q * 1024>(rws4, L2w, w2b + (60 - NL2 + q) * 1024, l * 16); };
    static_for<0, 80>([&](auto Tc) {
      constexpr int T = Tc, m = T / 20, k = T % 20;
      if constexpr (k == 14 && m < 3) {
        if constexpr (m > 0) waitv<(m == 1 ? 9 : 6)>();
        BAR();
      }
      if constexpr (k == 0) STAMP(26 + m);
      if constexpr (T == 66) flag_wait(fs, rws, few, 32, tag, l);
      const v4u cur = af[T % RD];
      static_for<0, 3>([&](auto Nc) {
        constexpr int n = Nc, G = k * 3 + n;
        mfma_w<k == 0, true>(W1F(G), cur, acc[n]);
        if constexpr (m > 0) egap(IC<m - 1>{}, IC<G>{});
        if constexpr (n == 2 && (T & 1) && T + RD < 80) { af[T % RD] = rda(T + RD); SB(); af[(T - 1) % RD] = rda(T + RD - 1); }
        if constexpr (G == 30) { const v2u u0 = *(LAS v2u*)(hr0 + ((m & 1) ? 0 : A3_0)), u1 = *(LAS v2u*)(hr1 + ((m & 1) ? 0 : A3_0)); hk[m] = v4u{u0.x, u0.y, u1.x, u1.y}; }
        if constexpr (m < 2 && n == 0 && k >= 14 && k < 19) ldh(IC<m + 2>{}, IC<k - 14>{});
        if constexpr (m == 0 && n == 1 && k < 14) rider(IC<k>{});
        if constexpr (m > 0 && m < 3 && n == 1 && (k & 1) == 0 && k < 14) rider(IC<14 + (m - 1) * 7 + k / 2>{});
        if constexpr (G >= 33 && G < 41 && (G & 1)) stats(m, IC<(G - 31) / 2>{});
        if constexpr (G >= 42 && G < 54) bp[(G - 42) / 2][G & 1] = P(__builtin_fmaf(nr, cs[(G - 42) / 4][(G - 42) & 3], bf[(G - 42) / 2][G & 1]));
        if constexpr (m >= 1 && G == 59) { waitv<(m == 1 ? 7 : m == 2 ? 0 : 7)>(); flag_st(rws, feo + (m - 1) * 32, tag, true, l); }
        if constexpr (n == 0 && T == 60) br = ldp(rb2, act ? j * 160 + c * 16 : OOB, 0);
        if constexpr (n == 1 && T >= 62 && T < 66) ldw2(IC<32 + T - 62>{});
        if constexpr (n == 0 && T == 61) fs = flag_ld(rws, few, 32, l);
        if constexpr (n == 0 && T >= 66 && T < 78) a4[T - 66] = lda4(T - 66);
        SB();
      });
      if constexpr (k == 19) epi1();
    });
    fs = flag_ld(rws, few + 32, 32, l);
#pragma unroll
    for (int e = 0; e < 6; e++) gu[e] = f32x2{__builtin_amdgcn_exp2f(gu[e][0]), __builtin_amdgcn_exp2f(gu[e][1])};
    SB();
#pragma unroll
    for (int e = 0; e < 6; e++) gu[e] = gu[e] + f32x2{1.f, 1.f};
    SB();
#pragma unroll
    for (int e = 0; e < 6; e++) gu[e] = f32x2{__builtin_amdgcn_rcpf(gu[e][0]), __builtin_amdgcn_rcpf(gu[e][1])};
    SB();
#pragma unroll
    for (int e = 0; e < 6; e++) gp[e] = pk2v(gv[e] * gu[e]);
    gstore(3);
    SB();
    STAMP(18);
    f32x4 ac4[2][5], r[8];
    float b2f[8], ov[8], ow[2];
#pragma unroll
    for (int e = 0; e < 8; e++) b2f[e] = (e & 1) ? bfhi(br[e >> 1]) : bflo(br[e >> 1]);
    auto fop = [&](auto Mc, auto Pc, auto Sc) { constexpr int m = Mc, st = Sc;
      static_for<0, 2>([&](auto Zc) { constexpr int z = Zc, e = 2 * Pc + z;
        if constexpr (st == 0) ov[e] = P(r[e >> 2][e & 3] + r[2 + (e >> 2)][e & 3]);
        if constexpr (st == 1) ow[z] = P(r[4 + (e >> 2)][e & 3] + r[6 + (e >> 2)][e & 3]);
        if constexpr (st == 2) ov[e] = P(ov[e] + ow[z]);
        if constexpr (st == 3) ow[z] = P(z ? bfhi(hk[m][e >> 1]) : bflo(hk[m][e >> 1]));
        if constexpr (st == 4) ow[z] = P(ow[z] + b2f[e]);
        if constexpr (st == 5) ov[e] = P(ov[e] + ow[z]);
        SB(); });
    };
    auto ost = [&](int m) { sts1(v4u{pk2(ov[0], ov[1]), pk2(ov[2], ov[3]), pk2(ov[4], ov[5]), pk2(ov[6], ov[7])}, ro, rowo, m * 20480); };
    SB();
    static_for<0, 48>([&](auto Tc) {
      constexpr int T = Tc, m = T / 12, k = T % 12;
      if constexpr (k == 0) STAMP(34 + 2 * m);
      if constexpr (k == 0 && m < 3) flag_wait(fs, rws, few + (m + 1) * 32, 32, tag, l);
      if constexpr (k == 4) BAR();
      const v4u cur = a4[k];
      static_for<0, 5>([&](auto Nc) { constexpr int n = Nc, G = k * 5 + n;
        mfma_w<k == 0, (G >= 32 || G < 3), true>(bw2[G], cur, ac4[m & 1][n]);
        if constexpr (m == 0 && G < NL2) bw2[60 - NL2 + G] = *(LAS v4u*)(L + W2L_OFF + w * (NL2 * 1024) + G * 1024 + l * 16);
        if constexpr (m > 0 && G >= 3 && G < 18 && G % 3 == 0) *(LAS f32x4*)(rdst + ((m - 1) & 1) * RED_BUF + (G / 3 - 1) * 64) = ac4[(m - 1) & 1][G / 3 - 1];
        if constexpr (m > 0 && G >= 20 && G < 28) { constexpr int q = ((G - 20) & 3) * 2 + ((G - 20) >> 2); r[q] = ((LAS f32x4*)(rsrc + ((m - 1) & 1) * RED_BUF + (q >> 1) * RED_W))[q & 1]; }
        if constexpr (m > 0 && G >= 32 && G < 56) fop(IC<m - 1>{}, IC<(G - 32) / 6>{}, IC<(G - 32) % 6>{});
        if constexpr (m > 0 && G == 57) ost(m - 1);
        if constexpr (m < 3 && n == 4) a4[k] = lda4(m * 12 + k + 12);
        if constexpr (n == 2 && T == 6) { waitv<6>(); flag_st(rws, feo + 96, tag, true, l); }
        if constexpr (n == 2 && m < 2 && k == 8) fs = flag_ld(rws, few + (m + 2) * 32, 32, l);
        SB();
      });
    });
    STAMP(21);
    asm volatile("s_nop 7");
    SB();
#pragma unroll
    for (int n = 0; n < 5; n++) *(LAS f32x4*)(rdst + RED_BUF + n * 64) = ac4[1][n];
    BAR(); red_ld(3, r);
#pragma unroll
    for (int e = 0; e < 8; e++) ov[e] = ((r[e >> 2][e & 3] + r[2 + (e >> 2)][e & 3]) + (r[4 + (e >> 2)][e & 3] + r[6 + (e >> 2)][e & 3])) + (((e & 1) ? bfhi(hk[3][e >> 1]) : bflo(hk[3][e >> 1])) + b2f[e]);
    ost(3);
    flag_st(rws, 0, tag, blockIdx.x == 0 && w == 0, l);
  }
  asm volatile(".Lk_end:");
}

extern "C" void kernel_launch(void* const* d_in, const int* in_sizes, int n_in, void* d_out, int out_size, void* d_ws, size_t ws_size, hipStream_t stream) {
  (void)hipFuncSetAttribute((const void*)k_fused, hipFuncAttributeMaxDynamicSharedMemorySize, 160 * 1024);
  k_fused<<<256, 256, 160 * 1024, stream>>>(d_in[0], d_in[1], d_in[2], d_in[4], d_in[5], d_in[6], d_in[7], d_out, (char*)d_ws);
}
```

```cpp
#include <hip/hip_runtime.h>
#include <hip/hip_bf16.h>
#include <stdint.h>
#include <type_traits>

#define LAS __attribute__((address_space(3)))
#define DI __device__ __forceinline__
typedef __attribute__((ext_vector_type(8))) __bf16 bf16x8;
typedef __attribute__((ext_vector_type(2))) __bf16 bf16x2;
typedef __attribute__((ext_vector_type(4))) float f32x4;
typedef __attribute__((ext_vector_type(2))) float f32x2;
typedef __attribute__((ext_vector_type(4))) unsigned v4u;
typedef __attribute__((ext_vector_type(2))) unsigned v2u;
typedef __attribute__((ext_vector_type(4))) short s16x4;
typedef __amdgpu_buffer_rsrc_t rsrc_t;

constexpr int S = 2048, D = 640, F = 1536;
constexpr int ACT = S * D * 2;
constexpr int EPOCH_OFF = 0;
constexpr int F0 = 1;
constexpr int FP = F0 + 256;
constexpr int FC = FP + 256;
constexpr int FD = FC + 1024;
constexpr int FE = FD + 1024;
constexpr int NFLAG = FE + 4096;
constexpr int WQP_OFF = (NFLAG * 256 + 65535) / 65536 * 65536, OF_OFF = WQP_OFF + 32 * 80 * 1024, HF_OFF = OF_OFF + ACT;
constexpr int GF_OFF = HF_OFF + ACT, WOP_OFF = GF_OFF + S * F * 2, W1P_OFF = WOP_OFF + D * D * 2, W2P_OFF = W1P_OFF + D * F * 2;
constexpr int KVP_OFF = W2P_OFF + D * F * 2, KVF_OFF = KVP_OFF + 32 * 32 * 2048, WS_END = KVF_OFF + 32 * 2048;
constexpr int OOB = 0x7ffffff0;
constexpr int SC1 = 16;
constexpr unsigned ONES2 = 0x3f803f80u;

DI rsrc_t mkrs(const void* p, int bytes) { return __builtin_amdgcn_make_buffer_rsrc((void*)p, 0, bytes, 0x00020000); }
DI v4u mkrs4(const void* p, int bytes) { const uint64_t a = (uint64_t)p; return v4u{(unsigned)a, (unsigned)(a >> 32) & 0xffffu, (unsigned)bytes, 0x00020000u}; }
DI f32x4 mfma(v4u a, v4u b, f32x4 c) {
  return __builtin_amdgcn_mfma_f32_16x16x32_bf16(__builtin_bit_cast(bf16x8, a), __builtin_bit_cast(bf16x8, b), c, 0, 0, 0);
}
DI void mfma_t(v4u a, v4u b, f32x4& c) { asm volatile("v_mfma_f32_16x16x32_bf16 %0, %1, %2, %0" : "+v"(c) : "v"(a), "v"(b)); }
DI void mfma_z(v4u a, v4u b, f32x4& c) { asm volatile("v_mfma_f32_16x16x32_bf16 %0, %1, %2, 0" : "=&v"(c) : "v"(a), "v"(b)); }
template <bool FIRST, bool AG, bool BG = false> DI void mfma_w(v4u a, v4u b, f32x4& c) {
#define MF_(CA, CB) do { if constexpr (FIRST) asm volatile("v_mfma_f32_16x16x32_bf16 %0, %1, %2, 0" : "=&v"(c) : CA(a), CB(b)); \
                         else asm volatile("v_mfma_f32_16x16x32_bf16 %0, %1, %2, %0" : "+v"(c) : CA(a), CB(b)); } while (0)
  if constexpr (AG && BG) MF_("a", "a"); else if constexpr (AG) MF_("a", "v"); else if constexpr (BG) MF_("v", "a"); else MF_("v", "v");
#undef MF_
}
template <bool FIRST> DI void mfma_a(v4u a, v4u b, f32x4& c) { if constexpr (FIRST) mfma_z(a, b, c); else mfma_t(a, b, c); }
DI unsigned pk2(float a, float b) { f32x2 f = {a, b}; return __builtin_bit_cast(unsigned, __builtin_convertvector(f, bf16x2)); }
DI float bflo(unsigned u) { return __uint_as_float(u << 16); }
DI float bfhi(unsigned u) { return __uint_as_float(u & 0xffff0000u); }
DI v4u ldp(rsrc_t r, int voff, int soff) { return __builtin_amdgcn_raw_buffer_load_b128(r, voff, soff, 0); }
DI v4u lds1(rsrc_t r, int voff, int soff) { return __builtin_amdgcn_raw_buffer_load_b128(r, voff, soff, SC1); }
DI void sts1(v4u v, rsrc_t r, int voff, int soff) { asm volatile("" : "+v"(v)); __builtin_amdgcn_raw_buffer_store_b128(v, r, voff, soff, SC1); asm volatile("s_nop 1" :: "v"(v) : "memory"); }
DI v2u trread(LAS char* p) { return __builtin_bit_cast(v2u, __builtin_amdgcn_ds_read_tr16_b64_v4i16((LAS s16x4*)p)); }
#define WAITV(n) asm volatile("s_waitcnt vmcnt(" #n ")" ::: "memory")
#define BAR() do { asm volatile("s_waitcnt lgkmcnt(0)" ::: "memory"); __builtin_amdgcn_s_barrier(); asm volatile("" ::: "memory"); } while (0)
#define SB() __builtin_amdgcn_sched_barrier(0)
constexpr float LOG2E = 1.4426950408889634f;
DI float phi(float t) { return fmaxf(t, 0.f) + __builtin_amdgcn_fmed3f(__builtin_amdgcn_exp2f(t * LOG2E), 0.f, 1.f) + 1e-6f; }
template <class T> DI void pin(T& x) { asm volatile("" : "+v"(x)); }
template <class T> DI T P(T x) { pin(x); return x; }
template <class T> DI T PS(T x) { asm volatile("" : "+s"(x)); return x; }
DI f32x2 fma2(f32x2 a, f32x2 b, f32x2 c) { return __builtin_elementwise_fma(a, b, c); }
DI f32x2 lo2(f32x4 v) { return f32x2{v[0], v[1]}; }
DI f32x2 hi2(f32x4 v) { return f32x2{v[2], v[3]}; }
DI unsigned pk2v(f32x2 f) { return __builtin_bit_cast(unsigned, __builtin_convertvector(f, bf16x2)); }
DI float maxf(float a, float b) { float r; asm("v_max_f32 %0, %1, %2" : "=v"(r) : "v"(a), "v"(b)); return r; }
DI unsigned phi2(f32x2 u) {
  const f32x2 a = fma2(u, f32x2{LOG2E, LOG2E}, f32x2{-LOG2E, -LOG2E});
  const float e0 = __builtin_amdgcn_fmed3f(__builtin_amdgcn_exp2f(a[0]), 0.f, 1.f), e1 = __builtin_amdgcn_fmed3f(__builtin_amdgcn_exp2f(a[1]), 0.f, 1.f);
  return pk2v(f32x2{__builtin_amdgcn_fmed3f(u[0], e0, 3.0e38f), __builtin_amdgcn_fmed3f(u[1], e1, 3.0e38f)} + f32x2{1e-6f, 1e-6f});
}
template <int N> DI void phi_n(const f32x2 (&u)[N], unsigned (&o)[N]) {
  f32x2 a[N];
#pragma unroll
  for (int e = 0; e < N; e++) a[e] = P(fma2(u[e], f32x2{LOG2E, LOG2E}, f32x2{-LOG2E, -LOG2E}));
  SB();
#pragma unroll
  for (int e = 0; e < N; e++) a[e] = P(f32x2{__builtin_amdgcn_fmed3f(__builtin_amdgcn_exp2f(a[e][0]), 0.f, 1.f), __builtin_amdgcn_fmed3f(__builtin_amdgcn_exp2f(a[e][1]), 0.f, 1.f)});
  SB();
#pragma unroll
  for (int e = 0; e < N; e++) a[e] = P(f32x2{__builtin_amdgcn_fmed3f(u[e][0], a[e][0], 3.0e38f), __builtin_amdgcn_fmed3f(u[e][1], a[e][1], 3.0e38f)});
  SB();
#pragma unroll
  for (int e = 0; e < N; e++) a[e] = P(a[e] + f32x2{1e-6f, 1e-6f});
  SB();
#pragma unroll
  for (int e = 0; e < N; e++) o[e] = pk2v(a[e]);
  SB();
}
DI float dot2(unsigned a, unsigned b, float c) { return __builtin_amdgcn_fdot2_f32_bf16(__builtin_bit_cast(bf16x2, a), __builtin_bit_cast(bf16x2, b), c, false); }
DI float gelu(float v) {
  const float u = v * (0.7978845608028654f * 2.f * LOG2E) * __builtin_fmaf(v * v, 0.044715f, 1.0f);
  return v * __builtin_amdgcn_rcpf(1.0f + __builtin_amdgcn_exp2f(-u));
}
template <int CTRL> DI float dppadd(float v) { return v + __uint_as_float(__builtin_amdgcn_update_dpp(0u, __float_as_uint(v), CTRL, 0xf, 0xf, true)); }
DI float sum16(float v) { v = dppadd<0xB1>(v); v = dppadd<0x4E>(v); v = dppadd<0x141>(v); return dppadd<0x140>(v); }
DI float add_x16(float v) { auto r = __builtin_amdgcn_permlane16_swap(__float_as_uint(v), __float_as_uint(v), false, false); return __uint_as_float(r[0]) + __uint_as_float(r[1]); }
DI float add_x32(float v) { auto r = __builtin_amdgcn_permlane32_swap(__float_as_uint(v), __float_as_uint(v), false, false); return __uint_as_float(r[0]) + __uint_as_float(r[1]); }

DI void glds4(v4u rs, unsigned dst, int soff, int v0, int v1, int v2, int v3) {
  asm volatile("s_nop 4\n\ts_mov_b32 m0, %1\n\ts_nop 0\n\tbuffer_load_dwordx4 %3, %0, %2 offen lds\n\t"
               "s_add_u32 m0, m0, 0x400\n\ts_nop 0\n\tbuffer_load_dwordx4 %4, %0, %2 offen lds\n\t"
               "s_add_u32 m0, m0, 0x400\n\ts_nop 0\n\tbuffer_load_dwordx4 %5, %0, %2 offen lds\n\t"
               "s_add_u32 m0, m0, 0x400\n\ts_nop 0\n\tbuffer_load_dwordx4 %6, %0, %2 offen lds"
               :: "s"(rs), "s"(dst), "s"(soff), "v"(v0), "v"(v1), "v"(v2), "v"(v3) : "memory", "scc");
}
template <int IMM> DI void glds1(v4u rs, unsigned base, int soff, int v) {
  asm volatile("s_add_u32 m0, %1, %4\n\ts_nop 0\n\tbuffer_load_dwordx4 %3, %0, %2 offen lds" :: "s"(rs), "s"(base), "s"(soff), "v"(v), "i"(IMM) : "memory", "scc");
}
template <int IMM> DI void glds1_sc1(v4u rs, unsigned base, int soff, int v) {
  asm volatile("s_add_u32 m0, %1, %4\n\ts_nop 0\n\tbuffer_load_dwordx4 %3, %0, %2 offen sc1 lds" :: "s"(rs), "s"(base), "s"(soff), "v"(v), "i"(IMM) : "memory", "scc");
}
template <int IMM> DI void set_m0(unsigned base) { asm volatile("s_add_u32 m0, %0, %1" :: "s"(base), "i"(IMM) : "memory", "scc"); }
template <int OFF> DI void glds_o(v4u rs, int soff, int v) { asm volatile("buffer_load_dwordx4 %2, %0, %1 offen offset:%3 sc1 lds" :: "s"(rs), "s"(soff), "v"(v), "i"(OFF) : "memory"); }
template <int N> using IC = std::integral_constant<int, N>;
template <int N> DI void waitv() { asm volatile("s_waitcnt vmcnt(%0)" :: "i"(N) : "memory"); }
template <int I, int N, class Fn> DI void static_for(Fn&& f) { if constexpr (I < N) { f(std::integral_constant<int, I>{}); static_for<I + 1, N>(f); } }
#define PIN(x) pin(x)
#define STAMP(id)
#define STAMP_ARG
#define STAMP_PASS
#define CSTAMP(id)

#define SLP 0
DI unsigned flag_ld(rsrc_t rws, int f, int n, int l) { asm volatile("" ::: "memory"); return __builtin_amdgcn_raw_buffer_load_b32(rws, (l < n ? l : 0) * 256, f * 256, SC1); }
DI void flag_wait(unsigned v, rsrc_t rws, int f, int n, unsigned tag, int l) {
  if (__builtin_expect(!__all(v == tag), 0)) {
#pragma unroll 1
    for (int sp = 0; sp < (1 << 16); sp++) { if (__all(flag_ld(rws, f, n, l) == tag)) break; }
  }
  asm volatile("" ::: "memory");
}
DI void wait_flags(rsrc_t rws, int f, int n, unsigned tag, int l) { flag_wait(flag_ld(rws, f, n, l), rws, f, n, tag, l); }
DI void flag_st(rsrc_t rws, int f, unsigned tag, bool me, int l) {
  asm volatile("" ::: "memory"); sts1(v4u{tag, tag, tag, tag}, rws, (me && l < 8) ? l * 16 : OOB, f * 256); asm volatile("" ::: "memory");
}

template <int MAT> DI int frag_off(int T, int ks) {
  if constexpr (MAT == 0) return WOP_OFF + ((T / 5) * 100 + ks * 5 + T % 5) * 1024;
  else if constexpr (MAT == 1) return W1P_OFF + ((T / 3) * 60 + ks * 3 + T % 3) * 1024;
  else return W2P_OFF + ((T / 5) * 240 + ks * 5 + T % 5) * 1024;
}
template <int MAT> DI void pack_issue(int ks, int cb, int l, v4u rs, unsigned stg) {
  const int r = l >> 3;
  static_for<0, 4>([&](auto Ic) { constexpr int i = Ic;
    const int sw = ((l & 7) ^ ((((r >> 1) & 1) | ((i & 1) << 1)) << 1)) * 16, sg = 4 * ks + i;
    if constexpr (MAT == 0) glds1<i * 1024>(rs, stg, sg * 8 * 1280 + cb * 128, r * 1280 + sw);
    else if constexpr (MAT == 1) { const int q = ks % 10;
      glds1<i * 1024>(rs, stg, ((2 * i + ks / 10) * 80 + (q < 8 ? 32 * (q >> 2) + 4 * (q & 3) : 8 * q)) * 3072 + cb * 128, (q < 8 ? 16 * (r >> 2) + (r & 3) : r) * 3072 + sw); }
    else { const int q = sg % 6; glds1<i * 1024>(rs, stg, (48 * (sg / 6) + (q < 4 ? 4 * q : 8 * q)) * 1280 + cb * 128, (q < 4 ? 16 * (r >> 2) + (r & 3) : r) * 1280 + sw); }
  });
}
DI int unit_lane(int t, int l) { const int g = l >> 4, q = (l >> 2) & 3, p = l & 3, f = ((q >> 1) & 1) | ((g & 1) << 1); return (8 * g + q) * 128 + ((t ^ f) * 32) + p * 8; }
DI v4u unit_read(LAS char* p) { const v2u a = trread(p), b = trread(p + 512); return v4u{a.x, a.y, b.x, b.y}; }
template <int MAT> DI void pack_dst(int ks, int cb, int (&d)[4]) {
#pragma unroll
  for (int t = 0; t < 4; t++) d[t] = frag_off<MAT>(cb * 4 + t, ks);
}
DI void pack_finish(const int (&d)[4], const int (&ul)[4], int l, rsrc_t rws, LAS char* stg) {
  v4u f[4];
#pragma unroll
  for (int t = 0; t < 4; t++) f[t] = unit_read(stg + ul[t]);
#pragma unroll
  for (int t = 0; t < 4; t++) sts1(f[t], rws, l * 16, d[t]);
}
constexpr int TAB_OFF = 81920, SCR_OFF = TAB_OFF + 512, SNK_OFF = SCR_OFF + 2048, XT_OFF = 92160, XT_W = 17408, OB_ROW = 176, WO_L = 61440;
constexpr int RED_ROW = 336, RED_W = 16 * RED_ROW, RED_BUF = 4 * RED_W, A3_0 = 2 * RED_BUF, W2L_OFF = 65536;
constexpr int NL2 = 24;
static_assert(SNK_OFF + 1024 <= WO_L + 28 * 1024 && A3_0 + 20480 <= W2L_OFF, "LDS map");

#define N1 (B0 + 18)
#define N2 42
#define N3 50
#define N5 66
#define X1 2
#define X2 8
#define XROT 3
#define B0 15
#define ABL 0
#define FPK 5
#define NORIDE 0
#define RD1 6
#define PQ 50
#define PFK 8
#define OPL 0
#define ODEF 0
#define PT 1
#define C2 8
#define C3 20
#define RK 12
#define DLY 4
#define FLK 4

__global__ __launch_bounds__(256, 1) void k_fused(const void* x, const void* wqkv, const void* wo, const void* w1, const void* b1p, const void* w2,
                                                  const void* b2p, void* out, char* ws) {
  extern __shared__ __attribute__((aligned(16))) char smem[];
  LAS char* L = (LAS char*)smem;
  const unsigned Lb = (unsigned)(size_t)L;
  STAMP(0);
  const int tid = threadIdx.x, l = tid & 63, w = __builtin_amdgcn_readfirstlane(tid >> 6), c = l & 15, g = l >> 4;
  const int xg = blockIdx.x & 7, i = blockIdx.x >> 3;
  const unsigned Lx = Lb + XT_OFF + w * XT_W;
  LAS char* xt = L + XT_OFF + w * XT_W;
  {
    uint64_t pc; unsigned len;
    asm volatile("s_getpc_b64 %0\n.Lk_here:\n\ts_mov_b32 %1, .Lk_end-.Lk_here\n\ts_nop 2" : "=s"(pc), "=s"(len));
    glds1<SNK_OFF>(mkrs4((const void*)pc, len & ~15u), Lb, (i * 4 + w) * 640, l < 40 ? l * 16 : OOB);
  }
  const int grp = xg * 4 + (i >> 3), j = i & 7;
  const bool p0 = j < 5;
  const int pk = (i >> 3) * 5 + j;
  const v4u rx4 = mkrs4(x, S * D * 2), rws4 = mkrs4(ws, WS_END);
  const rsrc_t rws = mkrs(ws, WS_END);
  STAMP(19);
  const int uc = 88 + (grp * 5 + j) * 3 + w - 1;
  const bool hasc = p0 && w > 0 && uc < 480;
  const int fi = grp * 3 + j - 5;
  if (p0) {
    const v4u rw4 = mkrs4(wqkv, D * 1920 * 2);
    const int rr = l / 30, e = l - rr * 30, sec = e / 10;
    const int v0 = l < 60 ? rr * 3840 + sec * 1280 + (e - sec * 10) * 16 : OOB, s0 = (32 * pk + 8 * w) * 3840 + xg * 160;
    static_for<0, 4>([&](auto Uc) { constexpr int u = Uc; glds1<u * 1024>(rw4, Lx, s0 + u * 2 * 3840, v0); });
  }
  glds1_sc1<16384>(rws4, Lx, 0, l == 0 ? EPOCH_OFF : (l == 1 ? (p0 ? F0 + xg * 32 + pk : FP + fi) * 256 : OOB));
  int va[2];
#pragma unroll
  for (int u = 0; u < 2; u++) va[u] = (grp * 64 + w * 16 + u * 8 + (l >> 3)) * 1280 + (((l & 7) ^ (g | (u << 2))) * 16);
  auto ldx = [&](auto Qc) { constexpr int q = Qc; int ln = (q >> 1) + (p0 ? 0 : (j - 5) * 3); ln = ln >= 10 ? ln - 10 : ln;
    glds1<(q & 1) * 1024>(rx4, Lb + w * 2048 + ln * 8192, ln * 128, va[q & 1]); };
  int ul[4], dc[4];
#pragma unroll
  for (int t = 0; t < 4; t++) ul[t] = unit_lane(t, l);
  unsigned tag = 0;
  auto get_tag = [&]() { tag = __builtin_amdgcn_readfirstlane(*(LAS unsigned*)(xt + 16384)) + 1u; };
  if (p0) {
    {
    const char* tp = l < 8 ? (const char*)wo + l * 102400 : l < 24 ? (const char*)w1 + (l - 8) * 122880 : l < 40 ? (const char*)w2 + (l - 24) * 122880 : l < 56 ? (const char*)out + (l - 40) * 163840 : l < 60 ? (const char*)b1p : (const char*)b2p;
    asm volatile("s_mov_b32 m0, %1\n\ts_nop 0\n\tglobal_load_lds_dword %0, off" :: "v"(tp), "s"(Lb + SNK_OFF) : "memory");
    }
    const int q = (l >> 2) & 3, p = l & 3;
    const int tp0 = P((q >> 1) * 1024 + (q & 1) * 480 + (g == 0 ? 160 + 8 * p : g == 1 ? 320 + 8 * p : g == 2 ? (p == 0 ? 192 : p == 1 ? 352 : 32) : 8 * p));
    const int vd = P(g * 1024 + c * 16), sd = PS(WQP_OFF + (xg * 80 + pk) * 4096 + w * 256);
    pack_dst<2>(uc % 48, uc / 48, dc);
#pragma unroll
    for (int t = 0; t < 4; t++) dc[t] = PS(dc[t]);
    waitv<2>();
    STAMP(20);
    v4u f[4];
#pragma unroll
    for (int t = 0; t < 4; t++) { const v2u a = trread(xt + tp0 + 40 * t), b = trread(xt + tp0 + 40 * t + 2048); f[t] = v4u{a.x, a.y, b.x, b.y}; }
#pragma unroll
    for (int t = 0; t < 4; t++) sts1(f[t], rws, vd, sd + t * 81920);
    STAMP(1);
    static_for<0, X2>(ldx);
    waitv<X2>();
    get_tag();
    BAR(); flag_st(rws, F0 + xg * 32 + pk, tag, w == 0, l);
    STAMP(2);
    static_for<X2, 20>(ldx);
    if (hasc) pack_issue<2>(uc % 48, uc / 48, l, mkrs4(w2, D * F * 2), Lx + 4096);
  } else {
    static_for<0, 8>(ldx);
#pragma unroll 1
    for (int r = 0; r < 2; r++) { const int u = fi * 8 + w * 2 + r;
      if (u < 200) pack_issue<0>(u % 20, u / 20, l, mkrs4(wo, D * D * 2), Lx + r * 4096);
      else if (u < 680) pack_issue<1>(u % 20, u / 20 - 10, l, mkrs4(w1, D * F * 2), Lx + r * 4096);
      else pack_issue<2>((u - 680) % 48, (u - 680) / 48, l, mkrs4(w2, D * F * 2), Lx + r * 4096); }
    static_for<8, 20>(ldx);
    STAMP(20);
    waitv<12>();
    get_tag();
#pragma unroll 1
    for (int r = 0; r < 2; r++) { const int u = fi * 8 + w * 2 + r;
      int d[4];
      if (u < 200) pack_dst<0>(u % 20, u / 20, d); else if (u < 680) pack_dst<1>(u % 20, u / 20 - 10, d); else pack_dst<2>((u - 680) % 48, (u - 680) / 48, d);
      pack_finish(d, ul, l, rws, xt + r * 4096); }
    STAMP(2);
  }
  const bool mid = c >= 4 && c < 12;
  const int c4 = c < 4 ? c : 4;
  const int vk1 = P(mid ? 1024 + (12 + g * 8 + c) * 16 : OOB), vg0 = P(g == 0 ? c * 16 : OOB), vk3 = P((g == 0 && mid) ? 1024 + (44 + c) * 16 : OOB);
  const int vsc = P(c >= 12 ? SCR_OFF + w * 512 + g * 64 + (c - 12) * 16 : SNK_OFF + l * 8);
  const int vpp = P(g * 2048 + ((xg == 7 && c >= 8) ? 7 : c) * 16), vf2 = P(1024 + (16 + g * 8 + c4) * 16), vf3 = P(1024 + (48 + c4) * 16);
  const int vtc = P(l < 28 ? (l < 16 ? FE + grp * 128 + (l >> 2) * 32 + j * 4 + (l & 3) : (l < 20 ? FC : FD) + grp * 32 + (l & 3) * 8 + j) * 256 + (l >= 24 ? 128 : 0) : OOB);
  const int vhf = P((g & 1) ? OOB : (g >> 1) * 256 + c * 16);
  LAS char* ap[2];
#pragma unroll
  for (int kk = 0; kk < 2; kk++) ap[kk] = L + c * 128 + (((4 * kk + g) ^ ((c >> 1) & 7)) * 16);
  auto rdx = [&](int ks, int m) { return *(LAS v4u*)(ap[ks & 1] + (ks >> 1) * 8192 + m * 2048); };
  auto ldf = [&](int base, int q) { return lds1(rws, l * 16 + (q & 3) * 1024, base + (q >> 2) * 4096); };
  const int hd = 4 * j + w;
  v4u bq[80];
  auto ldq = [&](auto Qc) { constexpr int q = Qc, f = q < 60 ? (q / 3) * 4 + 2 - q % 3 : (q - 60) * 4 + 3; bq[f] = ldf(WQP_OFF + hd * 81920, f); };
  STAMP(3);
  if (w == 0) wait_flags(rws, F0 + j * 32, 20, tag, l);
  BAR();
  STAMP(4);
  static_for<0, B0 - 10>(ldq); SB();
  if (hasc) waitv<B0 - 6>(); else if (p0) waitv<B0 - 10>(); else waitv<B0 - 2>();
  {
    v4u a[20];
#pragma unroll
    for (int ks = 0; ks < 20; ks++) a[ks] = *(LAS v4u*)(ap[ks & 1] + (ks >> 1) * 8192 + w * 2048);
    SB();
    float s[2] = {0.f, 0.f}, q[2] = {0.f, 0.f};
    static_for<0, 20>([&](auto Kc) { constexpr int ks = Kc;
      if constexpr (ks % 2 == 0) ldq(IC<B0 - 10 + ks / 2>{});
#pragma unroll
      for (int e = 0; e < 4; e++) { s[e & 1] = dot2(a[ks][e], ONES2, s[e & 1]); q[e & 1] = dot2(a[ks][e], a[ks][e], q[e & 1]); }
      SB(); });
    const float mean = add_x32(add_x16(s[0] + s[1])) * (1.f / D);
    const float rs = __builtin_amdgcn_rsqf(fmaxf(add_x32(add_x16(q[0] + q[1])) * (1.f / D) - mean * mean, 0.f) + 1e-5f);
    *(LAS float*)(L + TAB_OFF + (g & 1) * 256 + (w * 16 + c) * 4) = (g & 1) ? -mean * rs : rs;
  }
  if (!p0) waitv<B0>();
  BAR();
  if (!p0) flag_st(rws, FP + fi, tag, w == 0, l);
  v4u ones = {ONES2, ONES2, ONES2, ONES2};
  PIN(ones);
  v4u fa[2][4];
  unsigned fp = 0;
  const int fpa = FP + w * 24, fpn = 24;
  v4u bw1[60], bw2[60];
  const int w1b = W1P_OFF + hd * 61440, w2b = W2P_OFF + hd * 61440;
  auto ldw2 = [&](auto Qc) { constexpr int q = Qc; if constexpr (q >= 0 && q < 60) bw2[q] = ldf(w2b, q); };
  auto bulk = [&](auto Qc) { constexpr int q = Qc; if constexpr (q < 60) bw1[q] = ldf(w1b, q); else ldw2(IC<q - 60>{}); };
  auto run = [&](auto Nc, unsigned lds, int mem) { static_for<0, Nc>([&](auto Qc) { constexpr int q = Qc;
    if constexpr (q % 4 == 0) { set_m0<q * 1024>(lds); asm volatile("s_nop 0"); }
    glds_o<(q % 4) * 1024>(rws4, mem + (q / 4) * 4096, l * 16); }); };
#define W1F(r) bw1[r]
  {
    f32x4 acc[4][3], cs[3], trs[4], tnm[4];
    LAS char* tabp = L + TAB_OFF + g * 16;
#pragma unroll
    for (int m = 0; m < 4; m++) fa[0][m] = rdx(0, m);
    SB();
    static_for<0, 20>([&](auto Kc) { constexpr int ks = Kc;
      if constexpr (ks == PFK) { if (hasc) pack_finish(dc, ul, l, rws, xt + 4096); SB(); }
      if constexpr (ks == 19) { flag_wait(fp, rws, fpa, fpn, tag, l); BAR(); flag_st(rws, FP + 96 + grp * 5 + j, tag, p0 && w == 0, l); }
      if constexpr (ks == 5) STAMP(23);
      static_for<0, 15>([&](auto Qc) { constexpr int Q = Qc, n = Q / 5, m = Q % 5;
        if constexpr (m < 4) { if constexpr (!(ABL & 4) || m == 0 || ks == 0) mfma_w<ks == 0, false, true>(fa[ks & 1][m], bq[ks * 4 + n], acc[m][n]); } else if constexpr (!(ABL & 2) || ks == 0) mfma_w<ks == 0, false, true>(ones, bq[ks * 4 + n], cs[n]);
        if constexpr (Q >= 1 && Q < 9 && (Q & 1)) fa[(ks + 1) & 1][3 - Q / 2] = rdx(ks < 19 ? ks + 1 : 0, 3 - Q / 2);
        if constexpr (m == 2 && B0 + ks * 3 + n < 80) ldq(IC<B0 + ks * 3 + n>{});
        if constexpr (ks == 15 && Q == 0) fp = flag_ld(rws, fpa, fpn, l);
        if constexpr (ks >= 18 && Q >= 9 && Q < 13) (ks == 18 ? trs : tnm)[Q - 9] = *(LAS f32x4*)(tabp + (ks - 18) * 256 + (Q - 9) * 64);
        SB(); });
    });
    static_assert(B0 >= 12 && B0 <= 20, "");
    asm volatile("s_nop 7");
#pragma unroll
    for (int n = 0; n < 3; n++) pin(cs[n]);
    STAMP(5);
    v4u fk[2], fv[2], fr[2], fb[2];
    const f32x2 one2 = {1.f, 1.f}, mk = c < 8 ? one2 : f32x2{0.f, 0.f}, ck = c < 8 ? -one2 : one2;
    auto ride0 = [&](auto Ec) { if constexpr (B0 + 60 + Ec < 80) ldq(IC<B0 + 60 + Ec>{}); else bulk(IC<B0 + Ec - 20>{}); };
    auto ride = [&](auto Ec) { if constexpr (!NORIDE) ride0(Ec); };
    auto unused_ = [&](auto Ec) { if constexpr (B0 + 60 + Ec < 80) ldq(IC<B0 + 60 + Ec>{}); else bulk(IC<B0 + Ec - 20>{}); };
    static_for<0, 2>([&](auto Uc) { constexpr int u = Uc;
      f32x2 t[3][4], uu[8];
      unsigned o[8];
#pragma unroll
      for (int e = 0; e < 4; e++) { const f32x2 nm = (e & 1) ? hi2(tnm[2 * u + (e >> 1)]) : lo2(tnm[2 * u + (e >> 1)]);
        t[0][e] = P(fma2(nm, lo2(cs[0]), one2)); t[1][e] = P(nm * lo2(cs[1])); t[2][e] = P(fma2(nm, lo2(cs[2]), one2)); }
      ride(IC<4 * u>{});
      SB();
#pragma unroll
      for (int e = 0; e < 4; e++) { const int m = 2 * u + (e >> 1); const f32x2 rs = (e & 1) ? hi2(trs[m]) : lo2(trs[m]);
        uu[e] = fma2(rs, (e & 1) ? hi2(acc[m][0]) : lo2(acc[m][0]), t[0][e]); uu[4 + e] = fma2(rs, (e & 1) ? hi2(acc[m][2]) : lo2(acc[m][2]), t[2][e]);
        t[1][e] = fma2(rs, (e & 1) ? hi2(acc[m][1]) : lo2(acc[m][1]), t[1][e]); }
      ride(IC<4 * u + 1>{});
      SB();
#pragma unroll
      for (int e = 0; e < 4; e++) { t[2][e] = fma2(uu[4 + e], mk, ck); fv[u][e] = pk2v(t[1][e]); }
      ride(IC<4 * u + 2>{});
      phi_n<8>(uu, o);
#pragma unroll
      for (int e = 0; e < 4; e++) { fb[u][e] = pk2v(t[2][e]); fk[u][e] = o[e]; fr[u][e] = o[4 + e]; }
      ride(IC<4 * u + 3>{});
      SB();
    });
#pragma unroll
    for (int u = 0; u < 2; u++) { pin(fk[u]); pin(fv[u]); pin(fr[u]); pin(fb[u]); }
    asm volatile("s_nop 1");
    f32x4 t00, t01, t10, t11;
    mfma_z(fk[0], fv[0], t00); mfma_z(fk[0], fb[0], t01); mfma_z(fr[0], fv[0], t10); mfma_z(fr[0], fb[0], t11);
    mfma_t(fk[1], fv[1], t00); mfma_t(fk[1], fb[1], t01); mfma_t(fr[1], fv[1], t10); mfma_t(fr[1], fb[1], t11);
#pragma unroll
    for (int u = 0; u < 2; u++) *(LAS v4u*)(L + (vsc & ~15) + u * 256) = fr[u];
    asm volatile("s_nop 5");
    SB();
    const int kvo = KVP_OFF + (hd * 32 + grp) * 2048;
    sts1(v4u{pk2(t00[0], t00[1]), tag, pk2(t00[2], t00[3]), tag}, rws, l * 16, kvo);
    sts1(v4u{pk2(t01[0], t01[1]), tag, pk2(t01[2], t01[3]), tag}, rws, vk1, kvo);
    sts1(v4u{pk2(t10[0], t10[1]), tag, pk2(t10[2], t10[3]), tag}, rws, vg0, kvo + 1024);
    sts1(v4u{pk2(t11[0], t11[1]), tag, pk2(t11[2], t11[3]), tag}, rws, vk3, kvo);
    STAMP(6);
    if constexpr (NORIDE) static_for<0, 8>(ride0);
  }
  if (w == ((grp + 1) & 3)) glds1<SNK_OFF>(rws4, Lb, 0, vtc);
  const bool act = c < 10;
  v2u xr[5];
  {
    f32x4 acc[4], cs;
    float qrs[4], qnm[4];
    v2u q1[4];
    static_for<0, 21>([&](auto Kc) { constexpr int ks = Kc;
      if constexpr (ks == RK) {
        STAMP(7);
        if (w == (grp & 3)) {
          v4u pp[8];
#pragma unroll 1
          for (int sp = 0; sp < (1 << 16); sp++) {
            asm volatile("" ::: "memory");
#pragma unroll
            for (int q = 0; q < 8; q++) pp[q] = lds1(rws, vpp, KVP_OFF + (hd * 32 + 4 * q) * 2048 + xg * 256);
            SB();
            unsigned bad = 0;
#pragma unroll
            for (int q = 0; q < 8; q++) bad |= (pp[q].y ^ tag) | (pp[q].w ^ tag);
            if (__all(bad == 0)) break;
          }
          float sm[4] = {0.f, 0.f, 0.f, 0.f};
#pragma unroll
          for (int q = 0; q < 8; q++) { sm[0] += bflo(pp[q].x); sm[1] += bfhi(pp[q].x); sm[2] += bflo(pp[q].z); sm[3] += bfhi(pp[q].z); }
#pragma unroll
          for (int e = 0; e < 4; e++) sm[e] = add_x32(add_x16(sm[e]));
          sts1(v4u{pk2(sm[0], sm[1]), tag, pk2(sm[2], sm[3]), tag}, rws, vg0, KVF_OFF + hd * 2048 + xg * 256);
        } else { const int f = (28 + ((w - grp - 1) & 3) * 24) * 1024; run(IC<24>{}, Lb + WO_L + f, WOP_OFF + j * 102400 + f); }
        SB();
        STAMP(8);
      }
      if constexpr (ks < 20) static_for<0, 5>([&](auto Qc) { constexpr int m = Qc;
        if constexpr (m < 4) mfma_a<ks == 0>(bq[ks * 4 + 3], fa[ks & 1][m], acc[m]); else mfma_a<ks == 0>(bq[ks * 4 + 3], ones, cs);
        if constexpr (ks < 19 && m < 4) fa[(ks + 1) & 1][3 - m] = rdx(ks + 1, 3 - m);
        if constexpr (m == 4 || (m == 1 && (ks & 1))) bulk(IC<B0 - 12 + (ks / 2) * 3 + (ks & 1) + (m == 4 && (ks & 1))>{});
        SB(); });
    });
    {
      LAS char* qp = L + SCR_OFF + w * 512 + (l & 15) / 4 * 16 + (l & 3) * 64;
#pragma unroll
      for (int m = 0; m < 4; m++) {
        qrs[m] = *(LAS float*)(L + TAB_OFF + c * 4 + m * 64); qnm[m] = *(LAS float*)(L + TAB_OFF + c * 4 + 256 + m * 64);
        q1[m] = trread(qp + (m >> 1) * 256 + (m & 1) * 8);
      }
#pragma unroll
      for (int n = 0; n < 5; n++) { const int sg = 10 * j + 2 * n + (g >> 1);
        xr[n] = *(LAS v2u*)(L + (sg >> 3) * 8192 + (w * 16 + c) * 128 + (((sg & 7) ^ ((c >> 1) & 7)) * 16) + (g & 1) * 8); }
    }
    asm volatile("s_nop 7");
    SB();
    STAMP(9);
    v4u ka0, ka1, kb0, kb1;
    auto kv_ld = [&]() { asm volatile("" ::: "memory");
      ka0 = lds1(rws, l * 16, KVF_OFF + hd * 2048); ka1 = lds1(rws, 1024 + c * 16, KVF_OFF + hd * 2048);
      kb0 = lds1(rws, vf2, KVF_OFF + hd * 2048); kb1 = lds1(rws, vf3, KVF_OFF + hd * 2048); };
    auto kv_ok = [&]() { return __all(((ka0.y ^ tag) | (ka0.w ^ tag) | (ka1.y ^ tag) | (ka1.w ^ tag) | (kb0.y ^ tag) | (kb0.w ^ tag) | (kb1.y ^ tag) | (kb1.w ^ tag)) == 0); };
    const unsigned fq = flag_ld(rws, FP + 96 + w * 33, 33, l);
    static_for<N1, N2>(bulk); SB();
    kv_ld();
    v4u pq[4];
    {
      f32x2 uu[8];
      unsigned o[8];
#pragma unroll
      for (int e = 0; e < 8; e++) uu[e] = P(fma2(f32x2{qnm[e >> 1], qnm[e >> 1]}, (e & 1) ? hi2(cs) : lo2(cs), f32x2{1.f, 1.f}));
      SB();
#pragma unroll
      for (int e = 0; e < 8; e++) uu[e] = P(fma2(f32x2{qrs[e >> 1], qrs[e >> 1]}, (e & 1) ? hi2(acc[e >> 1]) : lo2(acc[e >> 1]), uu[e]));
      SB();
      phi_n<8>(uu, o);
#pragma unroll
      for (int m = 0; m < 4; m++) { pq[m] = v4u{o[2 * m], o[2 * m + 1], q1[m].x, q1[m].y}; pin(pq[m]); }
    }
    flag_wait(fq, rws, FP + 96 + w * 33, 33, tag, l);
    BAR();
    STAMP(10);
    if (__builtin_expect(!kv_ok(), 0)) {
#pragma unroll 1
      for (int sp = 0; sp < (1 << 16); sp++) { kv_ld(); if (kv_ok()) break; }
    }
    STAMP(24);
    v4u kf0 = {ka0.x, ka0.z, g == 3 ? ka1.x : 0u, g == 3 ? ka1.z : 0u}, kf1 = {kb0.x, kb0.z, g == 3 ? kb1.x : 0u, g == 3 ? kb1.z : 0u};
    pin(kf0); pin(kf1);
    asm volatile("s_nop 1");
    f32x4 n0[4], n1[4];
    LAS char* ob0 = L + c * OB_ROW + w * 40 + g * 8;
    LAS char* ob1 = L + c * OB_ROW + (g == 0 ? w * 40 + 32 : 160);
#pragma unroll
    for (int m = 0; m < 4; m++) { mfma_z(kf0, pq[m], n0[m]); mfma_z(kf1, pq[m], n1[m]); }
    asm volatile("s_nop 7");
    SB();
    float rr[4];
#pragma unroll
    for (int m = 0; m < 4; m++) rr[m] = __uint_as_float(__builtin_amdgcn_permlane16_swap(__float_as_uint(n1[m][0]), __float_as_uint(n1[m][0]), false, false)[1]);
    SB();
#pragma unroll
    for (int m = 0; m < 4; m++) rr[m] = P(rr[m] + 1e-6f);
    SB();
#pragma unroll
    for (int m = 0; m < 4; m++) rr[m] = P(__builtin_amdgcn_rcpf(rr[m]));
    SB();
#pragma unroll
    for (int m = 0; m < 4; m++) { n0[m] = P(n0[m] * rr[m]); n1[m] = P(n1[m] * rr[m]); }
    SB();
#pragma unroll
    for (int m = 0; m < 4; m++) {
      *(LAS v2u*)(ob0 + m * 16 * OB_ROW) = v2u{pk2v(lo2(n0[m])), pk2v(hi2(n0[m]))};
      *(LAS v2u*)(ob1 + m * 16 * OB_ROW) = v2u{pk2v(lo2(n1[m])), pk2v(hi2(n1[m]))};
    }
    BAR();
    v4u o[3];
#pragma unroll
    for (int q = 0; q < 3; q++) o[q] = *(LAS v4u*)(L + (w * 16 + c) * OB_ROW + (q * 4 + (q == 2 ? g & 1 : g)) * 16);
#pragma unroll
    for (int q = 0; q < 3; q++) sts1(o[q], rws, (q < 2 || g < 2) ? l * 16 + q * 1024 : OOB, OF_OFF + (grp * 4 + w) * 20480 + j * 2560);
    STAMP(25);
    SB();
    static_for<N2, N3>(bulk); SB();
    waitv<N3 - N2>();
    flag_st(rws, FC + grp * 32 + w * 8 + j, tag, true, l);
    run(IC<7>{}, Lb + WO_L + w * 7168, WOP_OFF + j * 102400 + w * 7168);
    STAMP(11);
  }
  LAS char* rdst = L + w * RED_W + c * RED_ROW + g * 16;
  LAS char* rsrc = L + (4 * w + g) * RED_ROW + (act ? c : 9) * 32;
  const int rowo = act ? (grp * 64 + 4 * w + g) * 1280 + j * 160 + c * 16 : OOB;
  auto red_ld = [&](int m, f32x4 (&r)[8]) {
#pragma unroll
    for (int ww = 0; ww < 4; ww++) { LAS f32x4* p = (LAS f32x4*)(rsrc + (m & 1) * RED_BUF + ww * RED_W); r[2 * ww] = p[0]; r[2 * ww + 1] = p[1]; }
  };
  v4u hs[4][2], hk[4];
  float hsa[4], hsb[4];
  const int hsv = g * 512 + c * 16;
  auto h_poll = [&](int m) { asm volatile("" ::: "memory");
    hs[m][0] = lds1(rws, hsv, (FD + grp * 32 + m * 8) * 256); hs[m][1] = lds1(rws, hsv + 256, (FD + grp * 32 + m * 8) * 256); };
  auto h_ok = [&](int m) { return __all(((hs[m][0].y ^ tag) | (hs[m][0].w ^ tag) | (hs[m][1].y ^ tag) | (hs[m][1].w ^ tag)) == 0); };
  auto h_wait = [&](int m) {
    if (__builtin_expect(!h_ok(m), 0)) {
#pragma unroll 1
      for (int sp = 0; sp < (1 << 16); sp++) { h_poll(m); if (h_ok(m)) break; }
    }
    asm volatile("" ::: "memory");
    hsa[m] = P(__uint_as_float(hs[m][0].x) + __uint_as_float(hs[m][1].x)); hsb[m] = P(__uint_as_float(hs[m][0].z) + __uint_as_float(hs[m][1].z));
  };
  v2u bias[3];
  const unsigned La = Lb + w * 5120;
  const int hso = HF_OFF + grp * 81920 + w * 5120;
  auto ldh = [&](auto Mc, auto Qc) { constexpr int m = Mc, q = Qc; glds1_sc1<((m & 1) ? 0 : A3_0) + q * 1024>(rws4, La, hso + m * 20480 + q * 1024, l * 16); };
  f32x4 cs[3];
  {
    const rsrc_t rb1 = mkrs(b1p, F * 2);
#pragma unroll
    for (int n = 0; n < 3; n++) bias[n] = __builtin_amdgcn_raw_buffer_load_b64(rb1, ((12 * j + 3 * w + n) * 16 + 4 * g) * 2, 0, 0);
    static_for<N3, N5>(bulk); SB();
    wait_flags(rws, FC + grp * 32 + w * 8, 8, tag, l);
    STAMP(12);
    v4u a[20], bo[100];
    auto lda = [&](auto Qc) { constexpr int q = Qc; a[q] = ldf(OF_OFF + (grp * 4 + w) * 20480, q); };
    static_for<0, 12>(lda);
    BAR();
    LAS char* wp[2] = {L + WO_L + l * 16, L + WO_L + 51200 + l * 16};
    auto rdo = [&](int f) { return *(LAS v4u*)(wp[f / 50] + (f % 50) * 1024); };
#pragma unroll
    for (int f = 0; f < 10; f++) bo[f] = rdo(f);
    SB();
    f32x4 acc[5];
    static_for<0, 100>([&](auto Fc) { constexpr int f = Fc, k = f / 5, n = f % 5;
      mfma_a<k == 0>(bo[f], a[k], acc[n]);
      if constexpr (k < 18) bo[(k + 2) * 5 + 4 - n] = rdo((k + 2) * 5 + 4 - n);
      if constexpr (n == 2 && k + 12 < 20) lda(IC<k + 12>{});
      SB(); });
    STAMP(13);
    asm volatile("s_nop 7");
    SB();
    unsigned hp[10];
    float s[2] = {0.f, 0.f}, q[2] = {0.f, 0.f};
#pragma unroll
    for (int n = 0; n < 5; n++) {
      hp[2 * n] = pk2v(lo2(acc[n]) + f32x2{bflo(xr[n].x), bfhi(xr[n].x)}); hp[2 * n + 1] = pk2v(hi2(acc[n]) + f32x2{bflo(xr[n].y), bfhi(xr[n].y)});
    }
    const int hb = HF_OFF + (grp * 4 + w) * 20480 + j * 2560;
    sts1(v4u{hp[0], hp[1], hp[2], hp[3]}, rws, l * 16, hb);
    sts1(v4u{hp[4], hp[5], hp[6], hp[7]}, rws, l * 16 + 1024, hb);
    auto s0 = __builtin_amdgcn_permlane16_swap(hp[8], hp[8], false, false);
    auto s1 = __builtin_amdgcn_permlane16_swap(hp[9], hp[9], false, false);
    sts1(v4u{s0[0], s1[0], s0[1], s1[1]}, rws, vhf, hb + 2048);
    STAMP(14);
#pragma unroll
    for (int e = 0; e < 10; e++) { s[e & 1] = dot2(hp[e], ONES2, s[e & 1]); q[e & 1] = dot2(hp[e], hp[e], q[e & 1]); }
    const v4u hst = {__float_as_uint(add_x32(add_x16(s[0] + s[1]))), tag, __float_as_uint(add_x32(add_x16(q[0] + q[1]))), tag};
    static_for<N5, N5 + RD1>(bulk); SB();
    waitv<RD1>();
    sts1(hst, rws, vg0, (FD + grp * 32 + w * 8 + j) * 256);
    BAR();
    STAMP(15);
    static_assert(N5 >= 60, "w1 has landed");
    asm volatile("s_nop 1");
    static_for<0, 60>([&](auto Qc) { constexpr int q = Qc; mfma_w<(q < 3), true>(W1F(q), ones, cs[q % 3]);
      if constexpr (q % 5 == 0 && q < PQ) bulk(IC<N5 + RD1 + q / 5>{});
      if constexpr (q == PQ) { h_poll(0); h_poll(1); }
      SB(); });
    h_wait(0); h_wait(1);
    STAMP(16);
    run(IC<5>{}, La + A3_0, hso);
    h_poll(2); h_poll(3);
    static_for<N5 + RD1 + PQ / 5, 92>(bulk); SB();
    h_wait(2); h_wait(3);
    run(IC<5>{}, La, hso + 20480);
  }
  {
    const int few = FE + grp * 128, feo = few + j * 4 + w;
    unsigned fs = 0;
    f32x2 bf[6];
#pragma unroll
    for (int e = 0; e < 6; e++) bf[e] = P(f32x2{bflo(bias[e >> 1][e & 1]), bfhi(bias[e >> 1][e & 1])});
    SB();
    BAR();
    STAMP(17);
    LAS char* ab = L + g * 5120 + c * 16;
    auto rda = [&](int t) { return *(LAS v4u*)(ab + (((t / 20) & 1) ? 0 : A3_0) + (t % 20) * 256); };
    const int cc = act ? c : 0;
    LAS char* hr0 = L + j * 2560 + (4 * w + g) * 16 + (cc < 8 ? ((cc >> 2) * 4 + (cc & 1) * 2) * 256 + ((cc >> 1) & 1) * 8 : cc * 256);
    LAS char* hr1 = hr0 + (cc < 8 ? 256 : 8);
    const unsigned L2w = Lb + W2L_OFF + w * (NL2 * 1024);
    constexpr int RD = 6;
    v4u af[RD];
    f32x4 acc[3];
    f32x2 bp[6], gv[6], gu[6];
    unsigned gp[6];
    float rs = 0.f, nr = 0.f, sa = 0.f, sb = 0.f;
    auto stats = [&](int m, auto Pc) { constexpr int p = Pc;
      if constexpr (p == 1) { auto x = __builtin_amdgcn_permlane16_swap(__float_as_uint(hsa[m]), __float_as_uint(hsb[m]), false, false); sa = P(__uint_as_float(x[0]) + __uint_as_float(x[1])); sb = P(sa); }
      if constexpr (p == 2) { auto x = __builtin_amdgcn_permlane32_swap(__float_as_uint(sa), __float_as_uint(sb), false, false); sa = P(__uint_as_float(x[0]) + __uint_as_float(x[1])); sb = P(sa); }
      if constexpr (p == 3) { auto x = __builtin_amdgcn_permlane16_swap(__float_as_uint(sa), __float_as_uint(sb), false, false);
        sa = P(__uint_as_float(x[0]) * (1.f / D)); sb = P(fmaxf(__uint_as_float(x[1]) * (1.f / D) - sa * sa, 0.f)); }
      if constexpr (p == 4) { const float r1 = __builtin_amdgcn_rsqf(sb + 1e-5f);
        rs = P(r1 * __builtin_amdgcn_rsqf(sb * r1 * r1 + 1e-5f));
        nr = P(-sa * rs); }
    };
    auto epi1 = [&]() {
#pragma unroll
      for (int e = 0; e < 6; e++) bp[e] = P(fma2(f32x2{nr, nr}, (e & 1) ? hi2(cs[e >> 1]) : lo2(cs[e >> 1]), bf[e]));
      asm volatile("s_nop 1");
      SB();
      constexpr float C0 = -0.7978845608028654f * 2.f * LOG2E, C1 = C0 * 0.044715f;
      const f32x2 rs2 = {rs, rs};
#pragma unroll
      for (int e = 0; e < 6; e++) gv[e] = P(fma2(rs2, (e & 1) ? hi2(acc[e >> 1]) : lo2(acc[e >> 1]), bp[e]));
      SB();
#pragma unroll
      for (int e = 0; e < 6; e++) gu[e] = P(gv[e] * gv[e]);
      SB();
#pragma unroll
      for (int e = 0; e < 6; e++) gu[e] = P(fma2(gu[e], f32x2{C1, C1}, f32x2{C0, C0}));
      SB();
#pragma unroll
      for (int e = 0; e < 6; e++) gu[e] = P(gu[e] * gv[e]);
      SB();
    };
    auto gstore = [&](int m) {
      const int base = GF_OFF + (grp * 4 + m) * 49152 + 6 * hd * 256;
      sts1(v4u{gp[0], gp[1], gp[2], gp[3]}, rws, l * 16, base);
      auto s0 = __builtin_amdgcn_permlane16_swap(gp[4], gp[4], false, false);
      auto s1 = __builtin_amdgcn_permlane16_swap(gp[5], gp[5], false, false);
      sts1(v4u{s0[0], s1[0], s0[1], s1[1]}, rws, vhf, base + 1024);
    };
    auto egap = [&](auto Mc, auto Gc) { constexpr int G = Gc;
      if constexpr (G < 18 && G % 3 < 2) { constexpr int e = G / 3, z = G % 3; gu[e][z] = P(__builtin_amdgcn_exp2f(gu[e][z])); }
      if constexpr (G >= 2 && G < 20 && G % 3 == 2) { constexpr int e = G / 3; gu[e][0] = P(gu[e][0] + 1.0f); SB(); gu[e][1] = P(gu[e][1] + 1.0f); }
      if constexpr (G >= 18 && G < 36 && G % 3 < 2) { constexpr int e = (G - 18) / 3, z = G % 3; gu[e][z] = P(__builtin_amdgcn_rcpf(gu[e][z])); }
      if constexpr (G >= 20 && G < 38 && G % 3 == 2) { constexpr int e = (G - 20) / 3; gv[e][0] = P(gv[e][0] * gu[e][0]); SB(); gv[e][1] = P(gv[e][1] * gu[e][1]); }
      if constexpr (G >= 36 && G < 39 + 1 && G != 38) { constexpr int e = (G == 39 ? 2 : G - 36) * 2; gp[e] = P(pk2v(gv[e])); gp[e + 1] = P(pk2v(gv[e + 1])); }
      if constexpr (G == 40) gstore(Mc);
    };
    v4u a4[48];
    auto lda4 = [&](int q) { return ldf(GF_OFF + ((grp * 4 + q / 12) * 48 + w * 12) * 1024, q % 12); };
    const rsrc_t ro = mkrs(out, S * D * 2), rb2 = mkrs(b2p, D * 2);
    v4u br;
#pragma unroll
    for (int q = 0; q < RD; q++) af[q] = rda(q);
    SB();
    asm volatile("s_nop 1");
    auto rider = [&](auto Qc) { constexpr int q = Qc; if constexpr (q < NL2) glds1_sc1<q * 1024>(rws4, L2w, w2b + (60 - NL2 + q) * 1024, l * 16); };
    static_for<0, 80>([&](auto Tc) {
      constexpr int T = Tc, m = T / 20, k = T % 20;
      if constexpr (k == 14 && m < 3) {
        waitv<(m == 0 ? 14 : m == 1 ? 9 : 6)>();
        BAR();
      }
      if constexpr (k == 0) STAMP(26 + m);
      if constexpr (T == 66) flag_wait(fs, rws, few, 32, tag, l);
      const v4u cur = af[T % RD];
      static_for<0, 3>([&](auto Nc) {
        constexpr int n = Nc, G = k * 3 + n;
        mfma_w<k == 0, true>(W1F(G), cur, acc[n]);
        if constexpr (m > 0) egap(IC<m - 1>{}, IC<G>{});
        if constexpr (n == 2 && (T & 1) && T + RD < 80) { af[T % RD] = rda(T + RD); SB(); af[(T - 1) % RD] = rda(T + RD - 1); }
        if constexpr (G == 41) { const v2u u0 = *(LAS v2u*)(hr0 + ((m & 1) ? 0 : A3_0)), u1 = *(LAS v2u*)(hr1 + ((m & 1) ? 0 : A3_0)); hk[m] = v4u{u0.x, u0.y, u1.x, u1.y}; }
        if constexpr (m < 2 && n == 0 && k >= 14 && k < 19) ldh(IC<m + 2>{}, IC<k - 14>{});
        if constexpr (m == 0 && n == 1 && k < 14) rider(IC<k>{});
        if constexpr (m > 0 && m < 3 && n == 1 && (k & 1) == 0 && k < 14) rider(IC<14 + (m - 1) * 7 + k / 2>{});
        if constexpr (G >= 46 && G < 58 && G % 3 == 1) stats(m, IC<(G - 43) / 3>{});

        if constexpr (n == 0 && T == 60) br = ldp(rb2, act ? j * 160 + c * 16 : OOB, 0);
        if constexpr (n == 1 && T >= 62 && T < 66) ldw2(IC<32 + T - 62>{});
        if constexpr (n == 0 && T == 61) fs = flag_ld(rws, few, 32, l);
        if constexpr (n == 1 && T == 74) fs = flag_ld(rws, few + 32, 32, l);
        if constexpr (n == 0 && T >= 66 && T < 78) a4[T - 66] = lda4(T - 66);
        SB();
      });
      if constexpr (k == 19) { epi1(); if constexpr (m >= 1) { waitv<(m == 1 ? 5 : m == 2 ? 0 : 5)>(); flag_st(rws, feo + (m - 1) * 32, tag, true, l); } }
    });
    flag_wait(fs, rws, few + 32, 32, tag, l);
#pragma unroll
    for (int e = 0; e < 6; e++) { gu[e] = P(f32x2{__builtin_amdgcn_exp2f(gu[e][0]), __builtin_amdgcn_exp2f(gu[e][1])}); if (e % 3 == 0) a4[12 + e / 3] = lda4(12 + e / 3); SB(); }
#pragma unroll
    for (int e = 0; e < 6; e++) gu[e] = P(gu[e] + f32x2{1.f, 1.f});
    SB();
#pragma unroll
    for (int e = 0; e < 6; e++) { gu[e] = P(f32x2{__builtin_amdgcn_rcpf(gu[e][0]), __builtin_amdgcn_rcpf(gu[e][1])}); if (e % 3 == 0) a4[14 + e / 3] = lda4(14 + e / 3); SB(); }
#pragma unroll
    for (int e = 0; e < 6; e++) gu[e] = P(gv[e] * gu[e]);
    SB();
#pragma unroll
    for (int e = 0; e < 6; e++) gp[e] = pk2v(gu[e]);
    gstore(3);
    SB();
    STAMP(18);
    f32x4 ac4[2][5], r[8];
    float b2f[8], ov[8], ow[2];
#pragma unroll
    for (int e = 0; e < 8; e++) b2f[e] = (e & 1) ? bfhi(br[e >> 1]) : bflo(br[e >> 1]);
    auto fop = [&](auto Mc, auto Pc, auto Sc) { constexpr int m = Mc, st = Sc;
      static_for<0, 2>([&](auto Zc) { constexpr int z = Zc, e = 2 * Pc + z;
        if constexpr (st == 0) ov[e] = P(r[e >> 2][e & 3] + r[2 + (e >> 2)][e & 3]);
        if constexpr (st == 1) ow[z] = P(r[4 + (e >> 2)][e & 3] + r[6 + (e >> 2)][e & 3]);
        if constexpr (st == 2) ov[e] = P(ov[e] + ow[z]);
        if constexpr (st == 3) ow[z] = P(z ? bfhi(hk[m][e >> 1]) : bflo(hk[m][e >> 1]));
        if constexpr (st == 4) ow[z] = P(ow[z] + b2f[e]);
        if constexpr (st == 5) ov[e] = P(ov[e] + ow[z]);
        SB(); });
    };
    auto opk = [&](int m) { hk[m] = P(v4u{pk2(ov[0], ov[1]), pk2(ov[2], ov[3]), pk2(ov[4], ov[5]), pk2(ov[6], ov[7])}); };
    auto ost = [&](int m) { if (OPL) { __builtin_amdgcn_raw_buffer_store_b128(hk[m], ro, rowo, m * 20480, 0); asm volatile("s_nop 1" :: "v"(hk[m]) : "memory"); } else sts1(hk[m], ro, rowo, m * 20480); };
    SB();
    static_for<0, 48>([&](auto Tc) {
      constexpr int T = Tc, m = T / 12, k = T % 12;
      if constexpr (k == 0) STAMP(34 + 2 * m);
      if constexpr (T == C2 || T == C3) flag_wait(fs, rws, few + (T == C2 ? 64 : 96), 32, tag, l);
      if constexpr (k == 4) BAR();
      const v4u cur = a4[T];
      static_for<0, 5>([&](auto Nc) { constexpr int n = Nc, G = k * 5 + n;
        mfma_w<k == 0, (G >= 32 || G < 3), true>(bw2[G], cur, ac4[m & 1][n]);
        if constexpr (m == 0 && G < NL2) bw2[60 - NL2 + G] = *(LAS v4u*)(L + W2L_OFF + w * (NL2 * 1024) + G * 1024 + l * 16);
        if constexpr (m > 0 && G >= 3 && G < 18 && G % 3 == 0) *(LAS f32x4*)(rdst + ((m - 1) & 1) * RED_BUF + (G / 3 - 1) * 64) = ac4[(m - 1) & 1][G / 3 - 1];
        if constexpr (m > 0 && G >= 20 && G < 28) { constexpr int q = ((G - 20) & 3) * 2 + ((G - 20) >> 2); r[q] = ((LAS f32x4*)(rsrc + ((m - 1) & 1) * RED_BUF + (q >> 1) * RED_W))[q & 1]; }
        if constexpr (m > 0 && G >= 32 && G < 56) fop(IC<m - 1>{}, IC<(G - 32) / 6>{}, IC<(G - 32) % 6>{});
        if constexpr (m > 0 && G == 57) { opk(m - 1); if constexpr (!ODEF || m == 3) ost(m - 1); }
        if constexpr (ODEF && m == 3 && (G == 1 || G == 7)) ost(G / 6);
        if constexpr (n == 1 || n == 4) {
          constexpr int z = n / 3;
          if constexpr (T < 4) a4[16 + 2 * T + z] = lda4(16 + 2 * T + z);
          if constexpr (T >= C2 && T < C2 + 6) a4[24 + 2 * (T - C2) + z] = lda4(24 + 2 * (T - C2) + z);
          if constexpr (T >= C3 && T < C3 + 6) a4[36 + 2 * (T - C3) + z] = lda4(36 + 2 * (T - C3) + z);
        }
        if constexpr (n == 2 && T == 5) { waitv<9>(); flag_st(rws, feo + 96, tag, true, l); }
        if constexpr (n == 2 && T == 4) fs = flag_ld(rws, few + 64, 32, l);
        if constexpr (n == 2 && T == C3 - 4) fs = flag_ld(rws, few + 96, 32, l);
        SB();
      });
    });
    STAMP(21);
    asm volatile("s_nop 7");
    SB();
#pragma unroll
    for (int n = 0; n < 5; n++) *(LAS f32x4*)(rdst + RED_BUF + n * 64) = ac4[1][n];
    BAR(); red_ld(3, r);
#pragma unroll
    for (int e = 0; e < 8; e++) ov[e] = ((r[e >> 2][e & 3] + r[2 + (e >> 2)][e & 3]) + (r[4 + (e >> 2)][e & 3] + r[6 + (e >> 2)][e & 3])) + (((e & 1) ? bfhi(hk[3][e >> 1]) : bflo(hk[3][e >> 1])) + b2f[e]);
    opk(3); ost(3);
    flag_st(rws, 0, tag, blockIdx.x == 0 && w == 0, l);
  }
  asm volatile(".Lk_end:");
}

extern "C" void kernel_launch(void* const* d_in, const int* in_sizes, int n_in, void* d_out, int out_size, void* d_ws, size_t ws_size, hipStream_t stream) {
  (void)hipFuncSetAttribute((const void*)k_fused, hipFuncAttributeMaxDynamicSharedMemorySize, 160 * 1024);
  k_fused<<<256, 256, 160 * 1024, stream>>>(d_in[0], d_in[1], d_in[2], d_in[4], d_in[5], d_in[6], d_in[7], d_out, (char*)d_ws);
}
```

```cpp
#include <hip/hip_runtime.h>
#include <hip/hip_bf16.h>
#include <stdint.h>
#include <type_traits>

#define LAS __attribute__((address_space(3)))
#define DI __device__ __forceinline__
typedef __attribute__((ext_vector_type(8))) __bf16 bf16x8;
typedef __attribute__((ext_vector_type(2))) __bf16 bf16x2;
typedef __attribute__((ext_vector_type(4))) float f32x4;
typedef __attribute__((ext_vector_type(2))) float f32x2;
typedef __attribute__((ext_vector_type(4))) unsigned v4u;
typedef __attribute__((ext_vector_type(2))) unsigned v2u;
typedef __attribute__((ext_vector_type(4))) short s16x4;
typedef __amdgpu_buffer_rsrc_t rsrc_t;

constexpr int S = 2048, D = 640, F = 1536;
constexpr int ACT = S * D * 2;
constexpr int EPOCH_OFF = 0;
constexpr int F0 = 1;
constexpr int FP = F0 + 256;
constexpr int FL = FP + 128;
constexpr int FC = FL + 256;
constexpr int FD = FC + 1024;
constexpr int FE = FD + 1024;
constexpr int NFLAG = FE + 4096;
constexpr int WQP_OFF = (NFLAG * 256 + 65535) / 65536 * 65536, OF_OFF = WQP_OFF + 32 * 80 * 1024, HF_OFF = OF_OFF + ACT;
constexpr int GF_OFF = HF_OFF + ACT, WOP_OFF = GF_OFF + S * F * 2, W1P_OFF = WOP_OFF + D * D * 2, W2P_OFF = W1P_OFF + D * F * 2;
constexpr int KVP_OFF = W2P_OFF + D * F * 2, KVF_OFF = KVP_OFF + 32 * 32 * 2048, WS_END = KVF_OFF + 32 * 2048;
constexpr int OOB = 0x7ffffff0;
constexpr int SC1 = 16;
constexpr unsigned ONES2 = 0x3f803f80u;

DI rsrc_t mkrs(const void* p, int bytes) { return __builtin_amdgcn_make_buffer_rsrc((void*)p, 0, bytes, 0x00020000); }
DI v4u mkrs4(const void* p, int bytes) { const uint64_t a = (uint64_t)p; return v4u{(unsigned)a, (unsigned)(a >> 32) & 0xffffu, (unsigned)bytes, 0x00020000u}; }
DI f32x4 mfma(v4u a, v4u b, f32x4 c) {
  return __builtin_amdgcn_mfma_f32_16x16x32_bf16(__builtin_bit_cast(bf16x8, a), __builtin_bit_cast(bf16x8, b), c, 0, 0, 0);
}
DI void mfma_t(v4u a, v4u b, f32x4& c) { asm volatile("v_mfma_f32_16x16x32_bf16 %0, %1, %2, %0" : "+v"(c) : "v"(a), "v"(b)); }
DI void mfma_z(v4u a, v4u b, f32x4& c) { asm volatile("v_mfma_f32_16x16x32_bf16 %0, %1, %2, 0" : "=&v"(c) : "v"(a), "v"(b)); }
template <bool FIRST, bool AG, bool BG = false> DI void mfma_w(v4u a, v4u b, f32x4& c) {
#define MF_(CA, CB) do { if constexpr (FIRST) asm volatile("v_mfma_f32_16x16x32_bf16 %0, %1, %2, 0" : "=&v"(c) : CA(a), CB(b)); \
                         else asm volatile("v_mfma_f32_16x16x32_bf16 %0, %1, %2, %0" : "+v"(c) : CA(a), CB(b)); } while (0)
  if constexpr (AG && BG) MF_("a", "a"); else if constexpr (AG) MF_("a", "v"); else if constexpr (BG) MF_("v", "a"); else MF_("v", "v");
#undef MF_
}
template <bool FIRST> DI void mfma_a(v4u a, v4u b, f32x4& c) { if constexpr (FIRST) mfma_z(a, b, c); else mfma_t(a, b, c); }
DI unsigned pk2(float a, float b) { f32x2 f = {a, b}; return __builtin_bit_cast(unsigned, __builtin_convertvector(f, bf16x2)); }
DI float bflo(unsigned u) { return __uint_as_float(u << 16); }
DI float bfhi(unsigned u) { return __uint_as_float(u & 0xffff0000u); }
DI v4u ldp(rsrc_t r, int voff, int soff) { return __builtin_amdgcn_raw_buffer_load_b128(r, voff, soff, 0); }
DI v4u lds1(rsrc_t r, int voff, int soff) { return __builtin_amdgcn_raw_buffer_load_b128(r, voff, soff, SC1); }
DI void sts1(v4u v, rsrc_t r, int voff, int soff) { asm volatile("" : "+v"(v)); __builtin_amdgcn_raw_buffer_store_b128(v, r, voff, soff, SC1); asm volatile("s_nop 1" :: "v"(v) : "memory"); }
DI v2u trread(LAS char* p) { return __builtin_bit_cast(v2u, __builtin_amdgcn_ds_read_tr16_b64_v4i16((LAS s16x4*)p)); }
#define WAITV(n) asm volatile("s_waitcnt vmcnt(" #n ")" ::: "memory")
#define BAR() do { asm volatile("s_waitcnt lgkmcnt(0)" ::: "memory"); __builtin_amdgcn_s_barrier(); asm volatile("" ::: "memory"); } while (0)
#define SB() __builtin_amdgcn_sched_barrier(0)
constexpr float LOG2E = 1.4426950408889634f;
DI float phi(float t) { return fmaxf(t, 0.f) + __builtin_amdgcn_fmed3f(__builtin_amdgcn_exp2f(t * LOG2E), 0.f, 1.f) + 1e-6f; }
template <class T> DI void pin(T& x) { asm volatile("" : "+v"(x)); }
template <class T> DI T P(T x) { pin(x); return x; }
template <class T> DI T PS(T x) { asm volatile("" : "+s"(x)); return x; }
DI f32x2 fma2(f32x2 a, f32x2 b, f32x2 c) { return __builtin_elementwise_fma(a, b, c); }
DI f32x2 lo2(f32x4 v) { return f32x2{v[0], v[1]}; }
DI f32x2 hi2(f32x4 v) { return f32x2{v[2], v[3]}; }
DI unsigned pk2v(f32x2 f) { return __builtin_bit_cast(unsigned, __builtin_convertvector(f, bf16x2)); }
DI float maxf(float a, float b) { float r; asm("v_max_f32 %0, %1, %2" : "=v"(r) : "v"(a), "v"(b)); return r; }
DI unsigned phi2(f32x2 u) {
  const f32x2 a = fma2(u, f32x2{LOG2E, LOG2E}, f32x2{-LOG2E, -LOG2E});
  const float e0 = __builtin_amdgcn_fmed3f(__builtin_amdgcn_exp2f(a[0]), 0.f, 1.f), e1 = __builtin_amdgcn_fmed3f(__builtin_amdgcn_exp2f(a[1]), 0.f, 1.f);
  return pk2v(f32x2{__builtin_amdgcn_fmed3f(u[0], e0, 3.0e38f), __builtin_amdgcn_fmed3f(u[1], e1, 3.0e38f)} + f32x2{1e-6f, 1e-6f});
}
template <int N> DI void phi_n(const f32x2 (&u)[N], unsigned (&o)[N]) {
  f32x2 a[N];
#pragma unroll
  for (int e = 0; e < N; e++) a[e] = P(fma2(u[e], f32x2{LOG2E, LOG2E}, f32x2{-LOG2E, -LOG2E}));
  SB();
#pragma unroll
  for (int e = 0; e < N; e++) a[e] = P(f32x2{__builtin_amdgcn_fmed3f(__builtin_amdgcn_exp2f(a[e][0]), 0.f, 1.f), __builtin_amdgcn_fmed3f(__builtin_amdgcn_exp2f(a[e][1]), 0.f, 1.f)});
  SB();
#pragma unroll
  for (int e = 0; e < N; e++) a[e] = P(f32x2{__builtin_amdgcn_fmed3f(u[e][0], a[e][0], 3.0e38f), __builtin_amdgcn_fmed3f(u[e][1], a[e][1], 3.0e38f)});
  SB();
#pragma unroll
  for (int e = 0; e < N; e++) a[e] = P(a[e] + f32x2{1e-6f, 1e-6f});
  SB();
#pragma unroll
  for (int e = 0; e < N; e++) o[e] = pk2v(a[e]);
  SB();
}
DI float dot2(unsigned a, unsigned b, float c) { return __builtin_amdgcn_fdot2_f32_bf16(__builtin_bit_cast(bf16x2, a), __builtin_bit_cast(bf16x2, b), c, false); }
DI float gelu(float v) {
  const float u = v * (0.7978845608028654f * 2.f * LOG2E) * __builtin_fmaf(v * v, 0.044715f, 1.0f);
  return v * __builtin_amdgcn_rcpf(1.0f + __builtin_amdgcn_exp2f(-u));
}
template <int CTRL> DI float dppadd(float v) { return v + __uint_as_float(__builtin_amdgcn_update_dpp(0u, __float_as_uint(v), CTRL, 0xf, 0xf, true)); }
DI float sum16(float v) { v = dppadd<0xB1>(v); v = dppadd<0x4E>(v); v = dppadd<0x141>(v); return dppadd<0x140>(v); }
DI float add_x16(float v) { auto r = __builtin_amdgcn_permlane16_swap(__float_as_uint(v), __float_as_uint(v), false, false); return __uint_as_float(r[0]) + __uint_as_float(r[1]); }
DI float add_x32(float v) { auto r = __builtin_amdgcn_permlane32_swap(__float_as_uint(v), __float_as_uint(v), false, false); return __uint_as_float(r[0]) + __uint_as_float(r[1]); }

DI void glds4(v4u rs, unsigned dst, int soff, int v0, int v1, int v2, int v3) {
  asm volatile("s_nop 4\n\ts_mov_b32 m0, %1\n\ts_nop 0\n\tbuffer_load_dwordx4 %3, %0, %2 offen lds\n\t"
               "s_add_u32 m0, m0, 0x400\n\ts_nop 0\n\tbuffer_load_dwordx4 %4, %0, %2 offen lds\n\t"
               "s_add_u32 m0, m0, 0x400\n\ts_nop 0\n\tbuffer_load_dwordx4 %5, %0, %2 offen lds\n\t"
               "s_add_u32 m0, m0, 0x400\n\ts_nop 0\n\tbuffer_load_dwordx4 %6, %0, %2 offen lds"
               :: "s"(rs), "s"(dst), "s"(soff), "v"(v0), "v"(v1), "v"(v2), "v"(v3) : "memory", "scc");
}
template <int IMM> DI void glds1(v4u rs, unsigned base, int soff, int v) {
  asm volatile("s_add_u32 m0, %1, %4\n\ts_nop 0\n\tbuffer_load_dwordx4 %3, %0, %2 offen lds" :: "s"(rs), "s"(base), "s"(soff), "v"(v), "i"(IMM) : "memory", "scc");
}
template <int IMM> DI void glds1_sc1(v4u rs, unsigned base, int soff, int v) {
  asm volatile("s_add_u32 m0, %1, %4\n\ts_nop 0\n\tbuffer_load_dwordx4 %3, %0, %2 offen sc1 lds" :: "s"(rs), "s"(base), "s"(soff), "v"(v), "i"(IMM) : "memory", "scc");
}
template <int IMM> DI void set_m0(unsigned base) { asm volatile("s_add_u32 m0, %0, %1" :: "s"(base), "i"(IMM) : "memory", "scc"); }
template <int OFF> DI void glds_o(v4u rs, int soff, int v) { asm volatile("buffer_load_dwordx4 %2, %0, %1 offen offset:%3 sc1 lds" :: "s"(rs), "s"(soff), "v"(v), "i"(OFF) : "memory"); }
template <int N> using IC = std::integral_constant<int, N>;
template <int N> DI void waitv() { asm volatile("s_waitcnt vmcnt(%0)" :: "i"(N) : "memory"); }
template <int I, int N, class Fn> DI void static_for(Fn&& f) { if constexpr (I < N) { f(std::integral_constant<int, I>{}); static_for<I + 1, N>(f); } }
#define PIN(x) pin(x)
#define STAMP(id)
#define STAMP_ARG
#define STAMP_PASS
#define CSTAMP(id)

#define SLP 0
DI unsigned flag_ld(rsrc_t rws, int f, int n, int l) { asm volatile("" ::: "memory"); return __builtin_amdgcn_raw_buffer_load_b32(rws, (l < n ? l : 0) * 256, f * 256, SC1); }
DI void flag_wait(unsigned v, rsrc_t rws, int f, int n, unsigned tag, int l) {
  if (__builtin_expect(!__all(v == tag), 0)) {
#pragma unroll 1
    for (int sp = 0; sp < (1 << 16); sp++) { if (__all(flag_ld(rws, f, n, l) == tag)) break; }
  }
  asm volatile("" ::: "memory");
}
DI void wait_flags(rsrc_t rws, int f, int n, unsigned tag, int l) { flag_wait(flag_ld(rws, f, n, l), rws, f, n, tag, l); }
DI void flag_st(rsrc_t rws, int f, unsigned tag, bool me, int l) {
  asm volatile("" ::: "memory"); sts1(v4u{tag, tag, tag, tag}, rws, (me && l < 8) ? l * 16 : OOB, f * 256); asm volatile("" ::: "memory");
}

template <int MAT> DI int frag_off(int T, int ks) {
  if constexpr (MAT == 0) return WOP_OFF + ((T / 5) * 100 + ks * 5 + T % 5) * 1024;
  else if constexpr (MAT == 1) return W1P_OFF + ((T / 3) * 60 + ks * 3 + T % 3) * 1024;
  else return W2P_OFF + ((T / 5) * 240 + ks * 5 + T % 5) * 1024;
}
template <int MAT> DI void pack_issue(int ks, int cb, int l, v4u rs, unsigned stg) {
  const int r = l >> 3;
  static_for<0, 4>([&](auto Ic) { constexpr int i = Ic;
    const int sw = ((l & 7) ^ ((((r >> 1) & 1) | ((i & 1) << 1)) << 1)) * 16, sg = 4 * ks + i;
    if constexpr (MAT == 0) glds1<i * 1024>(rs, stg, sg * 8 * 1280 + cb * 128, r * 1280 + sw);
    else if constexpr (MAT == 1) { const int q = ks % 10;
      glds1<i * 1024>(rs, stg, ((2 * i + ks / 10) * 80 + (q < 8 ? 32 * (q >> 2) + 4 * (q & 3) : 8 * q)) * 3072 + cb * 128, (q < 8 ? 16 * (r >> 2) + (r & 3) : r) * 3072 + sw); }
    else { const int q = sg % 6; glds1<i * 1024>(rs, stg, (48 * (sg / 6) + (q < 4 ? 4 * q : 8 * q)) * 1280 + cb * 128, (q < 4 ? 16 * (r >> 2) + (r & 3) : r) * 1280 + sw); }
  });
}
DI int unit_lane(int t, int l) { const int g = l >> 4, q = (l >> 2) & 3, p = l & 3, f = ((q >> 1) & 1) | ((g & 1) << 1); return (8 * g + q) * 128 + ((t ^ f) * 32) + p * 8; }
DI v4u unit_read(LAS char* p) { const v2u a = trread(p), b = trread(p + 512); return v4u{a.x, a.y, b.x, b.y}; }
template <int MAT> DI void pack_dst(int ks, int cb, int (&d)[4]) {
#pragma unroll
  for (int t = 0; t < 4; t++) d[t] = frag_off<MAT>(cb * 4 + t, ks);
}
DI void pack_finish(const int (&d)[4], const int (&ul)[4], int l, rsrc_t rws, LAS char* stg) {
  v4u f[4];
#pragma unroll
  for (int t = 0; t < 4; t++) f[t] = unit_read(stg + ul[t]);
#pragma unroll
  for (int t = 0; t < 4; t++) sts1(f[t], rws, l * 16, d[t]);
}
constexpr int TAB_OFF = 81920, SCR_OFF = TAB_OFF + 512, SNK_OFF = SCR_OFF + 2048, XT_OFF = 92160, XT_W = 17408, OB_ROW = 176, WO_L = 61440;
constexpr int RED_ROW = 336, RED_W = 16 * RED_ROW, RED_BUF = 4 * RED_W, A3_0 = 2 * RED_BUF, W2L_OFF = 65536;
constexpr int NL2 = 24;
static_assert(SNK_OFF + 1024 <= WO_L + 28 * 1024 && A3_0 + 20480 <= W2L_OFF, "LDS map");

#define N1 (B0 + 18)
#define N2 42
#define N3 50
#define N5 66
#define X1 2
#define X2 8
#define XROT 3
#define B0 15
#define ABL 0
#define FPK 5
#define NORIDE 0
#define RD1 6
#define PQ 50
#define PFK 4
#define RK 12
#define DLY 4
#define FLK 4

__global__ __launch_bounds__(256, 1) void k_fused(const void* x, const void* wqkv, const void* wo, const void* w1, const void* b1p, const void* w2,
                                                  const void* b2p, void* out, char* ws) {
  extern __shared__ __attribute__((aligned(16))) char smem[];
  LAS char* L = (LAS char*)smem;
  const unsigned Lb = (unsigned)(size_t)L;
  STAMP(0);
  const int tid = threadIdx.x, l = tid & 63, w = __builtin_amdgcn_readfirstlane(tid >> 6), c = l & 15, g = l >> 4;
  const int xg = blockIdx.x & 7, i = blockIdx.x >> 3;
  const unsigned Lx = Lb + XT_OFF + w * XT_W;
  LAS char* xt = L + XT_OFF + w * XT_W;
  {
    uint64_t pc; unsigned len;
    asm volatile("s_getpc_b64 %0\n.Lk_here:\n\ts_mov_b32 %1, .Lk_end-.Lk_here\n\ts_nop 2" : "=s"(pc), "=s"(len));
    glds1<SNK_OFF>(mkrs4((const void*)pc, len & ~15u), Lb, (i * 4 + w) * 640, l < 40 ? l * 16 : OOB);
  }
  const int grp = xg * 4 + (i >> 3), j = i & 7;
  const bool p0 = j < 5;
  const int pk = (i >> 3) * 5 + j;
  const v4u rx4 = mkrs4(x, S * D * 2), rws4 = mkrs4(ws, WS_END);
  const rsrc_t rws = mkrs(ws, WS_END);
  STAMP(19);
  const int fi = grp * 3 + j - 5;
  const int uc = p0 ? 384 + (grp * 5 + j) * 3 + w - 1 : fi * 4 + w;
  const bool hasc = (!p0 || w > 0) && uc < 776;
  auto late_issue = [&]() {
    if (uc < 200) pack_issue<0>(uc % 20, uc / 20, l, mkrs4(wo, D * D * 2), Lx + 4096);
    else if (uc < 296) pack_issue<1>(16 + (uc & 3), (uc - 200) >> 2, l, mkrs4(w1, D * F * 2), Lx + 4096);
    else pack_issue<2>((uc - 296) % 48, (uc - 296) / 48, l, mkrs4(w2, D * F * 2), Lx + 4096); };
  if (p0) {
    const v4u rw4 = mkrs4(wqkv, D * 1920 * 2);
    const int rr = l / 30, e = l - rr * 30, sec = e / 10;
    const int v0 = l < 60 ? rr * 3840 + sec * 1280 + (e - sec * 10) * 16 : OOB, s0 = (32 * pk + 8 * w) * 3840 + xg * 160;
    static_for<0, 4>([&](auto Uc) { constexpr int u = Uc; glds1<u * 1024>(rw4, Lx, s0 + u * 2 * 3840, v0); });
  }
  glds1_sc1<16384>(rws4, Lx, 0, l == 0 ? EPOCH_OFF : (l == 1 ? (p0 ? F0 + xg * 32 + pk : FP + fi) * 256 : OOB));
  int va[2];
#pragma unroll
  for (int u = 0; u < 2; u++) va[u] = (grp * 64 + w * 16 + u * 8 + (l >> 3)) * 1280 + (((l & 7) ^ (g | (u << 2))) * 16);
  auto ldx = [&](auto Qc) { constexpr int q = Qc; int ln = (q >> 1) + (p0 ? 0 : (j - 5) * 3); ln = ln >= 10 ? ln - 10 : ln;
    glds1<(q & 1) * 1024>(rx4, Lb + w * 2048 + ln * 8192, ln * 128, va[q & 1]); };
  int ul[4], dc[4], de[4] = {0, 0, 0, 0};
#pragma unroll
  for (int t = 0; t < 4; t++) ul[t] = unit_lane(t, l);
  auto late_dst = [&]() {
    if (uc < 200) pack_dst<0>(uc % 20, uc / 20, dc); else if (uc < 296) pack_dst<1>(16 + (uc & 3), (uc - 200) >> 2, dc); else pack_dst<2>((uc - 296) % 48, (uc - 296) / 48, dc);
#pragma unroll
    for (int t = 0; t < 4; t++) dc[t] = PS(dc[t]); };
  unsigned tag = 0;
  auto get_tag = [&]() { tag = __builtin_amdgcn_readfirstlane(*(LAS unsigned*)(xt + 16384)) + 1u; };
  if (p0) {
    {
    const char* tp = l < 8 ? (const char*)wo + l * 102400 : l < 24 ? (const char*)w1 + (l - 8) * 122880 : l < 40 ? (const char*)w2 + (l - 24) * 122880 : l < 56 ? (const char*)out + (l - 40) * 163840 : l < 60 ? (const char*)b1p : (const char*)b2p;
    asm volatile("s_mov_b32 m0, %1\n\ts_nop 0\n\tglobal_load_lds_dword %0, off" :: "v"(tp), "s"(Lb + SNK_OFF) : "memory");
    }
    const int q = (l >> 2) & 3, p = l & 3;
    const int tp0 = P((q >> 1) * 1024 + (q & 1) * 480 + (g == 0 ? 160 + 8 * p : g == 1 ? 320 + 8 * p : g == 2 ? (p == 0 ? 192 : p == 1 ? 352 : 32) : 8 * p));
    const int vd = P(g * 1024 + c * 16), sd = PS(WQP_OFF + (xg * 80 + pk) * 4096 + w * 256);
    late_dst();
    waitv<2>();
    STAMP(20);
    v4u f[4];
#pragma unroll
    for (int t = 0; t < 4; t++) { const v2u a = trread(xt + tp0 + 40 * t), b = trread(xt + tp0 + 40 * t + 2048); f[t] = v4u{a.x, a.y, b.x, b.y}; }
#pragma unroll
    for (int t = 0; t < 4; t++) sts1(f[t], rws, vd, sd + t * 81920);
    STAMP(1);
    static_for<0, X2>(ldx);
    waitv<X2>();
    get_tag();
    BAR(); flag_st(rws, F0 + xg * 32 + pk, tag, w == 0, l);
    STAMP(2);
    static_for<X2, 20>(ldx);
  } else {
    static_for<0, 8>(ldx);
    pack_issue<1>(uc & 15, uc >> 4, l, mkrs4(w1, D * F * 2), Lx);
    static_for<8, 20>(ldx);
    pack_dst<1>(uc & 15, uc >> 4, de);
#pragma unroll
    for (int t = 0; t < 4; t++) de[t] = PS(de[t]);
    late_dst();
    STAMP(20);
    waitv<24>();
    get_tag();
    STAMP(2);
  }
  const bool mid = c >= 4 && c < 12;
  const int c4 = c < 4 ? c : 4;
  const int vk1 = P(mid ? 1024 + (12 + g * 8 + c) * 16 : OOB), vg0 = P(g == 0 ? c * 16 : OOB), vk3 = P((g == 0 && mid) ? 1024 + (44 + c) * 16 : OOB);
  const int vsc = P(c >= 12 ? SCR_OFF + w * 512 + g * 64 + (c - 12) * 16 : SNK_OFF + l * 8);
  const int vpp = P(g * 2048 + ((xg == 7 && c >= 8) ? 7 : c) * 16), vf2 = P(1024 + (16 + g * 8 + c4) * 16), vf3 = P(1024 + (48 + c4) * 16);
  const int vtc = P(l < 28 ? (l < 16 ? FE + grp * 128 + (l >> 2) * 32 + j * 4 + (l & 3) : (l < 20 ? FC : FD) + grp * 32 + (l & 3) * 8 + j) * 256 + (l >= 24 ? 128 : 0) : OOB);
  const int vhf = P((g & 1) ? OOB : (g >> 1) * 256 + c * 16);
  LAS char* ap[2];
#pragma unroll
  for (int kk = 0; kk < 2; kk++) ap[kk] = L + c * 128 + (((4 * kk + g) ^ ((c >> 1) & 7)) * 16);
  auto rdx = [&](int ks, int m) { return *(LAS v4u*)(ap[ks & 1] + (ks >> 1) * 8192 + m * 2048); };
  auto ldf = [&](int base, int q) { return lds1(rws, l * 16 + (q & 3) * 1024, base + (q >> 2) * 4096); };
  const int hd = 4 * j + w;
  v4u bq[80];
  auto ldq = [&](auto Qc) { constexpr int q = Qc, f = q < 60 ? (q / 3) * 4 + 2 - q % 3 : (q - 60) * 4 + 3; bq[f] = ldf(WQP_OFF + hd * 81920, f); };
  STAMP(3);
  if (w == 0) wait_flags(rws, F0 + j * 32, 20, tag, l);
  if (hasc) late_issue();
  BAR();
  STAMP(4);
  static_for<0, B0 - 10>(ldq); SB();
  if (hasc) waitv<B0 - 6>(); else waitv<B0 - 10>();
  if (!p0) pack_finish(de, ul, l, rws, xt);
  {
    v4u a[20];
#pragma unroll
    for (int ks = 0; ks < 20; ks++) a[ks] = *(LAS v4u*)(ap[ks & 1] + (ks >> 1) * 8192 + w * 2048);
    SB();
    float s[2] = {0.f, 0.f}, q[2] = {0.f, 0.f};
    static_for<0, 20>([&](auto Kc) { constexpr int ks = Kc;
      if constexpr (ks % 2 == 0) ldq(IC<B0 - 10 + ks / 2>{});
#pragma unroll
      for (int e = 0; e < 4; e++) { s[e & 1] = dot2(a[ks][e], ONES2, s[e & 1]); q[e & 1] = dot2(a[ks][e], a[ks][e], q[e & 1]); }
      SB(); });
    const float mean = add_x32(add_x16(s[0] + s[1])) * (1.f / D);
    const float rs = __builtin_amdgcn_rsqf(fmaxf(add_x32(add_x16(q[0] + q[1])) * (1.f / D) - mean * mean, 0.f) + 1e-5f);
    *(LAS float*)(L + TAB_OFF + (g & 1) * 256 + (w * 16 + c) * 4) = (g & 1) ? -mean * rs : rs;
  }
  BAR();
  v4u ones = {ONES2, ONES2, ONES2, ONES2};
  PIN(ones);
  v4u fa[2][4];
  unsigned fp = 0;
  const int fpa = FP + w * 24, fpn = 24;
  v4u bw1[60], bw2[60];
  const int w1b = W1P_OFF + hd * 61440, w2b = W2P_OFF + hd * 61440;
  auto ldw2 = [&](auto Qc) { constexpr int q = Qc; if constexpr (q >= 0 && q < 60) bw2[q] = ldf(w2b, q); };
  auto bulk = [&](auto Qc) { constexpr int q = Qc; if constexpr (q < 60) bw1[q] = ldf(w1b, q); else ldw2(IC<q - 60>{}); };
  auto run = [&](auto Nc, unsigned lds, int mem) { static_for<0, Nc>([&](auto Qc) { constexpr int q = Qc;
    if constexpr (q % 4 == 0) { set_m0<q * 1024>(lds); asm volatile("s_nop 0"); }
    glds_o<(q % 4) * 1024>(rws4, mem + (q / 4) * 4096, l * 16); }); };
#define W1F(r) bw1[r]
  {
    f32x4 acc[4][3], cs[3], trs[4], tnm[4];
    LAS char* tabp = L + TAB_OFF + g * 16;
#pragma unroll
    for (int m = 0; m < 4; m++) fa[0][m] = rdx(0, m);
    SB();
    static_for<0, 20>([&](auto Kc) { constexpr int ks = Kc;
      if constexpr (ks == 3) { if (!p0) { waitv<19>(); BAR(); flag_st(rws, FP + fi, tag, w == 0, l); } SB(); }
      if constexpr (ks == PFK) { if (hasc) pack_finish(dc, ul, l, rws, xt + 4096); SB(); }
      if constexpr (ks == 19) { flag_wait(fp, rws, fpa, fpn, tag, l); BAR(); flag_st(rws, FL + (p0 ? 96 + grp * 5 + j : fi), tag, w == 0, l); }
      if constexpr (ks == 5) STAMP(23);
      static_for<0, 15>([&](auto Qc) { constexpr int Q = Qc, n = Q / 5, m = Q % 5;
        if constexpr (m < 4) { if constexpr (!(ABL & 4) || m == 0 || ks == 0) mfma_w<ks == 0, false, true>(fa[ks & 1][m], bq[ks * 4 + n], acc[m][n]); } else if constexpr (!(ABL & 2) || ks == 0) mfma_w<ks == 0, false, true>(ones, bq[ks * 4 + n], cs[n]);
        if constexpr (Q >= 1 && Q < 9 && (Q & 1)) fa[(ks + 1) & 1][3 - Q / 2] = rdx(ks < 19 ? ks + 1 : 0, 3 - Q / 2);
        if constexpr (m == 2 && B0 + ks * 3 + n < 80) ldq(IC<B0 + ks * 3 + n>{});
        if constexpr (ks == 15 && Q == 0) fp = flag_ld(rws, fpa, fpn, l);
        if constexpr (ks >= 18 && Q >= 9 && Q < 13) (ks == 18 ? trs : tnm)[Q - 9] = *(LAS f32x4*)(tabp + (ks - 18) * 256 + (Q - 9) * 64);
        SB(); });
    });
    static_assert(B0 >= 12 && B0 <= 20, "");
    asm volatile("s_nop 7");
#pragma unroll
    for (int n = 0; n < 3; n++) pin(cs[n]);
    STAMP(5);
    v4u fk[2], fv[2], fr[2], fb[2];
    const f32x2 one2 = {1.f, 1.f}, mk = c < 8 ? one2 : f32x2{0.f, 0.f}, ck = c < 8 ? -one2 : one2;
    auto ride0 = [&](auto Ec) { if constexpr (B0 + 60 + Ec < 80) ldq(IC<B0 + 60 + Ec>{}); else bulk(IC<B0 + Ec - 20>{}); };
    auto ride = [&](auto Ec) { if constexpr (!NORIDE) ride0(Ec); };
    auto unused_ = [&](auto Ec) { if constexpr (B0 + 60 + Ec < 80) ldq(IC<B0 + 60 + Ec>{}); else bulk(IC<B0 + Ec - 20>{}); };
    static_for<0, 2>([&](auto Uc) { constexpr int u = Uc;
      f32x2 t[3][4], uu[8];
      unsigned o[8];
#pragma unroll
      for (int e = 0; e < 4; e++) { const f32x2 nm = (e & 1) ? hi2(tnm[2 * u + (e >> 1)]) : lo2(tnm[2 * u + (e >> 1)]);
        t[0][e] = P(fma2(nm, lo2(cs[0]), one2)); t[1][e] = P(nm * lo2(cs[1])); t[2][e] = P(fma2(nm, lo2(cs[2]), one2)); }
      ride(IC<4 * u>{});
      SB();
#pragma unroll
      for (int e = 0; e < 4; e++) { const int m = 2 * u + (e >> 1); const f32x2 rs = (e & 1) ? hi2(trs[m]) : lo2(trs[m]);
        uu[e] = fma2(rs, (e & 1) ? hi2(acc[m][0]) : lo2(acc[m][0]), t[0][e]); uu[4 + e] = fma2(rs, (e & 1) ? hi2(acc[m][2]) : lo2(acc[m][2]), t[2][e]);
        t[1][e] = fma2(rs, (e & 1) ? hi2(acc[m][1]) : lo2(acc[m][1]), t[1][e]); }
      ride(IC<4 * u + 1>{});
      SB();
#pragma unroll
      for (int e = 0; e < 4; e++) { t[2][e] = fma2(uu[4 + e], mk, ck); fv[u][e] = pk2v(t[1][e]); }
      ride(IC<4 * u + 2>{});
      phi_n<8>(uu, o);
#pragma unroll
      for (int e = 0; e < 4; e++) { fb[u][e] = pk2v(t[2][e]); fk[u][e] = o[e]; fr[u][e] = o[4 + e]; }
      ride(IC<4 * u + 3>{});
      SB();
    });
#pragma unroll
    for (int u = 0; u < 2; u++) { pin(fk[u]); pin(fv[u]); pin(fr[u]); pin(fb[u]); }
    asm volatile("s_nop 1");
    f32x4 t00, t01, t10, t11;
    mfma_z(fk[0], fv[0], t00); mfma_z(fk[0], fb[0], t01); mfma_z(fr[0], fv[0], t10); mfma_z(fr[0], fb[0], t11);
    mfma_t(fk[1], fv[1], t00); mfma_t(fk[1], fb[1], t01); mfma_t(fr[1], fv[1], t10); mfma_t(fr[1], fb[1], t11);
#pragma unroll
    for (int u = 0; u < 2; u++) *(LAS v4u*)(L + (vsc & ~15) + u * 256) = fr[u];
    asm volatile("s_nop 5");
    SB();
    const int kvo = KVP_OFF + (hd * 32 + grp) * 2048;
    sts1(v4u{pk2(t00[0], t00[1]), tag, pk2(t00[2], t00[3]), tag}, rws, l * 16, kvo);
    sts1(v4u{pk2(t01[0], t01[1]), tag, pk2(t01[2], t01[3]), tag}, rws, vk1, kvo);
    sts1(v4u{pk2(t10[0], t10[1]), tag, pk2(t10[2], t10[3]), tag}, rws, vg0, kvo + 1024);
    sts1(v4u{pk2(t11[0], t11[1]), tag, pk2(t11[2], t11[3]), tag}, rws, vk3, kvo);
    STAMP(6);
    if constexpr (NORIDE) static_for<0, 8>(ride0);
  }
  if (w == ((grp + 1) & 3)) glds1<SNK_OFF>(rws4, Lb, 0, vtc);
  const bool act = c < 10;
  v2u xr[5];
  {
    f32x4 acc[4], cs;
    float qrs[4], qnm[4];
    v2u q1[4];
    unsigned fw = 0;
    static_assert(N2 <= 48, "only w1 k-steps 0..15 are known packed");
    static_for<0, 21>([&](auto Kc) { constexpr int ks = Kc;
      if constexpr (ks == RK - 6) fw = flag_ld(rws, FL + 5 * ((5 * j) >> 2), 10, l);
      if constexpr (ks == RK) {
        STAMP(7);
        if (w == (grp & 3)) {
          v4u pp[8];
#pragma unroll 1
          for (int sp = 0; sp < (1 << 16); sp++) {
            asm volatile("" ::: "memory");
#pragma unroll
            for (int q = 0; q < 8; q++) pp[q] = lds1(rws, vpp, KVP_OFF + (hd * 32 + 4 * q) * 2048 + xg * 256);
            SB();
            unsigned bad = 0;
#pragma unroll
            for (int q = 0; q < 8; q++) bad |= (pp[q].y ^ tag) | (pp[q].w ^ tag);
            if (__all(bad == 0)) break;
          }
          float sm[4] = {0.f, 0.f, 0.f, 0.f};
#pragma unroll
          for (int q = 0; q < 8; q++) { sm[0] += bflo(pp[q].x); sm[1] += bfhi(pp[q].x); sm[2] += bflo(pp[q].z); sm[3] += bfhi(pp[q].z); }
#pragma unroll
          for (int e = 0; e < 4; e++) sm[e] = add_x32(add_x16(sm[e]));
          sts1(v4u{pk2(sm[0], sm[1]), tag, pk2(sm[2], sm[3]), tag}, rws, vg0, KVF_OFF + hd * 2048 + xg * 256);
        } else { flag_wait(fw, rws, FL + 5 * ((5 * j) >> 2), 10, tag, l); const int f = (28 + ((w - grp - 1) & 3) * 24) * 1024; run(IC<24>{}, Lb + WO_L + f, WOP_OFF + j * 102400 + f); }
        SB();
        STAMP(8);
      }
      if constexpr (ks < 20) static_for<0, 5>([&](auto Qc) { constexpr int m = Qc;
        if constexpr (m < 4) mfma_a<ks == 0>(bq[ks * 4 + 3], fa[ks & 1][m], acc[m]); else mfma_a<ks == 0>(bq[ks * 4 + 3], ones, cs);
        if constexpr (ks < 19 && m < 4) fa[(ks + 1) & 1][3 - m] = rdx(ks + 1, 3 - m);
        if constexpr (m == 4 || (m == 1 && (ks & 1))) bulk(IC<B0 - 12 + (ks / 2) * 3 + (ks & 1) + (m == 4 && (ks & 1))>{});
        SB(); });
    });
    {
      LAS char* qp = L + SCR_OFF + w * 512 + (l & 15) / 4 * 16 + (l & 3) * 64;
#pragma unroll
      for (int m = 0; m < 4; m++) {
        qrs[m] = *(LAS float*)(L + TAB_OFF + c * 4 + m * 64); qnm[m] = *(LAS float*)(L + TAB_OFF + c * 4 + 256 + m * 64);
        q1[m] = trread(qp + (m >> 1) * 256 + (m & 1) * 8);
      }
#pragma unroll
      for (int n = 0; n < 5; n++) { const int sg = 10 * j + 2 * n + (g >> 1);
        xr[n] = *(LAS v2u*)(L + (sg >> 3) * 8192 + (w * 16 + c) * 128 + (((sg & 7) ^ ((c >> 1) & 7)) * 16) + (g & 1) * 8); }
    }
    asm volatile("s_nop 7");
    SB();
    STAMP(9);
    v4u ka0, ka1, kb0, kb1;
    auto kv_ld = [&]() { asm volatile("" ::: "memory");
      ka0 = lds1(rws, l * 16, KVF_OFF + hd * 2048); ka1 = lds1(rws, 1024 + c * 16, KVF_OFF + hd * 2048);
      kb0 = lds1(rws, vf2, KVF_OFF + hd * 2048); kb1 = lds1(rws, vf3, KVF_OFF + hd * 2048); };
    auto kv_ok = [&]() { return __all(((ka0.y ^ tag) | (ka0.w ^ tag) | (ka1.y ^ tag) | (ka1.w ^ tag) | (kb0.y ^ tag) | (kb0.w ^ tag) | (kb1.y ^ tag) | (kb1.w ^ tag)) == 0); };
    const unsigned fq = flag_ld(rws, FL + w * 57, 57, l);
    static_for<N1, N2>(bulk); SB();
    kv_ld();
    v4u pq[4];
    {
      f32x2 uu[8];
      unsigned o[8];
#pragma unroll
      for (int e = 0; e < 8; e++) uu[e] = P(fma2(f32x2{qnm[e >> 1], qnm[e >> 1]}, (e & 1) ? hi2(cs) : lo2(cs), f32x2{1.f, 1.f}));
      SB();
#pragma unroll
      for (int e = 0; e < 8; e++) uu[e] = P(fma2(f32x2{qrs[e >> 1], qrs[e >> 1]}, (e & 1) ? hi2(acc[e >> 1]) : lo2(acc[e >> 1]), uu[e]));
      SB();
      phi_n<8>(uu, o);
#pragma unroll
      for (int m = 0; m < 4; m++) { pq[m] = v4u{o[2 * m], o[2 * m + 1], q1[m].x, q1[m].y}; pin(pq[m]); }
    }
    flag_wait(fq, rws, FL + w * 57, 57, tag, l);
    BAR();
    STAMP(10);
    if (__builtin_expect(!kv_ok(), 0)) {
#pragma unroll 1
      for (int sp = 0; sp < (1 << 16); sp++) { kv_ld(); if (kv_ok()) break; }
    }
    STAMP(24);
    v4u kf0 = {ka0.x, ka0.z, g == 3 ? ka1.x : 0u, g == 3 ? ka1.z : 0u}, kf1 = {kb0.x, kb0.z, g == 3 ? kb1.x : 0u, g == 3 ? kb1.z : 0u};
    pin(kf0); pin(kf1);
    asm volatile("s_nop 1");
    f32x4 n0[4], n1[4];
    LAS char* ob0 = L + c * OB_ROW + w * 40 + g * 8;
    LAS char* ob1 = L + c * OB_ROW + (g == 0 ? w * 40 + 32 : 160);
#pragma unroll
    for (int m = 0; m < 4; m++) { mfma_z(kf0, pq[m], n0[m]); mfma_z(kf1, pq[m], n1[m]); }
    asm volatile("s_nop 7");
    SB();
    float rr[4];
#pragma unroll
    for (int m = 0; m < 4; m++) rr[m] = __uint_as_float(__builtin_amdgcn_permlane16_swap(__float_as_uint(n1[m][0]), __float_as_uint(n1[m][0]), false, false)[1]);
    SB();
#pragma unroll
    for (int m = 0; m < 4; m++) rr[m] = P(rr[m] + 1e-6f);
    SB();
#pragma unroll
    for (int m = 0; m < 4; m++) rr[m] = P(__builtin_amdgcn_rcpf(rr[m]));
    SB();
#pragma unroll
    for (int m = 0; m < 4; m++) { n0[m] = P(n0[m] * rr[m]); n1[m] = P(n1[m] * rr[m]); }
    SB();
#pragma unroll
    for (int m = 0; m < 4; m++) {
      *(LAS v2u*)(ob0 + m * 16 * OB_ROW) = v2u{pk2v(lo2(n0[m])), pk2v(hi2(n0[m]))};
      *(LAS v2u*)(ob1 + m * 16 * OB_ROW) = v2u{pk2v(lo2(n1[m])), pk2v(hi2(n1[m]))};
    }
    BAR();
    v4u o[3];
#pragma unroll
    for (int q = 0; q < 3; q++) o[q] = *(LAS v4u*)(L + (w * 16 + c) * OB_ROW + (q * 4 + (q == 2 ? g & 1 : g)) * 16);
#pragma unroll
    for (int q = 0; q < 3; q++) sts1(o[q], rws, (q < 2 || g < 2) ? l * 16 + q * 1024 : OOB, OF_OFF + (grp * 4 + w) * 20480 + j * 2560);
    STAMP(25);
    SB();
    static_for<N2, N3>(bulk); SB();
    waitv<N3 - N2>();
    flag_st(rws, FC + grp * 32 + w * 8 + j, tag, true, l);
    run(IC<7>{}, Lb + WO_L + w * 7168, WOP_OFF + j * 102400 + w * 7168);
    STAMP(11);
  }
  LAS char* rdst = L + w * RED_W + c * RED_ROW + g * 16;
  LAS char* rsrc = L + (4 * w + g) * RED_ROW + (act ? c : 9) * 32;
  const int rowo = act ? (grp * 64 + 4 * w + g) * 1280 + j * 160 + c * 16 : OOB;
  auto red_ld = [&](int m, f32x4 (&r)[8]) {
#pragma unroll
    for (int ww = 0; ww < 4; ww++) { LAS f32x4* p = (LAS f32x4*)(rsrc + (m & 1) * RED_BUF + ww * RED_W); r[2 * ww] = p[0]; r[2 * ww + 1] = p[1]; }
  };
  v4u hs[4][2], hk[4];
  float hsa[4], hsb[4];
  const int hsv = g * 512 + c * 16;
  auto h_poll = [&](int m) { asm volatile("" ::: "memory");
    hs[m][0] = lds1(rws, hsv, (FD + grp * 32 + m * 8) * 256); hs[m][1] = lds1(rws, hsv + 256, (FD + grp * 32 + m * 8) * 256); };
  auto h_ok = [&](int m) { return __all(((hs[m][0].y ^ tag) | (hs[m][0].w ^ tag) | (hs[m][1].y ^ tag) | (hs[m][1].w ^ tag)) == 0); };
  auto h_wait = [&](int m) {
    if (__builtin_expect(!h_ok(m), 0)) {
#pragma unroll 1
      for (int sp = 0; sp < (1 << 16); sp++) { h_poll(m); if (h_ok(m)) break; }
    }
    asm volatile("" ::: "memory");
    hsa[m] = P(__uint_as_float(hs[m][0].x) + __uint_as_float(hs[m][1].x)); hsb[m] = P(__uint_as_float(hs[m][0].z) + __uint_as_float(hs[m][1].z));
  };
  v2u bias[3];
  const unsigned La = Lb + w * 5120;
  const int hso = HF_OFF + grp * 81920 + w * 5120;
  auto ldh = [&](auto Mc, auto Qc) { constexpr int m = Mc, q = Qc; glds1_sc1<((m & 1) ? 0 : A3_0) + q * 1024>(rws4, La, hso + m * 20480 + q * 1024, l * 16); };
  f32x4 cs[3];
  {
    const rsrc_t rb1 = mkrs(b1p, F * 2);
#pragma unroll
    for (int n = 0; n < 3; n++) bias[n] = __builtin_amdgcn_raw_buffer_load_b64(rb1, ((12 * j + 3 * w + n) * 16 + 4 * g) * 2, 0, 0);
    static_for<N3, N5>(bulk); SB();
    wait_flags(rws, FC + grp * 32 + w * 8, 8, tag, l);
    STAMP(12);
    v4u a[20], bo[100];
    auto lda = [&](auto Qc) { constexpr int q = Qc; a[q] = ldf(OF_OFF + (grp * 4 + w) * 20480, q); };
    static_for<0, 12>(lda);
    BAR();
    LAS char* wp[2] = {L + WO_L + l * 16, L + WO_L + 51200 + l * 16};
    auto rdo = [&](int f) { return *(LAS v4u*)(wp[f / 50] + (f % 50) * 1024); };
#pragma unroll
    for (int f = 0; f < 10; f++) bo[f] = rdo(f);
    SB();
    f32x4 acc[5];
    static_for<0, 100>([&](auto Fc) { constexpr int f = Fc, k = f / 5, n = f % 5;
      mfma_a<k == 0>(bo[f], a[k], acc[n]);
      if constexpr (k < 18) bo[(k + 2) * 5 + 4 - n] = rdo((k + 2) * 5 + 4 - n);
      if constexpr (n == 2 && k + 12 < 20) lda(IC<k + 12>{});
      SB(); });
    STAMP(13);
    asm volatile("s_nop 7");
    SB();
    unsigned hp[10];
    float s[2] = {0.f, 0.f}, q[2] = {0.f, 0.f};
#pragma unroll
    for (int n = 0; n < 5; n++) {
      hp[2 * n] = pk2v(lo2(acc[n]) + f32x2{bflo(xr[n].x), bfhi(xr[n].x)}); hp[2 * n + 1] = pk2v(hi2(acc[n]) + f32x2{bflo(xr[n].y), bfhi(xr[n].y)});
    }
    const int hb = HF_OFF + (grp * 4 + w) * 20480 + j * 2560;
    sts1(v4u{hp[0], hp[1], hp[2], hp[3]}, rws, l * 16, hb);
    sts1(v4u{hp[4], hp[5], hp[6], hp[7]}, rws, l * 16 + 1024, hb);
    auto s0 = __builtin_amdgcn_permlane16_swap(hp[8], hp[8], false, false);
    auto s1 = __builtin_amdgcn_permlane16_swap(hp[9], hp[9], false, false);
    sts1(v4u{s0[0], s1[0], s0[1], s1[1]}, rws, vhf, hb + 2048);
    STAMP(14);
#pragma unroll
    for (int e = 0; e < 10; e++) { s[e & 1] = dot2(hp[e], ONES2, s[e & 1]); q[e & 1] = dot2(hp[e], hp[e], q[e & 1]); }
    const v4u hst = {__float_as_uint(add_x32(add_x16(s[0] + s[1]))), tag, __float_as_uint(add_x32(add_x16(q[0] + q[1]))), tag};
    static_for<N5, N5 + RD1>(bulk); SB();
    waitv<RD1>();
    sts1(hst, rws, vg0, (FD + grp * 32 + w * 8 + j) * 256);
    BAR();
    STAMP(15);
    static_assert(N5 >= 60, "w1 has landed");
    asm volatile("s_nop 1");
    static_for<0, 60>([&](auto Qc) { constexpr int q = Qc; mfma_w<(q < 3), true>(W1F(q), ones, cs[q % 3]);
      if constexpr (q % 5 == 0 && q < PQ) bulk(IC<N5 + RD1 + q / 5>{});
      if constexpr (q == PQ) { h_poll(0); h_poll(1); }
      SB(); });
    h_wait(0); h_wait(1);
    STAMP(16);
    run(IC<5>{}, La + A3_0, hso);
    h_poll(2); h_poll(3);
    static_for<N5 + RD1 + PQ / 5, 92>(bulk); SB();
    h_wait(2); h_wait(3);
    run(IC<5>{}, La, hso + 20480);
  }
  {
    const int few = FE + grp * 128, feo = few + j * 4 + w;
    unsigned fs = 0;
    f32x2 bf[6];
#pragma unroll
    for (int e = 0; e < 6; e++) bf[e] = P(f32x2{bflo(bias[e >> 1][e & 1]), bfhi(bias[e >> 1][e & 1])});
    SB();
    BAR();
    STAMP(17);
    LAS char* ab = L + g * 5120 + c * 16;
    auto rda = [&](int t) { return *(LAS v4u*)(ab + (((t / 20) & 1) ? 0 : A3_0) + (t % 20) * 256); };
    const int cc = act ? c : 0;
    LAS char* hr0 = L + j * 2560 + (4 * w + g) * 16 + (cc < 8 ? ((cc >> 2) * 4 + (cc & 1) * 2) * 256 + ((cc >> 1) & 1) * 8 : cc * 256);
    LAS char* hr1 = hr0 + (cc < 8 ? 256 : 8);
    const unsigned L2w = Lb + W2L_OFF + w * (NL2 * 1024);
    constexpr int RD = 6;
    v4u af[RD];
    f32x4 acc[3];
    f32x2 bp[6], gv[6], gu[6];
    unsigned gp[6];
    float rs = 0.f, nr = 0.f, sa = 0.f, sb = 0.f;
    auto stats = [&](int m, auto Pc) { constexpr int p = Pc;
      if constexpr (p == 1) { auto x = __builtin_amdgcn_permlane16_swap(__float_as_uint(hsa[m]), __float_as_uint(hsb[m]), false, false); sa = P(__uint_as_float(x[0]) + __uint_as_float(x[1])); sb = P(sa); }
      if constexpr (p == 2) { auto x = __builtin_amdgcn_permlane32_swap(__float_as_uint(sa), __float_as_uint(sb), false, false); sa = P(__uint_as_float(x[0]) + __uint_as_float(x[1])); sb = P(sa); }
      if constexpr (p == 3) { auto x = __builtin_amdgcn_permlane16_swap(__float_as_uint(sa), __float_as_uint(sb), false, false);
        sa = P(__uint_as_float(x[0]) * (1.f / D)); sb = P(fmaxf(__uint_as_float(x[1]) * (1.f / D) - sa * sa, 0.f)); }
      if constexpr (p == 4) { const float r1 = __builtin_amdgcn_rsqf(sb + 1e-5f);
        rs = P(r1 * __builtin_amdgcn_rsqf(sb * r1 * r1 + 1e-5f));
        nr = P(-sa * rs); }
    };
    auto epi1 = [&]() {
#pragma unroll
      for (int e = 0; e < 6; e++) bp[e] = P(fma2(f32x2{nr, nr}, (e & 1) ? hi2(cs[e >> 1]) : lo2(cs[e >> 1]), bf[e]));
      asm volatile("s_nop 1");
      SB();
      constexpr float C0 = -0.7978845608028654f * 2.f * LOG2E, C1 = C0 * 0.044715f;
      const f32x2 rs2 = {rs, rs};
#pragma unroll
      for (int e = 0; e < 6; e++) gv[e] = P(fma2(rs2, (e & 1) ? hi2(acc[e >> 1]) : lo2(acc[e >> 1]), bp[e]));
      SB();
#pragma unroll
      for (int e = 0; e < 6; e++) gu[e] = P(gv[e] * gv[e]);
      SB();
#pragma unroll
      for (int e = 0; e < 6; e++) gu[e] = P(fma2(gu[e], f32x2{C1, C1}, f32x2{C0, C0}));
      SB();
#pragma unroll
      for (int e = 0; e < 6; e++) gu[e] = P(gu[e] * gv[e]);
      SB();
    };
    auto gstore = [&](int m) {
      const int base = GF_OFF + (grp * 4 + m) * 49152 + 6 * hd * 256;
      sts1(v4u{gp[0], gp[1], gp[2], gp[3]}, rws, l * 16, base);
      auto s0 = __builtin_amdgcn_permlane16_swap(gp[4], gp[4], false, false);
      auto s1 = __builtin_amdgcn_permlane16_swap(gp[5], gp[5], false, false);
      sts1(v4u{s0[0], s1[0], s0[1], s1[1]}, rws, vhf, base + 1024);
    };
    auto egap = [&](auto Mc, auto Gc) { constexpr int G = Gc;
      if constexpr (G < 18 && G % 3 < 2) { constexpr int e = G / 3, z = G % 3; gu[e][z] = P(__builtin_amdgcn_exp2f(gu[e][z])); }
      if constexpr (G >= 2 && G < 20 && G % 3 == 2) { constexpr int e = G / 3; gu[e][0] = P(gu[e][0] + 1.0f); SB(); gu[e][1] = P(gu[e][1] + 1.0f); }
      if constexpr (G >= 18 && G < 36 && G % 3 < 2) { constexpr int e = (G - 18) / 3, z = G % 3; gu[e][z] = P(__builtin_amdgcn_rcpf(gu[e][z])); }
      if constexpr (G >= 20 && G < 38 && G % 3 == 2) { constexpr int e = (G - 20) / 3; gv[e][0] = P(gv[e][0] * gu[e][0]); SB(); gv[e][1] = P(gv[e][1] * gu[e][1]); }
      if constexpr (G >= 36 && G < 39 + 1 && G != 38) { constexpr int e = (G == 39 ? 2 : G - 36) * 2; gp[e] = P(pk2v(gv[e])); gp[e + 1] = P(pk2v(gv[e + 1])); }
      if constexpr (G == 40) gstore(Mc);
    };
    v4u a4[12];
    auto lda4 = [&](int q) { return ldf(GF_OFF + ((grp * 4 + q / 12) * 48 + w * 12) * 1024, q % 12); };
    const rsrc_t ro = mkrs(out, S * D * 2), rb2 = mkrs(b2p, D * 2);
    v4u br;
#pragma unroll
    for (int q = 0; q < RD; q++) af[q] = rda(q);
    SB();
    asm volatile("s_nop 1");
    auto rider = [&](auto Qc) { constexpr int q = Qc; if constexpr (q < NL2) glds1_sc1<q * 1024>(rws4, L2w, w2b + (60 - NL2 + q) * 1024, l * 16); };
    static_for<0, 80>([&](auto Tc) {
      constexpr int T = Tc, m = T / 20, k = T % 20;
      if constexpr (k == 14 && m < 3) {
        waitv<(m == 0 ? 14 : m == 1 ? 9 : 6)>();
        BAR();
      }
      if constexpr (k == 0) STAMP(26 + m);
      if constexpr (T == 66) flag_wait(fs, rws, few, 32, tag, l);
      const v4u cur = af[T % RD];
      static_for<0, 3>([&](auto Nc) {
        constexpr int n = Nc, G = k * 3 + n;
        mfma_w<k == 0, true>(W1F(G), cur, acc[n]);
        if constexpr (m > 0) egap(IC<m - 1>{}, IC<G>{});
        if constexpr (n == 2 && (T & 1) && T + RD < 80) { af[T % RD] = rda(T + RD); SB(); af[(T - 1) % RD] = rda(T + RD - 1); }
        if constexpr (G == 41) { const v2u u0 = *(LAS v2u*)(hr0 + ((m & 1) ? 0 : A3_0)), u1 = *(LAS v2u*)(hr1 + ((m & 1) ? 0 : A3_0)); hk[m] = v4u{u0.x, u0.y, u1.x, u1.y}; }
        if constexpr (m < 2 && n == 0 && k >= 14 && k < 19) ldh(IC<m + 2>{}, IC<k - 14>{});
        if constexpr (m == 0 && n == 1 && k < 14) rider(IC<k>{});
        if constexpr (m > 0 && m < 3 && n == 1 && (k & 1) == 0 && k < 14) rider(IC<14 + (m - 1) * 7 + k / 2>{});
        if constexpr (G >= 46 && G < 58 && G % 3 == 1) stats(m, IC<(G - 43) / 3>{});

        if constexpr (n == 0 && T == 60) br = ldp(rb2, act ? j * 160 + c * 16 : OOB, 0);
        if constexpr (n == 1 && T >= 62 && T < 66) ldw2(IC<32 + T - 62>{});
        if constexpr (n == 0 && T == 61) fs = flag_ld(rws, few, 32, l);
        if constexpr (n == 0 && T >= 66 && T < 78) a4[T - 66] = lda4(T - 66);
        SB();
      });
      if constexpr (k == 19) { epi1(); if constexpr (m >= 1) { waitv<(m == 1 ? 5 : m == 2 ? 0 : 4)>(); flag_st(rws, feo + (m - 1) * 32, tag, true, l); } }
    });
    fs = flag_ld(rws, few + 32, 32, l);
#pragma unroll
    for (int e = 0; e < 6; e++) gu[e] = P(f32x2{__builtin_amdgcn_exp2f(gu[e][0]), __builtin_amdgcn_exp2f(gu[e][1])});
    SB();
#pragma unroll
    for (int e = 0; e < 6; e++) gu[e] = P(gu[e] + f32x2{1.f, 1.f});
    SB();
#pragma unroll
    for (int e = 0; e < 6; e++) gu[e] = P(f32x2{__builtin_amdgcn_rcpf(gu[e][0]), __builtin_amdgcn_rcpf(gu[e][1])});
    SB();
#pragma unroll
    for (int e = 0; e < 6; e++) gu[e] = P(gv[e] * gu[e]);
    SB();
#pragma unroll
    for (int e = 0; e < 6; e++) gp[e] = pk2v(gu[e]);
    gstore(3);
    SB();
    STAMP(18);
    f32x4 ac4[2][5], r[8];
    float b2f[8], ov[8], ow[2];
#pragma unroll
    for (int e = 0; e < 8; e++) b2f[e] = (e & 1) ? bfhi(br[e >> 1]) : bflo(br[e >> 1]);
    auto fop = [&](auto Mc, auto Pc, auto Sc) { constexpr int m = Mc, st = Sc;
      static_for<0, 2>([&](auto Zc) { constexpr int z = Zc, e = 2 * Pc + z;
        if constexpr (st == 0) ov[e] = P(r[e >> 2][e & 3] + r[2 + (e >> 2)][e & 3]);
        if constexpr (st == 1) ow[z] = P(r[4 + (e >> 2)][e & 3] + r[6 + (e >> 2)][e & 3]);
        if constexpr (st == 2) ov[e] = P(ov[e] + ow[z]);
        if constexpr (st == 3) ow[z] = P(z ? bfhi(hk[m][e >> 1]) : bflo(hk[m][e >> 1]));
        if constexpr (st == 4) ow[z] = P(ow[z] + b2f[e]);
        if constexpr (st == 5) ov[e] = P(ov[e] + ow[z]);
        SB(); });
    };
    auto ost = [&](int m) { sts1(v4u{pk2(ov[0], ov[1]), pk2(ov[2], ov[3]), pk2(ov[4], ov[5]), pk2(ov[6], ov[7])}, ro, rowo, m * 20480); };
    SB();
    static_for<0, 48>([&](auto Tc) {
      constexpr int T = Tc, m = T / 12, k = T % 12;
      if constexpr (k == 0) STAMP(34 + 2 * m);
      if constexpr (k == 0 && m < 3) flag_wait(fs, rws, few + (m + 1) * 32, 32, tag, l);
      if constexpr (k == 4) BAR();
      const v4u cur = a4[k];
      static_for<0, 5>([&](auto Nc) { constexpr int n = Nc, G = k * 5 + n;
        mfma_w<k == 0, (G >= 32 || G < 3), true>(bw2[G], cur, ac4[m & 1][n]);
        if constexpr (m == 0 && G < NL2) bw2[60 - NL2 + G] = *(LAS v4u*)(L + W2L_OFF + w * (NL2 * 1024) + G * 1024 + l * 16);
        if constexpr (m > 0 && G >= 3 && G < 18 && G % 3 == 0) *(LAS f32x4*)(rdst + ((m - 1) & 1) * RED_BUF + (G / 3 - 1) * 64) = ac4[(m - 1) & 1][G / 3 - 1];
        if constexpr (m > 0 && G >= 20 && G < 28) { constexpr int q = ((G - 20) & 3) * 2 + ((G - 20) >> 2); r[q] = ((LAS f32x4*)(rsrc + ((m - 1) & 1) * RED_BUF + (q >> 1) * RED_W))[q & 1]; }
        if constexpr (m > 0 && G >= 32 && G < 56) fop(IC<m - 1>{}, IC<(G - 32) / 6>{}, IC<(G - 32) % 6>{});
        if constexpr (m > 0 && G == 57) ost(m - 1);
        if constexpr (m < 3 && n == 4) a4[k] = lda4(m * 12 + k + 12);
        if constexpr (n == 2 && T == 6) { waitv<6>(); flag_st(rws, feo + 96, tag, true, l); }
        if constexpr (n == 2 && m < 2 && k == 8) fs = flag_ld(rws, few + (m + 2) * 32, 32, l);
        SB();
      });
    });
    STAMP(21);
    asm volatile("s_nop 7");
    SB();
#pragma unroll
    for (int n = 0; n < 5; n++) *(LAS f32x4*)(rdst + RED_BUF + n * 64) = ac4[1][n];
    BAR(); red_ld(3, r);
#pragma unroll
    for (int e = 0; e < 8; e++) ov[e] = ((r[e >> 2][e & 3] + r[2 + (e >> 2)][e & 3]) + (r[4 + (e >> 2)][e & 3] + r[6 + (e >> 2)][e & 3])) + (((e & 1) ? bfhi(hk[3][e >> 1]) : bflo(hk[3][e >> 1])) + b2f[e]);
    ost(3);
    flag_st(rws, 0, tag, blockIdx.x == 0 && w == 0, l);
  }
  asm volatile(".Lk_end:");
}

extern "C" void kernel_launch(void* const* d_in, const int* in_sizes, int n_in, void* d_out, int out_size, void* d_ws, size_t ws_size, hipStream_t stream) {
  (void)hipFuncSetAttribute((const void*)k_fused, hipFuncAttributeMaxDynamicSharedMemorySize, 160 * 1024);
  k_fused<<<256, 256, 160 * 1024, stream>>>(d_in[0], d_in[1], d_in[2], d_in[4], d_in[5], d_in[6], d_in[7], d_out, (char*)d_ws);
}
```

```cpp
#include <hip/hip_runtime.h>
#include <hip/hip_bf16.h>
#include <stdint.h>
#include <type_traits>

#define LAS __attribute__((address_space(3)))
#define DI __device__ __forceinline__
typedef __attribute__((ext_vector_type(8))) __bf16 bf16x8;
typedef __attribute__((ext_vector_type(2))) __bf16 bf16x2;
typedef __attribute__((ext_vector_type(4))) float f32x4;
typedef __attribute__((ext_vector_type(2))) float f32x2;
typedef __attribute__((ext_vector_type(4))) unsigned v4u;
typedef __attribute__((ext_vector_type(2))) unsigned v2u;
typedef __attribute__((ext_vector_type(4))) short s16x4;
typedef __amdgpu_buffer_rsrc_t rsrc_t;

constexpr int S = 2048, D = 640, F = 1536;
constexpr int ACT = S * D * 2;
constexpr int EPOCH_OFF = 0;
constexpr int F0 = 1;
constexpr int FP = F0 + 256;
constexpr int FC = FP + 256;
constexpr int FD = FC + 1024;
constexpr int FE = FD + 1024;
constexpr int NFLAG = FE + 4096;
constexpr int WQP_OFF = (NFLAG * 256 + 65535) / 65536 * 65536, OF_OFF = WQP_OFF + 32 * 80 * 1024, HF_OFF = OF_OFF + ACT;
constexpr int GF_OFF = HF_OFF + ACT, WOP_OFF = GF_OFF + S * F * 2, W1P_OFF = WOP_OFF + D * D * 2, W2P_OFF = W1P_OFF + D * F * 2;
constexpr int KVP_OFF = W2P_OFF + D * F * 2, KVF_OFF = KVP_OFF + 32 * 32 * 2048, WS_END = KVF_OFF + 32 * 2048;
constexpr int OOB = 0x7ffffff0;
constexpr int SC1 = 16;
constexpr unsigned ONES2 = 0x3f803f80u;

DI rsrc_t mkrs(const void* p, int bytes) { return __builtin_amdgcn_make_buffer_rsrc((void*)p, 0, bytes, 0x00020000); }
DI v4u mkrs4(const void* p, int bytes) { const uint64_t a = (uint64_t)p; return v4u{(unsigned)a, (unsigned)(a >> 32) & 0xffffu, (unsigned)bytes, 0x00020000u}; }
DI f32x4 mfma(v4u a, v4u b, f32x4 c) {
  return __builtin_amdgcn_mfma_f32_16x16x32_bf16(__builtin_bit_cast(bf16x8, a), __builtin_bit_cast(bf16x8, b), c, 0, 0, 0);
}
DI void mfma_t(v4u a, v4u b, f32x4& c) { asm volatile("v_mfma_f32_16x16x32_bf16 %0, %1, %2, %0" : "+v"(c) : "v"(a), "v"(b)); }
DI void mfma_z(v4u a, v4u b, f32x4& c) { asm volatile("v_mfma_f32_16x16x32_bf16 %0, %1, %2, 0" : "=&v"(c) : "v"(a), "v"(b)); }
template <bool FIRST, bool AG, bool BG = false> DI void mfma_w(v4u a, v4u b, f32x4& c) {
#define MF_(CA, CB) do { if constexpr (FIRST) asm volatile("v_mfma_f32_16x16x32_bf16 %0, %1, %2, 0" : "=&v"(c) : CA(a), CB(b)); \
                         else asm volatile("v_mfma_f32_16x16x32_bf16 %0, %1, %2, %0" : "+v"(c) : CA(a), CB(b)); } while (0)
  if constexpr (AG && BG) MF_("a", "a"); else if constexpr (AG) MF_("a", "v"); else if constexpr (BG) MF_("v", "a"); else MF_("v", "v");
#undef MF_
}
template <bool FIRST> DI void mfma_a(v4u a, v4u b, f32x4& c) { if constexpr (FIRST) mfma_z(a, b, c); else mfma_t(a, b, c); }
DI unsigned pk2(float a, float b) { f32x2 f = {a, b}; return __builtin_bit_cast(unsigned, __builtin_convertvector(f, bf16x2)); }
DI float bflo(unsigned u) { return __uint_as_float(u << 16); }
DI float bfhi(unsigned u) { return __uint_as_float(u & 0xffff0000u); }
DI v4u ldp(rsrc_t r, int voff, int soff) { return __builtin_amdgcn_raw_buffer_load_b128(r, voff, soff, 0); }
DI v4u lds1(rsrc_t r, int voff, int soff) { return __builtin_amdgcn_raw_buffer_load_b128(r, voff, soff, SC1); }
DI void sts1(v4u v, rsrc_t r, int voff, int soff) { asm volatile("" : "+v"(v)); __builtin_amdgcn_raw_buffer_store_b128(v, r, voff, soff, SC1); asm volatile("s_nop 1" :: "v"(v) : "memory"); }
DI v2u trread(LAS char* p) { return __builtin_bit_cast(v2u, __builtin_amdgcn_ds_read_tr16_b64_v4i16((LAS s16x4*)p)); }
#define WAITV(n) asm volatile("s_waitcnt vmcnt(" #n ")" ::: "memory")
#define BAR() do { asm volatile("s_waitcnt lgkmcnt(0)" ::: "memory"); __builtin_amdgcn_s_barrier(); asm volatile("" ::: "memory"); } while (0)
#define SB() __builtin_amdgcn_sched_barrier(0)
constexpr float LOG2E = 1.4426950408889634f;
DI float phi(float t) { return fmaxf(t, 0.f) + __builtin_amdgcn_fmed3f(__builtin_amdgcn_exp2f(t * LOG2E), 0.f, 1.f) + 1e-6f; }
template <class T> DI void pin(T& x) { asm volatile("" : "+v"(x)); }
template <class T> DI T P(T x) { pin(x); return x; }
template <class T> DI T PS(T x) { asm volatile("" : "+s"(x)); return x; }
DI f32x2 fma2(f32x2 a, f32x2 b, f32x2 c) { return __builtin_elementwise_fma(a, b, c); }
DI f32x2 lo2(f32x4 v) { return f32x2{v[0], v[1]}; }
DI f32x2 hi2(f32x4 v) { return f32x2{v[2], v[3]}; }
DI unsigned pk2v(f32x2 f) { return __builtin_bit_cast(unsigned, __builtin_convertvector(f, bf16x2)); }
DI float maxf(float a, float b) { float r; asm("v_max_f32 %0, %1, %2" : "=v"(r) : "v"(a), "v"(b)); return r; }
DI unsigned phi2(f32x2 u) {
  const f32x2 a = fma2(u, f32x2{LOG2E, LOG2E}, f32x2{-LOG2E, -LOG2E});
  const float e0 = __builtin_amdgcn_fmed3f(__builtin_amdgcn_exp2f(a[0]), 0.f, 1.f), e1 = __builtin_amdgcn_fmed3f(__builtin_amdgcn_exp2f(a[1]), 0.f, 1.f);
  return pk2v(f32x2{__builtin_amdgcn_fmed3f(u[0], e0, 3.0e38f), __builtin_amdgcn_fmed3f(u[1], e1, 3.0e38f)} + f32x2{1e-6f, 1e-6f});
}
template <int N> DI void phi_n(const f32x2 (&u)[N], unsigned (&o)[N]) {
  f32x2 a[N];
#pragma unroll
  for (int e = 0; e < N; e++) a[e] = P(fma2(u[e], f32x2{LOG2E, LOG2E}, f32x2{-LOG2E, -LOG2E}));
  SB();
#pragma unroll
  for (int e = 0; e < N; e++) a[e] = P(f32x2{__builtin_amdgcn_fmed3f(__builtin_amdgcn_exp2f(a[e][0]), 0.f, 1.f), __builtin_amdgcn_fmed3f(__builtin_amdgcn_exp2f(a[e][1]), 0.f, 1.f)});
  SB();
#pragma unroll
  for (int e = 0; e < N; e++) a[e] = P(f32x2{__builtin_amdgcn_fmed3f(u[e][0], a[e][0], 3.0e38f), __builtin_amdgcn_fmed3f(u[e][1], a[e][1], 3.0e38f)});
  SB();
#pragma unroll
  for (int e = 0; e < N; e++) a[e] = P(a[e] + f32x2{1e-6f, 1e-6f});
  SB();
#pragma unroll
  for (int e = 0; e < N; e++) o[e] = pk2v(a[e]);
  SB();
}
DI float dot2(unsigned a, unsigned b, float c) { return __builtin_amdgcn_fdot2_f32_bf16(__builtin_bit_cast(bf16x2, a), __builtin_bit_cast(bf16x2, b), c, false); }
DI float gelu(float v) {
  const float u = v * (0.7978845608028654f * 2.f * LOG2E) * __builtin_fmaf(v * v, 0.044715f, 1.0f);
  return v * __builtin_amdgcn_rcpf(1.0f + __builtin_amdgcn_exp2f(-u));
}
template <int CTRL> DI float dppadd(float v) { return v + __uint_as_float(__builtin_amdgcn_update_dpp(0u, __float_as_uint(v), CTRL, 0xf, 0xf, true)); }
DI float sum16(float v) { v = dppadd<0xB1>(v); v = dppadd<0x4E>(v); v = dppadd<0x141>(v); return dppadd<0x140>(v); }
DI float add_x16(float v) { auto r = __builtin_amdgcn_permlane16_swap(__float_as_uint(v), __float_as_uint(v), false, false); return __uint_as_float(r[0]) + __uint_as_float(r[1]); }
DI float add_x32(float v) { auto r = __builtin_amdgcn_permlane32_swap(__float_as_uint(v), __float_as_uint(v), false, false); return __uint_as_float(r[0]) + __uint_as_float(r[1]); }

DI void glds4(v4u rs, unsigned dst, int soff, int v0, int v1, int v2, int v3) {
  asm volatile("s_nop 4\n\ts_mov_b32 m0, %1\n\ts_nop 0\n\tbuffer_load_dwordx4 %3, %0, %2 offen lds\n\t"
               "s_add_u32 m0, m0, 0x400\n\ts_nop 0\n\tbuffer_load_dwordx4 %4, %0, %2 offen lds\n\t"
               "s_add_u32 m0, m0, 0x400\n\ts_nop 0\n\tbuffer_load_dwordx4 %5, %0, %2 offen lds\n\t"
               "s_add_u32 m0, m0, 0x400\n\ts_nop 0\n\tbuffer_load_dwordx4 %6, %0, %2 offen lds"
               :: "s"(rs), "s"(dst), "s"(soff), "v"(v0), "v"(v1), "v"(v2), "v"(v3) : "memory", "scc");
}
template <int IMM> DI void glds1(v4u rs, unsigned base, int soff, int v) {
  asm volatile("s_add_u32 m0, %1, %4\n\ts_nop 0\n\tbuffer_load_dwordx4 %3, %0, %2 offen lds" :: "s"(rs), "s"(base), "s"(soff), "v"(v), "i"(IMM) : "memory", "scc");
}
template <int IMM> DI void glds1_sc1(v4u rs, unsigned base, int soff, int v) {
  asm volatile("s_add_u32 m0, %1, %4\n\ts_nop 0\n\tbuffer_load_dwordx4 %3, %0, %2 offen sc1 lds" :: "s"(rs), "s"(base), "s"(soff), "v"(v), "i"(IMM) : "memory", "scc");
}
template <int IMM> DI void set_m0(unsigned base) { asm volatile("s_add_u32 m0, %0, %1" :: "s"(base), "i"(IMM) : "memory", "scc"); }
template <int OFF> DI void glds_o(v4u rs, int soff, int v) { asm volatile("buffer_load_dwordx4 %2, %0, %1 offen offset:%3 sc1 lds" :: "s"(rs), "s"(soff), "v"(v), "i"(OFF) : "memory"); }
template <int N> using IC = std::integral_constant<int, N>;
template <int N> DI void waitv() { asm volatile("s_waitcnt vmcnt(%0)" :: "i"(N) : "memory"); }
template <int I, int N, class Fn> DI void static_for(Fn&& f) { if constexpr (I < N) { f(std::integral_constant<int, I>{}); static_for<I + 1, N>(f); } }
#define PIN(x) pin(x)
#define STAMP(id)
#define STAMP_ARG
#define STAMP_PASS
#define CSTAMP(id)

#define SLP 0
DI unsigned flag_ld(rsrc_t rws, int f, int n, int l) { asm volatile("" ::: "memory"); return __builtin_amdgcn_raw_buffer_load_b32(rws, (l < n ? l : 0) * 256, f * 256, SC1); }
DI void flag_wait(unsigned v, rsrc_t rws, int f, int n, unsigned tag, int l) {
  if (__builtin_expect(!__all(v == tag), 0)) {
#pragma unroll 1
    for (int sp = 0; sp < (1 << 16); sp++) { if (__all(flag_ld(rws, f, n, l) == tag)) break; }
  }
  asm volatile("" ::: "memory");
}
DI void wait_flags(rsrc_t rws, int f, int n, unsigned tag, int l) { flag_wait(flag_ld(rws, f, n, l), rws, f, n, tag, l); }
DI void flag_st(rsrc_t rws, int f, unsigned tag, bool me, int l) {
  asm volatile("" ::: "memory"); sts1(v4u{tag, tag, tag, tag}, rws, (me && l < 8) ? l * 16 : OOB, f * 256); asm volatile("" ::: "memory");
}

template <int MAT> DI int frag_off(int T, int ks) {
  if constexpr (MAT == 0) return WOP_OFF + ((T / 5) * 100 + ks * 5 + T % 5) * 1024;
  else if constexpr (MAT == 1) return W1P_OFF + ((T / 3) * 60 + ks * 3 + T % 3) * 1024;
  else return W2P_OFF + ((T / 5) * 240 + ks * 5 + T % 5) * 1024;
}
template <int MAT> DI void pack_issue(int ks, int cb, int l, v4u rs, unsigned stg) {
  const int r = l >> 3;
  static_for<0, 4>([&](auto Ic) { constexpr int i = Ic;
    const int sw = ((l & 7) ^ ((((r >> 1) & 1) | ((i & 1) << 1)) << 1)) * 16, sg = 4 * ks + i;
    if constexpr (MAT == 0) glds1<i * 1024>(rs, stg, sg * 8 * 1280 + cb * 128, r * 1280 + sw);
    else if constexpr (MAT == 1) { const int q = ks % 10;
      glds1<i * 1024>(rs, stg, ((2 * i + ks / 10) * 80 + (q < 8 ? 32 * (q >> 2) + 4 * (q & 3) : 8 * q)) * 3072 + cb * 128, (q < 8 ? 16 * (r >> 2) + (r & 3) : r) * 3072 + sw); }
    else { const int q = sg % 6; glds1<i * 1024>(rs, stg, (48 * (sg / 6) + (q < 4 ? 4 * q : 8 * q)) * 1280 + cb * 128, (q < 4 ? 16 * (r >> 2) + (r & 3) : r) * 1280 + sw); }
  });
}
DI int unit_lane(int t, int l) { const int g = l >> 4, q = (l >> 2) & 3, p = l & 3, f = ((q >> 1) & 1) | ((g & 1) << 1); return (8 * g + q) * 128 + ((t ^ f) * 32) + p * 8; }
DI v4u unit_read(LAS char* p) { const v2u a = trread(p), b = trread(p + 512); return v4u{a.x, a.y, b.x, b.y}; }
template <int MAT> DI void pack_dst(int ks, int cb, int (&d)[4]) {
#pragma unroll
  for (int t = 0; t < 4; t++) d[t] = frag_off<MAT>(cb * 4 + t, ks);
}
DI void pack_finish(const int (&d)[4], const int (&ul)[4], int l, rsrc_t rws, LAS char* stg) {
  v4u f[4];
#pragma unroll
  for (int t = 0; t < 4; t++) f[t] = unit_read(stg + ul[t]);
#pragma unroll
  for (int t = 0; t < 4; t++) sts1(f[t], rws, l * 16, d[t]);
}
constexpr int TAB_OFF = 81920, SCR_OFF = TAB_OFF + 512, SNK_OFF = SCR_OFF + 2048, XT_OFF = 92160, XT_W = 17408, OB_ROW = 176, WO_L = 61440;
constexpr int RED_ROW = 336, RED_W = 16 * RED_ROW, RED_BUF = 4 * RED_W, A3_0 = 2 * RED_BUF, W2L_OFF = 65536;
constexpr int NL2 = 24;
static_assert(SNK_OFF + 1024 <= WO_L + 28 * 1024 && A3_0 + 20480 <= W2L_OFF, "LDS map");

#define N1 (B0 + 18)
#define N2 42
#define N3 50
#define N5 66
#define X1 2
#define X2 8
#define XROT 3
#define B0 15
#define ABL 0
#define FPK 5
#define NORIDE 0
#define RD1 6
#define PQ 50
#define PFK 8
#define RK 12
#define DLY 4
#define FLK 4

__global__ __launch_bounds__(256, 1) void k_fused(const void* x, const void* wqkv, const void* wo, const void* w1, const void* b1p, const void* w2,
                                                  const void* b2p, void* out, char* ws) {
  extern __shared__ __attribute__((aligned(16))) char smem[];
  LAS char* L = (LAS char*)smem;
  const unsigned Lb = (unsigned)(size_t)L;
  STAMP(0);
  const int tid = threadIdx.x, l = tid & 63, w = __builtin_amdgcn_readfirstlane(tid >> 6), c = l & 15, g = l >> 4;
  const int xg = blockIdx.x & 7, i = blockIdx.x >> 3;
  const unsigned Lx = Lb + XT_OFF + w * XT_W;
  LAS char* xt = L + XT_OFF + w * XT_W;
  {
    uint64_t pc; unsigned len;
    asm volatile("s_getpc_b64 %0\n.Lk_here:\n\ts_mov_b32 %1, .Lk_end-.Lk_here\n\ts_nop 2" : "=s"(pc), "=s"(len));
    glds1<SNK_OFF>(mkrs4((const void*)pc, len & ~15u), Lb, (i * 4 + w) * 640, l < 40 ? l * 16 : OOB);
  }
  const int grp = xg * 4 + (i >> 3), j = i & 7;
  const bool p0 = j < 5;
  const int pk = (i >> 3) * 5 + j;
  const v4u rx4 = mkrs4(x, S * D * 2), rws4 = mkrs4(ws, WS_END);
  const rsrc_t rws = mkrs(ws, WS_END);
  STAMP(19);
  const int uc = 88 + (grp * 5 + j) * 3 + w - 1;
  const bool hasc = p0 && w > 0 && uc < 480;
  const int fi = grp * 3 + j - 5;
  if (p0) {
    const v4u rw4 = mkrs4(wqkv, D * 1920 * 2);
    const int rr = l / 30, e = l - rr * 30, sec = e / 10;
    const int v0 = l < 60 ? rr * 3840 + sec * 1280 + (e - sec * 10) * 16 : OOB, s0 = (32 * pk + 8 * w) * 3840 + xg * 160;
    static_for<0, 4>([&](auto Uc) { constexpr int u = Uc; glds1<u * 1024>(rw4, Lx, s0 + u * 2 * 3840, v0); });
  }
  glds1_sc1<16384>(rws4, Lx, 0, l == 0 ? EPOCH_OFF : (l == 1 ? (p0 ? F0 + xg * 32 + pk : FP + fi) * 256 : OOB));
  int va[2];
#pragma unroll
  for (int u = 0; u < 2; u++) va[u] = (grp * 64 + w * 16 + u * 8 + (l >> 3)) * 1280 + (((l & 7) ^ (g | (u << 2))) * 16);
  auto ldx = [&](auto Qc) { constexpr int q = Qc; int ln = (q >> 1) + (p0 ? 0 : (j - 5) * 3); ln = ln >= 10 ? ln - 10 : ln;
    glds1<(q & 1) * 1024>(rx4, Lb + w * 2048 + ln * 8192, ln * 128, va[q & 1]); };
  int ul[4], dc[4];
#pragma unroll
  for (int t = 0; t < 4; t++) ul[t] = unit_lane(t, l);
  unsigned tag = 0;
  auto get_tag = [&]() { tag = __builtin_amdgcn_readfirstlane(*(LAS unsigned*)(xt + 16384)) + 1u; };
  if (p0) {
    {
    const char* tp = l < 8 ? (const char*)wo + l * 102400 : l < 24 ? (const char*)w1 + (l - 8) * 122880 : l < 40 ? (const char*)w2 + (l - 24) * 122880 : l < 56 ? (const char*)out + (l - 40) * 163840 : l < 60 ? (const char*)b1p : (const char*)b2p;
    asm volatile("s_mov_b32 m0, %1\n\ts_nop 0\n\tglobal_load_lds_dword %0, off" :: "v"(tp), "s"(Lb + SNK_OFF) : "memory");
    }
    const int q = (l >> 2) & 3, p = l & 3;
    const int tp0 = P((q >> 1) * 1024 + (q & 1) * 480 + (g == 0 ? 160 + 8 * p : g == 1 ? 320 + 8 * p : g == 2 ? (p == 0 ? 192 : p == 1 ? 352 : 32) : 8 * p));
    const int vd = P(g * 1024 + c * 16), sd = PS(WQP_OFF + (xg * 80 + pk) * 4096 + w * 256);
    pack_dst<2>(uc % 48, uc / 48, dc);
#pragma unroll
    for (int t = 0; t < 4; t++) dc[t] = PS(dc[t]);
    waitv<2>();
    STAMP(20);
    v4u f[4];
#pragma unroll
    for (int t = 0; t < 4; t++) { const v2u a = trread(xt + tp0 + 40 * t), b = trread(xt + tp0 + 40 * t + 2048); f[t] = v4u{a.x, a.y, b.x, b.y}; }
#pragma unroll
    for (int t = 0; t < 4; t++) sts1(f[t], rws, vd, sd + t * 81920);
    STAMP(1);
    static_for<0, X2>(ldx);
    waitv<X2>();
    get_tag();
    BAR(); flag_st(rws, F0 + xg * 32 + pk, tag, w == 0, l);
    STAMP(2);
    static_for<X2, 20>(ldx);
    if (hasc) pack_issue<2>(uc % 48, uc / 48, l, mkrs4(w2, D * F * 2), Lx + 4096);
  } else {
    static_for<0, 8>(ldx);
#pragma unroll 1
    for (int r = 0; r < 2; r++) { const int u = fi * 8 + w * 2 + r;
      if (u < 200) pack_issue<0>(u % 20, u / 20, l, mkrs4(wo, D * D * 2), Lx + r * 4096);
      else if (u < 680) pack_issue<1>(u % 20, u / 20 - 10, l, mkrs4(w1, D * F * 2), Lx + r * 4096);
      else pack_issue<2>((u - 680) % 48, (u - 680) / 48, l, mkrs4(w2, D * F * 2), Lx + r * 4096); }
    static_for<8, 20>(ldx);
    STAMP(20);
    waitv<12>();
    get_tag();
#pragma unroll 1
    for (int r = 0; r < 2; r++) { const int u = fi * 8 + w * 2 + r;
      int d[4];
      if (u < 200) pack_dst<0>(u % 20, u / 20, d); else if (u < 680) pack_dst<1>(u % 20, u / 20 - 10, d); else pack_dst<2>((u - 680) % 48, (u - 680) / 48, d);
      pack_finish(d, ul, l, rws, xt + r * 4096); }
    STAMP(2);
  }
  const bool mid = c >= 4 && c < 12;
  const int c4 = c < 4 ? c : 4;
  const int vk1 = P(mid ? 1024 + (12 + g * 8 + c) * 16 : OOB), vg0 = P(g == 0 ? c * 16 : OOB), vk3 = P((g == 0 && mid) ? 1024 + (44 + c) * 16 : OOB);
  const int vsc = P(c >= 12 ? SCR_OFF + w * 512 + g * 64 + (c - 12) * 16 : SNK_OFF + l * 8);
  const int vpp = P(g * 2048 + ((xg == 7 && c >= 8) ? 7 : c) * 16), vf2 = P(1024 + (16 + g * 8 + c4) * 16), vf3 = P(1024 + (48 + c4) * 16);
  const int vtc = P(l < 28 ? (l < 16 ? FE + grp * 128 + (l >> 2) * 32 + j * 4 + (l & 3) : (l < 20 ? FC : FD) + grp * 32 + (l & 3) * 8 + j) * 256 + (l >= 24 ? 128 : 0) : OOB);
  const int vhf = P((g & 1) ? OOB : (g >> 1) * 256 + c * 16);
  LAS char* ap[2];
#pragma unroll
  for (int kk = 0; kk < 2; kk++) ap[kk] = L + c * 128 + (((4 * kk + g) ^ ((c >> 1) & 7)) * 16);
  auto rdx = [&](int ks, int m) { return *(LAS v4u*)(ap[ks & 1] + (ks >> 1) * 8192 + m * 2048); };
  auto ldf = [&](int base, int q) { return lds1(rws, l * 16 + (q & 3) * 1024, base + (q >> 2) * 4096); };
  const int hd = 4 * j + w;
  v4u bq[80];
  auto ldq = [&](auto Qc) { constexpr int q = Qc, f = q < 60 ? (q / 3) * 4 + 2 - q % 3 : (q - 60) * 4 + 3; bq[f] = ldf(WQP_OFF + hd * 81920, f); };
  STAMP(3);
  if (w == 0) wait_flags(rws, F0 + j * 32, 20, tag, l);
  BAR();
  STAMP(4);
  static_for<0, B0 - 10>(ldq); SB();
  if (hasc) waitv<B0 - 6>(); else if (p0) waitv<B0 - 10>(); else waitv<B0 - 2>();
  {
    v4u a[20];
#pragma unroll
    for (int ks = 0; ks < 20; ks++) a[ks] = *(LAS v4u*)(ap[ks & 1] + (ks >> 1) * 8192 + w * 2048);
    SB();
    float s[2] = {0.f, 0.f}, q[2] = {0.f, 0.f};
    static_for<0, 20>([&](auto Kc) { constexpr int ks = Kc;
      if constexpr (ks % 2 == 0) ldq(IC<B0 - 10 + ks / 2>{});
#pragma unroll
      for (int e = 0; e < 4; e++) { s[e & 1] = dot2(a[ks][e], ONES2, s[e & 1]); q[e & 1] = dot2(a[ks][e], a[ks][e], q[e & 1]); }
      SB(); });
    const float mean = add_x32(add_x16(s[0] + s[1])) * (1.f / D);
    const float rs = __builtin_amdgcn_rsqf(fmaxf(add_x32(add_x16(q[0] + q[1])) * (1.f / D) - mean * mean, 0.f) + 1e-5f);
    *(LAS float*)(L + TAB_OFF + (g & 1) * 256 + (w * 16 + c) * 4) = (g & 1) ? -mean * rs : rs;
  }
  if (!p0) waitv<B0>();
  BAR();
  if (!p0) flag_st(rws, FP + fi, tag, w == 0, l);
  v4u ones = {ONES2, ONES2, ONES2, ONES2};
  PIN(ones);
  v4u fa[2][4];
  unsigned fp = 0;
  const int fpa = FP + w * 24, fpn = 24;
  v4u bw1[60], bw2[60];
  const int w1b = W1P_OFF + hd * 61440, w2b = W2P_OFF + hd * 61440;
  auto ldw2 = [&](auto Qc) { constexpr int q = Qc; if constexpr (q >= 0 && q < 60) bw2[q] = ldf(w2b, q); };
  auto bulk = [&](auto Qc) { constexpr int q = Qc; if constexpr (q < 60) bw1[q] = ldf(w1b, q); else ldw2(IC<q - 60>{}); };
  auto run = [&](auto Nc, unsigned lds, int mem) { static_for<0, Nc>([&](auto Qc) { constexpr int q = Qc;
    if constexpr (q % 4 == 0) { set_m0<q * 1024>(lds); asm volatile("s_nop 0"); }
    glds_o<(q % 4) * 1024>(rws4, mem + (q / 4) * 4096, l * 16); }); };
#define W1F(r) bw1[r]
  {
    f32x4 acc[4][3], cs[3], trs[4], tnm[4];
    LAS char* tabp = L + TAB_OFF + g * 16;
#pragma unroll
    for (int m = 0; m < 4; m++) fa[0][m] = rdx(0, m);
    SB();
    static_for<0, 20>([&](auto Kc) { constexpr int ks = Kc;
      if constexpr (ks == PFK) { if (hasc) pack_finish(dc, ul, l, rws, xt + 4096); SB(); }
      if constexpr (ks == 19) { flag_wait(fp, rws, fpa, fpn, tag, l); BAR(); flag_st(rws, FP + 96 + grp * 5 + j, tag, p0 && w == 0, l); }
      if constexpr (ks == 5) STAMP(23);
      static_for<0, 15>([&](auto Qc) { constexpr int Q = Qc, n = Q / 5, m = Q % 5;
        if constexpr (m < 4) { if constexpr (!(ABL & 4) || m == 0 || ks == 0) mfma_w<ks == 0, false, true>(fa[ks & 1][m], bq[ks * 4 + n], acc[m][n]); } else if constexpr (!(ABL & 2) || ks == 0) mfma_w<ks == 0, false, true>(ones, bq[ks * 4 + n], cs[n]);
        if constexpr (Q >= 1 && Q < 9 && (Q & 1)) fa[(ks + 1) & 1][3 - Q / 2] = rdx(ks < 19 ? ks + 1 : 0, 3 - Q / 2);
        if constexpr (m == 2 && B0 + ks * 3 + n < 80) ldq(IC<B0 + ks * 3 + n>{});
        if constexpr (ks == 15 && Q == 0) fp = flag_ld(rws, fpa, fpn, l);
        if constexpr (ks >= 18 && Q >= 9 && Q < 13) (ks == 18 ? trs : tnm)[Q - 9] = *(LAS f32x4*)(tabp + (ks - 18) * 256 + (Q - 9) * 64);
        SB(); });
    });
    static_assert(B0 >= 12 && B0 <= 20, "");
    asm volatile("s_nop 7");
#pragma unroll
    for (int n = 0; n < 3; n++) pin(cs[n]);
    STAMP(5);
    v4u fk[2], fv[2], fr[2], fb[2];
    const f32x2 one2 = {1.f, 1.f}, mk = c < 8 ? one2 : f32x2{0.f, 0.f}, ck = c < 8 ? -one2 : one2;
    auto ride0 = [&](auto Ec) { if constexpr (B0 + 60 + Ec < 80) ldq(IC<B0 + 60 + Ec>{}); else bulk(IC<B0 + Ec - 20>{}); };
    auto ride = [&](auto Ec) { if constexpr (!NORIDE) ride0(Ec); };
    auto unused_ = [&](auto Ec) { if constexpr (B0 + 60 + Ec < 80) ldq(IC<B0 + 60 + Ec>{}); else bulk(IC<B0 + Ec - 20>{}); };
    static_for<0, 2>([&](auto Uc) { constexpr int u = Uc;
      f32x2 t[3][4], uu[8];
      unsigned o[8];
#pragma unroll
      for (int e = 0; e < 4; e++) { const f32x2 nm = (e & 1) ? hi2(tnm[2 * u + (e >> 1)]) : lo2(tnm[2 * u + (e >> 1)]);
        t[0][e] = P(fma2(nm, lo2(cs[0]), one2)); t[1][e] = P(nm * lo2(cs[1])); t[2][e] = P(fma2(nm, lo2(cs[2]), one2)); }
      ride(IC<4 * u>{});
      SB();
#pragma unroll
      for (int e = 0; e < 4; e++) { const int m = 2 * u + (e >> 1); const f32x2 rs = (e & 1) ? hi2(trs[m]) : lo2(trs[m]);
        uu[e] = fma2(rs, (e & 1) ? hi2(acc[m][0]) : lo2(acc[m][0]), t[0][e]); uu[4 + e] = fma2(rs, (e & 1) ? hi2(acc[m][2]) : lo2(acc[m][2]), t[2][e]);
        t[1][e] = fma2(rs, (e & 1) ? hi2(acc[m][1]) : lo2(acc[m][1]), t[1][e]); }
      ride(IC<4 * u + 1>{});
      SB();
#pragma unroll
      for (int e = 0; e < 4; e++) { t[2][e] = fma2(uu[4 + e], mk, ck); fv[u][e] = pk2v(t[1][e]); }
      ride(IC<4 * u + 2>{});
      phi_n<8>(uu, o);
#pragma unroll
      for (int e = 0; e < 4; e++) { fb[u][e] = pk2v(t[2][e]); fk[u][e] = o[e]; fr[u][e] = o[4 + e]; }
      ride(IC<4 * u + 3>{});
      SB();
    });
#pragma unroll
    for (int u = 0; u < 2; u++) { pin(fk[u]); pin(fv[u]); pin(fr[u]); pin(fb[u]); }
    asm volatile("s_nop 1");
    f32x4 t00, t01, t10, t11;
    mfma_z(fk[0], fv[0], t00); mfma_z(fk[0], fb[0], t01); mfma_z(fr[0], fv[0], t10); mfma_z(fr[0], fb[0], t11);
    mfma_t(fk[1], fv[1], t00); mfma_t(fk[1], fb[1], t01); mfma_t(fr[1], fv[1], t10); mfma_t(fr[1], fb[1], t11);
#pragma unroll
    for (int u = 0; u < 2; u++) *(LAS v4u*)(L + (vsc & ~15) + u * 256) = fr[u];
    asm volatile("s_nop 5");
    SB();
    const int kvo = KVP_OFF + (hd * 32 + grp) * 2048;
    sts1(v4u{pk2(t00[0], t00[1]), tag, pk2(t00[2], t00[3]), tag}, rws, l * 16, kvo);
    sts1(v4u{pk2(t01[0], t01[1]), tag, pk2(t01[2], t01[3]), tag}, rws, vk1, kvo);
    sts1(v4u{pk2(t10[0], t10[1]), tag, pk2(t10[2], t10[3]), tag}, rws, vg0, kvo + 1024);
    sts1(v4u{pk2(t11[0], t11[1]), tag, pk2(t11[2], t11[3]), tag}, rws, vk3, kvo);
    STAMP(6);
    if constexpr (NORIDE) static_for<0, 8>(ride0);
  }
  if (w == ((grp + 1) & 3)) glds1<SNK_OFF>(rws4, Lb, 0, vtc);
  const bool act = c < 10;
  v2u xr[5];
  {
    f32x4 acc[4], cs;
    float qrs[4], qnm[4];
    v2u q1[4];
    static_for<0, 21>([&](auto Kc) { constexpr int ks = Kc;
      if constexpr (ks == RK) {
        STAMP(7);
        if (w == (grp & 3)) {
          v4u pp[8];
#pragma unroll 1
          for (int sp = 0; sp < (1 << 16); sp++) {
            asm volatile("" ::: "memory");
#pragma unroll
            for (int q = 0; q < 8; q++) pp[q] = lds1(rws, vpp, KVP_OFF + (hd * 32 + 4 * q) * 2048 + xg * 256);
            SB();
            unsigned bad = 0;
#pragma unroll
            for (int q = 0; q < 8; q++) bad |= (pp[q].y ^ tag) | (pp[q].w ^ tag);
            if (__all(bad == 0)) break;
          }
          float sm[4] = {0.f, 0.f, 0.f, 0.f};
#pragma unroll
          for (int q = 0; q < 8; q++) { sm[0] += bflo(pp[q].x); sm[1] += bfhi(pp[q].x); sm[2] += bflo(pp[q].z); sm[3] += bfhi(pp[q].z); }
#pragma unroll
          for (int e = 0; e < 4; e++) sm[e] = add_x32(add_x16(sm[e]));
          sts1(v4u{pk2(sm[0], sm[1]), tag, pk2(sm[2], sm[3]), tag}, rws, vg0, KVF_OFF + hd * 2048 + xg * 256);
        } else { const int f = (28 + ((w - grp - 1) & 3) * 24) * 1024; run(IC<24>{}, Lb + WO_L + f, WOP_OFF + j * 102400 + f); }
        SB();
        STAMP(8);
      }
      if constexpr (ks < 20) static_for<0, 5>([&](auto Qc) { constexpr int m = Qc;
        if constexpr (m < 4) mfma_a<ks == 0>(bq[ks * 4 + 3], fa[ks & 1][m], acc[m]); else mfma_a<ks == 0>(bq[ks * 4 + 3], ones, cs);
        if constexpr (ks < 19 && m < 4) fa[(ks + 1) & 1][3 - m] = rdx(ks + 1, 3 - m);
        if constexpr (m == 4 || (m == 1 && (ks & 1))) bulk(IC<B0 - 12 + (ks / 2) * 3 + (ks & 1) + (m == 4 && (ks & 1))>{});
        SB(); });
    });
    {
      LAS char* qp = L + SCR_OFF + w * 512 + (l & 15) / 4 * 16 + (l & 3) * 64;
#pragma unroll
      for (int m = 0; m < 4; m++) {
        qrs[m] = *(LAS float*)(L + TAB_OFF + c * 4 + m * 64); qnm[m] = *(LAS float*)(L + TAB_OFF + c * 4 + 256 + m * 64);
        q1[m] = trread(qp + (m >> 1) * 256 + (m & 1) * 8);
      }
#pragma unroll
      for (int n = 0; n < 5; n++) { const int sg = 10 * j + 2 * n + (g >> 1);
        xr[n] = *(LAS v2u*)(L + (sg >> 3) * 8192 + (w * 16 + c) * 128 + (((sg & 7) ^ ((c >> 1) & 7)) * 16) + (g & 1) * 8); }
    }
    asm volatile("s_nop 7");
    SB();
    STAMP(9);
    v4u ka0, ka1, kb0, kb1;
    auto kv_ld = [&]() { asm volatile("" ::: "memory");
      ka0 = lds1(rws, l * 16, KVF_OFF + hd * 2048); ka1 = lds1(rws, 1024 + c * 16, KVF_OFF + hd * 2048);
      kb0 = lds1(rws, vf2, KVF_OFF + hd * 2048); kb1 = lds1(rws, vf3, KVF_OFF + hd * 2048); };
    auto kv_ok = [&]() { return __all(((ka0.y ^ tag) | (ka0.w ^ tag) | (ka1.y ^ tag) | (ka1.w ^ tag) | (kb0.y ^ tag) | (kb0.w ^ tag) | (kb1.y ^ tag) | (kb1.w ^ tag)) == 0); };
    const unsigned fq = flag_ld(rws, FP + 96 + w * 33, 33, l);
    static_for<N1, N2>(bulk); SB();
    kv_ld();
    v4u pq[4];
    {
      f32x2 uu[8];
      unsigned o[8];
#pragma unroll
      for (int e = 0; e < 8; e++) uu[e] = P(fma2(f32x2{qnm[e >> 1], qnm[e >> 1]}, (e & 1) ? hi2(cs) : lo2(cs), f32x2{1.f, 1.f}));
      SB();
#pragma unroll
      for (int e = 0; e < 8; e++) uu[e] = P(fma2(f32x2{qrs[e >> 1], qrs[e >> 1]}, (e & 1) ? hi2(acc[e >> 1]) : lo2(acc[e >> 1]), uu[e]));
      SB();
      phi_n<8>(uu, o);
#pragma unroll
      for (int m = 0; m < 4; m++) { pq[m] = v4u{o[2 * m], o[2 * m + 1], q1[m].x, q1[m].y}; pin(pq[m]); }
    }
    flag_wait(fq, rws, FP + 96 + w * 33, 33, tag, l);
    BAR();
    STAMP(10);
    if (__builtin_expect(!kv_ok(), 0)) {
#pragma unroll 1
      for (int sp = 0; sp < (1 << 16); sp++) { kv_ld(); if (kv_ok()) break; }
    }
    STAMP(24);
    v4u kf0 = {ka0.x, ka0.z, g == 3 ? ka1.x : 0u, g == 3 ? ka1.z : 0u}, kf1 = {kb0.x, kb0.z, g == 3 ? kb1.x : 0u, g == 3 ? kb1.z : 0u};
    pin(kf0); pin(kf1);
    asm volatile("s_nop 1");
    f32x4 n0[4], n1[4];
    LAS char* ob0 = L + c * OB_ROW + w * 40 + g * 8;
    LAS char* ob1 = L + c * OB_ROW + (g == 0 ? w * 40 + 32 : 160);
#pragma unroll
    for (int m = 0; m < 4; m++) { mfma_z(kf0, pq[m], n0[m]); mfma_z(kf1, pq[m], n1[m]); }
    asm volatile("s_nop 7");
    SB();
    float rr[4];
#pragma unroll
    for (int m = 0; m < 4; m++) rr[m] = __uint_as_float(__builtin_amdgcn_permlane16_swap(__float_as_uint(n1[m][0]), __float_as_uint(n1[m][0]), false, false)[1]);
    SB();
#pragma unroll
    for (int m = 0; m < 4; m++) rr[m] = P(rr[m] + 1e-6f);
    SB();
#pragma unroll
    for (int m = 0; m < 4; m++) rr[m] = P(__builtin_amdgcn_rcpf(rr[m]));
    SB();
#pragma unroll
    for (int m = 0; m < 4; m++) { n0[m] = P(n0[m] * rr[m]); n1[m] = P(n1[m] * rr[m]); }
    SB();
#pragma unroll
    for (int m = 0; m < 4; m++) {
      *(LAS v2u*)(ob0 + m * 16 * OB_ROW) = v2u{pk2v(lo2(n0[m])), pk2v(hi2(n0[m]))};
      *(LAS v2u*)(ob1 + m * 16 * OB_ROW) = v2u{pk2v(lo2(n1[m])), pk2v(hi2(n1[m]))};
    }
    BAR();
    v4u o[3];
#pragma unroll
    for (int q = 0; q < 3; q++) o[q] = *(LAS v4u*)(L + (w * 16 + c) * OB_ROW + (q * 4 + (q == 2 ? g & 1 : g)) * 16);
#pragma unroll
    for (int q = 0; q < 3; q++) sts1(o[q], rws, (q < 2 || g < 2) ? l * 16 + q * 1024 : OOB, OF_OFF + (grp * 4 + w) * 20480 + j * 2560);
    STAMP(25);
    SB();
    static_for<N2, N3>(bulk); SB();
    waitv<N3 - N2>();
    flag_st(rws, FC + grp * 32 + w * 8 + j, tag, true, l);
    run(IC<7>{}, Lb + WO_L + w * 7168, WOP_OFF + j * 102400 + w * 7168);
    STAMP(11);
  }
  LAS char* rdst = L + w * RED_W + c * RED_ROW + g * 16;
  LAS char* rsrc = L + (4 * w + g) * RED_ROW + (act ? c : 9) * 32;
  const int rowo = act ? (grp * 64 + 4 * w + g) * 1280 + j * 160 + c * 16 : OOB;
  auto red_ld = [&](int m, f32x4 (&r)[8]) {
#pragma unroll
    for (int ww = 0; ww < 4; ww++) { LAS f32x4* p = (LAS f32x4*)(rsrc + (m & 1) * RED_BUF + ww * RED_W); r[2 * ww] = p[0]; r[2 * ww + 1] = p[1]; }
  };
  v4u hs[4][2], hk[4];
  float hsa[4], hsb[4];
  const int hsv = g * 512 + c * 16;
  auto h_poll = [&](int m) { asm volatile("" ::: "memory");
    hs[m][0] = lds1(rws, hsv, (FD + grp * 32 + m * 8) * 256); hs[m][1] = lds1(rws, hsv + 256, (FD + grp * 32 + m * 8) * 256); };
  auto h_ok = [&](int m) { return __all(((hs[m][0].y ^ tag) | (hs[m][0].w ^ tag) | (hs[m][1].y ^ tag) | (hs[m][1].w ^ tag)) == 0); };
  auto h_wait = [&](int m) {
    if (__builtin_expect(!h_ok(m), 0)) {
#pragma unroll 1
      for (int sp = 0; sp < (1 << 16); sp++) { h_poll(m); if (h_ok(m)) break; }
    }
    asm volatile("" ::: "memory");
    hsa[m] = P(__uint_as_float(hs[m][0].x) + __uint_as_float(hs[m][1].x)); hsb[m] = P(__uint_as_float(hs[m][0].z) + __uint_as_float(hs[m][1].z));
  };
  v2u bias[3];
  const unsigned La = Lb + w * 5120;
  const int hso = HF_OFF + grp * 81920 + w * 5120;
  auto ldh = [&](auto Mc, auto Qc) { constexpr int m = Mc, q = Qc; glds1_sc1<((m & 1) ? 0 : A3_0) + q * 1024>(rws4, La, hso + m * 20480 + q * 1024, l * 16); };
  f32x4 cs[3];
  {
    const rsrc_t rb1 = mkrs(b1p, F * 2);
#pragma unroll
    for (int n = 0; n < 3; n++) bias[n] = __builtin_amdgcn_raw_buffer_load_b64(rb1, ((12 * j + 3 * w + n) * 16 + 4 * g) * 2, 0, 0);
    static_for<N3, N5>(bulk); SB();
    wait_flags(rws, FC + grp * 32 + w * 8, 8, tag, l);
    STAMP(12);
    v4u a[20], bo[100];
    auto lda = [&](auto Qc) { constexpr int q = Qc; a[q] = ldf(OF_OFF + (grp * 4 + w) * 20480, q); };
    static_for<0, 12>(lda);
    BAR();
    LAS char* wp[2] = {L + WO_L + l * 16, L + WO_L + 51200 + l * 16};
    auto rdo = [&](int f) { return *(LAS v4u*)(wp[f / 50] + (f % 50) * 1024); };
#pragma unroll
    for (int f = 0; f < 10; f++) bo[f] = rdo(f);
    SB();
    f32x4 acc[5];
    static_for<0, 100>([&](auto Fc) { constexpr int f = Fc, k = f / 5, n = f % 5;
      mfma_a<k == 0>(bo[f], a[k], acc[n]);
      if constexpr (k < 18) bo[(k + 2) * 5 + 4 - n] = rdo((k + 2) * 5 + 4 - n);
      if constexpr (n == 2 && k + 12 < 20) lda(IC<k + 12>{});
      SB(); });
    STAMP(13);
    asm volatile("s_nop 7");
    SB();
    unsigned hp[10];
    float s[2] = {0.f, 0.f}, q[2] = {0.f, 0.f};
#pragma unroll
    for (int n = 0; n < 5; n++) {
      hp[2 * n] = pk2v(lo2(acc[n]) + f32x2{bflo(xr[n].x), bfhi(xr[n].x)}); hp[2 * n + 1] = pk2v(hi2(acc[n]) + f32x2{bflo(xr[n].y), bfhi(xr[n].y)});
    }
    const int hb = HF_OFF + (grp * 4 + w) * 20480 + j * 2560;
    sts1(v4u{hp[0], hp[1], hp[2], hp[3]}, rws, l * 16, hb);
    sts1(v4u{hp[4], hp[5], hp[6], hp[7]}, rws, l * 16 + 1024, hb);
    auto s0 = __builtin_amdgcn_permlane16_swap(hp[8], hp[8], false, false);
    auto s1 = __builtin_amdgcn_permlane16_swap(hp[9], hp[9], false, false);
    sts1(v4u{s0[0], s1[0], s0[1], s1[1]}, rws, vhf, hb + 2048);
    STAMP(14);
#pragma unroll
    for (int e = 0; e < 10; e++) { s[e & 1] = dot2(hp[e], ONES2, s[e & 1]); q[e & 1] = dot2(hp[e], hp[e], q[e & 1]); }
    const v4u hst = {__float_as_uint(add_x32(add_x16(s[0] + s[1]))), tag, __float_as_uint(add_x32(add_x16(q[0] + q[1]))), tag};
    static_for<N5, N5 + RD1>(bulk); SB();
    waitv<RD1>();
    sts1(hst, rws, vg0, (FD + grp * 32 + w * 8 + j) * 256);
    BAR();
    STAMP(15);
    static_assert(N5 >= 60, "w1 has landed");
    asm volatile("s_nop 1");
    static_for<0, 60>([&](auto Qc) { constexpr int q = Qc; mfma_w<(q < 3), true>(W1F(q), ones, cs[q % 3]);
      if constexpr (q % 5 == 0 && q < PQ) bulk(IC<N5 + RD1 + q / 5>{});
      if constexpr (q == PQ) { h_poll(0); h_poll(1); }
      SB(); });
    h_wait(0); h_wait(1);
    STAMP(16);
    run(IC<5>{}, La + A3_0, hso);
    h_poll(2); h_poll(3);
    static_for<N5 + RD1 + PQ / 5, 92>(bulk); SB();
    h_wait(2); h_wait(3);
    run(IC<5>{}, La, hso + 20480);
  }
  {
    const int few = FE + grp * 128, feo = few + j * 4 + w;
    unsigned fs = 0;
    f32x2 bf[6];
#pragma unroll
    for (int e = 0; e < 6; e++) bf[e] = P(f32x2{bflo(bias[e >> 1][e & 1]), bfhi(bias[e >> 1][e & 1])});
    SB();
    BAR();
    STAMP(17);
    LAS char* ab = L + g * 5120 + c * 16;
    auto rda = [&](int t) { return *(LAS v4u*)(ab + (((t / 20) & 1) ? 0 : A3_0) + (t % 20) * 256); };
    const int cc = act ? c : 0;
    LAS char* hr0 = L + j * 2560 + (4 * w + g) * 16 + (cc < 8 ? ((cc >> 2) * 4 + (cc & 1) * 2) * 256 + ((cc >> 1) & 1) * 8 : cc * 256);
    LAS char* hr1 = hr0 + (cc < 8 ? 256 : 8);
    const unsigned L2w = Lb + W2L_OFF + w * (NL2 * 1024);
    constexpr int RD = 6;
    v4u af[RD];
    f32x4 acc[3];
    f32x2 bp[6], gv[6], gu[6];
    unsigned gp[6];
    float rs = 0.f, nr = 0.f, sa = 0.f, sb = 0.f;
    auto stats = [&](int m, auto Pc) { constexpr int p = Pc;
      if constexpr (p == 1) { auto x = __builtin_amdgcn_permlane16_swap(__float_as_uint(hsa[m]), __float_as_uint(hsb[m]), false, false); sa = P(__uint_as_float(x[0]) + __uint_as_float(x[1])); sb = P(sa); }
      if constexpr (p == 2) { auto x = __builtin_amdgcn_permlane32_swap(__float_as_uint(sa), __float_as_uint(sb), false, false); sa = P(__uint_as_float(x[0]) + __uint_as_float(x[1])); sb = P(sa); }
      if constexpr (p == 3) { auto x = __builtin_amdgcn_permlane16_swap(__float_as_uint(sa), __float_as_uint(sb), false, false);
        sa = P(__uint_as_float(x[0]) * (1.f / D)); sb = P(fmaxf(__uint_as_float(x[1]) * (1.f / D) - sa * sa, 0.f)); }
      if constexpr (p == 4) { const float r1 = __builtin_amdgcn_rsqf(sb + 1e-5f);
        rs = P(r1 * __builtin_amdgcn_rsqf(sb * r1 * r1 + 1e-5f));
        nr = P(-sa * rs); }
    };
    auto epi1 = [&]() {
#pragma unroll
      for (int e = 0; e < 6; e++) bp[e] = P(fma2(f32x2{nr, nr}, (e & 1) ? hi2(cs[e >> 1]) : lo2(cs[e >> 1]), bf[e]));
      asm volatile("s_nop 1");
      SB();
      constexpr float C0 = -0.7978845608028654f * 2.f * LOG2E, C1 = C0 * 0.044715f;
      const f32x2 rs2 = {rs, rs};
#pragma unroll
      for (int e = 0; e < 6; e++) gv[e] = P(fma2(rs2, (e & 1) ? hi2(acc[e >> 1]) : lo2(acc[e >> 1]), bp[e]));
      SB();
#pragma unroll
      for (int e = 0; e < 6; e++) gu[e] = P(gv[e] * gv[e]);
      SB();
#pragma unroll
      for (int e = 0; e < 6; e++) gu[e] = P(fma2(gu[e], f32x2{C1, C1}, f32x2{C0, C0}));
      SB();
#pragma unroll
      for (int e = 0; e < 6; e++) gu[e] = P(gu[e] * gv[e]);
      SB();
    };
    auto gstore = [&](int m) {
      const int base = GF_OFF + (grp * 4 + m) * 49152 + 6 * hd * 256;
      sts1(v4u{gp[0], gp[1], gp[2], gp[3]}, rws, l * 16, base);
      auto s0 = __builtin_amdgcn_permlane16_swap(gp[4], gp[4], false, false);
      auto s1 = __builtin_amdgcn_permlane16_swap(gp[5], gp[5], false, false);
      sts1(v4u{s0[0], s1[0], s0[1], s1[1]}, rws, vhf, base + 1024);
    };
    auto egap = [&](auto Mc, auto Gc) { constexpr int G = Gc;
      if constexpr (G < 18 && G % 3 < 2) { constexpr int e = G / 3, z = G % 3; gu[e][z] = P(__builtin_amdgcn_exp2f(gu[e][z])); }
      if constexpr (G >= 2 && G < 20 && G % 3 == 2) { constexpr int e = G / 3; gu[e][0] = P(gu[e][0] + 1.0f); SB(); gu[e][1] = P(gu[e][1] + 1.0f); }
      if constexpr (G >= 18 && G < 36 && G % 3 < 2) { constexpr int e = (G - 18) / 3, z = G % 3; gu[e][z] = P(__builtin_amdgcn_rcpf(gu[e][z])); }
      if constexpr (G >= 20 && G < 38 && G % 3 == 2) { constexpr int e = (G - 20) / 3; gv[e][0] = P(gv[e][0] * gu[e][0]); SB(); gv[e][1] = P(gv[e][1] * gu[e][1]); }
      if constexpr (G >= 36 && G < 39 + 1 && G != 38) { constexpr int e = (G == 39 ? 2 : G - 36) * 2; gp[e] = P(pk2v(gv[e])); gp[e + 1] = P(pk2v(gv[e + 1])); }
      if constexpr (G == 40) gstore(Mc);
    };
    v4u a4[12];
    auto lda4 = [&](int q) { return ldf(GF_OFF + ((grp * 4 + q / 12) * 48 + w * 12) * 1024, q % 12); };
    const rsrc_t ro = mkrs(out, S * D * 2), rb2 = mkrs(b2p, D * 2);
    v4u br;
#pragma unroll
    for (int q = 0; q < RD; q++) af[q] = rda(q);
    SB();
    asm volatile("s_nop 1");
    auto rider = [&](auto Qc) { constexpr int q = Qc; if constexpr (q < NL2) glds1_sc1<q * 1024>(rws4, L2w, w2b + (60 - NL2 + q) * 1024, l * 16); };
    static_for<0, 80>([&](auto Tc) {
      constexpr int T = Tc, m = T / 20, k = T % 20;
      if constexpr (k == 14 && m < 3) {
        waitv<(m == 0 ? 14 : m == 1 ? 9 : 6)>();
        BAR();
      }
      if constexpr (k == 0) STAMP(26 + m);
      if constexpr (T == 66) flag_wait(fs, rws, few, 32, tag, l);
      const v4u cur = af[T % RD];
      static_for<0, 3>([&](auto Nc) {
        constexpr int n = Nc, G = k * 3 + n;
        mfma_w<k == 0, true>(W1F(G), cur, acc[n]);
        if constexpr (m > 0) egap(IC<m - 1>{}, IC<G>{});
        if constexpr (n == 2 && (T & 1) && T + RD < 80) { af[T % RD] = rda(T + RD); SB(); af[(T - 1) % RD] = rda(T + RD - 1); }
        if constexpr (G == 41) { const v2u u0 = *(LAS v2u*)(hr0 + ((m & 1) ? 0 : A3_0)), u1 = *(LAS v2u*)(hr1 + ((m & 1) ? 0 : A3_0)); hk[m] = v4u{u0.x, u0.y, u1.x, u1.y}; }
        if constexpr (m < 2 && n == 0 && k >= 14 && k < 19) ldh(IC<m + 2>{}, IC<k - 14>{});
        if constexpr (m == 0 && n == 1 && k < 14) rider(IC<k>{});
        if constexpr (m > 0 && m < 3 && n == 1 && (k & 1) == 0 && k < 14) rider(IC<14 + (m - 1) * 7 + k / 2>{});
        if constexpr (G >= 46 && G < 58 && G % 3 == 1) stats(m, IC<(G - 43) / 3>{});

        if constexpr (n == 0 && T == 60) br = ldp(rb2, act ? j * 160 + c * 16 : OOB, 0);
        if constexpr (n == 1 && T >= 62 && T < 66) ldw2(IC<32 + T - 62>{});
        if constexpr (n == 0 && T == 61) fs = flag_ld(rws, few, 32, l);
        if constexpr (n == 0 && T >= 66 && T < 78) a4[T - 66] = lda4(T - 66);
        SB();
      });
      if constexpr (k == 19) { epi1(); if constexpr (m >= 1) { waitv<(m == 1 ? 5 : m == 2 ? 0 : 4)>(); flag_st(rws, feo + (m - 1) * 32, tag, true, l); } }
    });
    fs = flag_ld(rws, few + 32, 32, l);
#pragma unroll
    for (int e = 0; e < 6; e++) gu[e] = P(f32x2{__builtin_amdgcn_exp2f(gu[e][0]), __builtin_amdgcn_exp2f(gu[e][1])});
    SB();
#pragma unroll
    for (int e = 0; e < 6; e++) gu[e] = P(gu[e] + f32x2{1.f, 1.f});
    SB();
#pragma unroll
    for (int e = 0; e < 6; e++) gu[e] = P(f32x2{__builtin_amdgcn_rcpf(gu[e][0]), __builtin_amdgcn_rcpf(gu[e][1])});
    SB();
#pragma unroll
    for (int e = 0; e < 6; e++) gu[e] = P(gv[e] * gu[e]);
    SB();
#pragma unroll
    for (int e = 0; e < 6; e++) gp[e] = pk2v(gu[e]);
    gstore(3);
    SB();
    STAMP(18);
    f32x4 ac4[2][5], r[8];
    float b2f[8], ov[8], ow[2];
#pragma unroll
    for (int e = 0; e < 8; e++) b2f[e] = (e & 1) ? bfhi(br[e >> 1]) : bflo(br[e >> 1]);
    auto fop = [&](auto Mc, auto Pc, auto Sc) { constexpr int m = Mc, st = Sc;
      static_for<0, 2>([&](auto Zc) { constexpr int z = Zc, e = 2 * Pc + z;
        if constexpr (st == 0) ov[e] = P(r[e >> 2][e & 3] + r[2 + (e >> 2)][e & 3]);
        if constexpr (st == 1) ow[z] = P(r[4 + (e >> 2)][e & 3] + r[6 + (e >> 2)][e & 3]);
        if constexpr (st == 2) ov[e] = P(ov[e] + ow[z]);
        if constexpr (st == 3) ow[z] = P(z ? bfhi(hk[m][e >> 1]) : bflo(hk[m][e >> 1]));
        if constexpr (st == 4) ow[z] = P(ow[z] + b2f[e]);
        if constexpr (st == 5) ov[e] = P(ov[e] + ow[z]);
        SB(); });
    };
    auto ost = [&](int m) { sts1(v4u{pk2(ov[0], ov[1]), pk2(ov[2], ov[3]), pk2(ov[4], ov[5]), pk2(ov[6], ov[7])}, ro, rowo, m * 20480); };
    SB();
    static_for<0, 48>([&](auto Tc) {
      constexpr int T = Tc, m = T / 12, k = T % 12;
      if constexpr (k == 0) STAMP(34 + 2 * m);
      if constexpr (k == 0 && m < 3) flag_wait(fs, rws, few + (m + 1) * 32, 32, tag, l);
      if constexpr (k == 4) BAR();
      const v4u cur = a4[k];
      static_for<0, 5>([&](auto Nc) { constexpr int n = Nc, G = k * 5 + n;
        mfma_w<k == 0, (G >= 32 || G < 3), true>(bw2[G], cur, ac4[m & 1][n]);
        if constexpr (m == 0 && G < NL2) bw2[60 - NL2 + G] = *(LAS v4u*)(L + W2L_OFF + w * (NL2 * 1024) + G * 1024 + l * 16);
        if constexpr (m > 0 && G >= 3 && G < 18 && G % 3 == 0) *(LAS f32x4*)(rdst + ((m - 1) & 1) * RED_BUF + (G / 3 - 1) * 64) = ac4[(m - 1) & 1][G / 3 - 1];
        if constexpr (m > 0 && G >= 20 && G < 28) { constexpr int q = ((G - 20) & 3) * 2 + ((G - 20) >> 2); r[q] = ((LAS f32x4*)(rsrc + ((m - 1) & 1) * RED_BUF + (q >> 1) * RED_W))[q & 1]; }
        if constexpr (m > 0 && G >= 32 && G < 56) fop(IC<m - 1>{}, IC<(G - 32) / 6>{}, IC<(G - 32) % 6>{});
        if constexpr (m > 0 && G == 57) ost(m - 1);
        if constexpr (m < 3 && n == 4) a4[k] = lda4(m * 12 + k + 12);
        if constexpr (n == 2 && T == 6) { waitv<6>(); flag_st(rws, feo + 96, tag, true, l); }
        if constexpr (n == 2 && m < 2 && k == 8) fs = flag_ld(rws, few + (m + 2) * 32, 32, l);
        SB();
      });
    });
    STAMP(21);
    asm volatile("s_nop 7");
    SB();
#pragma unroll
    for (int n = 0; n < 5; n++) *(LAS f32x4*)(rdst + RED_BUF + n * 64) = ac4[1][n];
    BAR(); red_ld(3, r);
#pragma unroll
    for (int e = 0; e < 8; e++) ov[e] = ((r[e >> 2][e & 3] + r[2 + (e >> 2)][e & 3]) + (r[4 + (e >> 2)][e & 3] + r[6 + (e >> 2)][e & 3])) + (((e & 1) ? bfhi(hk[3][e >> 1]) : bflo(hk[3][e >> 1])) + b2f[e]);
    ost(3);
    flag_st(rws, 0, tag, blockIdx.x == 0 && w == 0, l);
  }
  asm volatile(".Lk_end:");
}

extern "C" void kernel_launch(void* const* d_in, const int* in_sizes, int n_in, void* d_out, int out_size, void* d_ws, size_t ws_size, hipStream_t stream) {
  (void)hipFuncSetAttribute((const void*)k_fused, hipFuncAttributeMaxDynamicSharedMemorySize, 160 * 1024);
  k_fused<<<256, 256, 160 * 1024, stream>>>(d_in[0], d_in[1], d_in[2], d_in[4], d_in[5], d_in[6], d_in[7], d_out, (char*)d_ws);
}
```

```cpp
#include <hip/hip_runtime.h>
#include <hip/hip_bf16.h>
#include <stdint.h>
#include <type_traits>

#define LAS __attribute__((address_space(3)))
#define DI __device__ __forceinline__
typedef __attribute__((ext_vector_type(2))) __bf16 bf16x2;
typedef __attribute__((ext_vector_type(4))) float f32x4;
typedef __attribute__((ext_vector_type(2))) float f32x2;
typedef __attribute__((ext_vector_type(4))) unsigned v4u;
typedef __attribute__((ext_vector_type(2))) unsigned v2u;
typedef __attribute__((ext_vector_type(4))) short s16x4;
typedef __amdgpu_buffer_rsrc_t rsrc_t;

constexpr int S = 2048, D = 640, F = 1536;
constexpr int ACT = S * D * 2;
constexpr int EPOCH_OFF = 0;
constexpr int F0 = 1;
constexpr int FP = F0 + 256;
constexpr int FC = FP + 256;
constexpr int FD = FC + 1024;
constexpr int FE = FD + 1024;
constexpr int NFLAG = FE + 4096;
constexpr int WQP_OFF = (NFLAG * 256 + 65535) / 65536 * 65536, OF_OFF = WQP_OFF + 32 * 80 * 1024, HF_OFF = OF_OFF + ACT;
constexpr int GF_OFF = HF_OFF + ACT, WOP_OFF = GF_OFF + S * F * 2, W1P_OFF = WOP_OFF + D * D * 2, W2P_OFF = W1P_OFF + D * F * 2;
constexpr int KVP_OFF = W2P_OFF + D * F * 2, KVF_OFF = KVP_OFF + 32 * 32 * 2048, WS_END = KVF_OFF + 32 * 2048;
constexpr int OOB = 0x7ffffff0;
constexpr int SC1 = 16;
constexpr unsigned ONES2 = 0x3f803f80u;

DI rsrc_t mkrs(const void* p, int bytes) { return __builtin_amdgcn_make_buffer_rsrc((void*)p, 0, bytes, 0x00020000); }
DI v4u mkrs4(const void* p, int bytes) { const uint64_t a = (uint64_t)p; return v4u{(unsigned)a, (unsigned)(a >> 32) & 0xffffu, (unsigned)bytes, 0x00020000u}; }
DI void mfma_t(v4u a, v4u b, f32x4& c) { asm volatile("v_mfma_f32_16x16x32_bf16 %0, %1, %2, %0" : "+v"(c) : "v"(a), "v"(b)); }
DI void mfma_z(v4u a, v4u b, f32x4& c) { asm volatile("v_mfma_f32_16x16x32_bf16 %0, %1, %2, 0" : "=&v"(c) : "v"(a), "v"(b)); }
template <bool FIRST, bool AG, bool BG = false> DI void mfma_w(v4u a, v4u b, f32x4& c) {
#define MF_(CA, CB) do { if constexpr (FIRST) asm volatile("v_mfma_f32_16x16x32_bf16 %0, %1, %2, 0" : "=&v"(c) : CA(a), CB(b)); \
                         else asm volatile("v_mfma_f32_16x16x32_bf16 %0, %1, %2, %0" : "+v"(c) : CA(a), CB(b)); } while (0)
  if constexpr (AG && BG) MF_("a", "a"); else if constexpr (AG) MF_("a", "v"); else if constexpr (BG) MF_("v", "a"); else MF_("v", "v");
#undef MF_
}
template <bool FIRST> DI void mfma_a(v4u a, v4u b, f32x4& c) { if constexpr (FIRST) mfma_z(a, b, c); else mfma_t(a, b, c); }
DI unsigned pk2(float a, float b) { f32x2 f = {a, b}; return __builtin_bit_cast(unsigned, __builtin_convertvector(f, bf16x2)); }
DI float bflo(unsigned u) { return __uint_as_float(u << 16); }
DI float bfhi(unsigned u) { return __uint_as_float(u & 0xffff0000u); }
DI v4u ldp(rsrc_t r, int voff, int soff) { return __builtin_amdgcn_raw_buffer_load_b128(r, voff, soff, 0); }
DI v4u lds1(rsrc_t r, int voff, int soff) { return __builtin_amdgcn_raw_buffer_load_b128(r, voff, soff, SC1); }
DI void sts1(v4u v, rsrc_t r, int voff, int soff) { asm volatile("" : "+v"(v)); __builtin_amdgcn_raw_buffer_store_b128(v, r, voff, soff, SC1); asm volatile("s_nop 1" :: "v"(v) : "memory"); }
DI v2u trread(LAS char* p) { return __builtin_bit_cast(v2u, __builtin_amdgcn_ds_read_tr16_b64_v4i16((LAS s16x4*)p)); }
#define BAR() do { asm volatile("s_waitcnt lgkmcnt(0)" ::: "memory"); __builtin_amdgcn_s_barrier(); asm volatile("" ::: "memory"); } while (0)
#define SB() __builtin_amdgcn_sched_barrier(0)
constexpr float LOG2E = 1.4426950408889634f;
template <class T> DI void pin(T& x) { asm volatile("" : "+v"(x)); }
template <class T> DI T P(T x) { pin(x); return x; }
template <class T> DI T PS(T x) { asm volatile("" : "+s"(x)); return x; }
DI f32x2 fma2(f32x2 a, f32x2 b, f32x2 c) { return __builtin_elementwise_fma(a, b, c); }
DI f32x2 lo2(f32x4 v) { return f32x2{v[0], v[1]}; }
DI f32x2 hi2(f32x4 v) { return f32x2{v[2], v[3]}; }
DI unsigned pk2v(f32x2 f) { return __builtin_bit_cast(unsigned, __builtin_convertvector(f, bf16x2)); }
template <int N> DI void phi_n(const f32x2 (&u)[N], unsigned (&o)[N]) {
  f32x2 a[N];
#pragma unroll
  for (int e = 0; e < N; e++) a[e] = P(fma2(u[e], f32x2{LOG2E, LOG2E}, f32x2{-LOG2E, -LOG2E}));
  SB();
#pragma unroll
  for (int e = 0; e < N; e++) a[e] = P(f32x2{__builtin_amdgcn_fmed3f(__builtin_amdgcn_exp2f(a[e][0]), 0.f, 1.f), __builtin_amdgcn_fmed3f(__builtin_amdgcn_exp2f(a[e][1]), 0.f, 1.f)});
  SB();
#pragma unroll
  for (int e = 0; e < N; e++) a[e] = P(f32x2{__builtin_amdgcn_fmed3f(u[e][0], a[e][0], 3.0e38f), __builtin_amdgcn_fmed3f(u[e][1], a[e][1], 3.0e38f)});
  SB();
#pragma unroll
  for (int e = 0; e < N; e++) a[e] = P(a[e] + f32x2{1e-6f, 1e-6f});
  SB();
#pragma unroll
  for (int e = 0; e < N; e++) o[e] = pk2v(a[e]);
  SB();
}
DI float dot2(unsigned a, unsigned b, float c) { return __builtin_amdgcn_fdot2_f32_bf16(__builtin_bit_cast(bf16x2, a), __builtin_bit_cast(bf16x2, b), c, false); }
DI float add_x16(float v) { auto r = __builtin_amdgcn_permlane16_swap(__float_as_uint(v), __float_as_uint(v), false, false); return __uint_as_float(r[0]) + __uint_as_float(r[1]); }
DI float add_x32(float v) { auto r = __builtin_amdgcn_permlane32_swap(__float_as_uint(v), __float_as_uint(v), false, false); return __uint_as_float(r[0]) + __uint_as_float(r[1]); }

template <int IMM> DI void glds1(v4u rs, unsigned base, int soff, int v) {
  asm volatile("s_add_u32 m0, %1, %4\n\ts_nop 0\n\tbuffer_load_dwordx4 %3, %0, %2 offen lds" :: "s"(rs), "s"(base), "s"(soff), "v"(v), "i"(IMM) : "memory", "scc");
}
template <int IMM> DI void glds1_sc1(v4u rs, unsigned base, int soff, int v) {
  asm volatile("s_add_u32 m0, %1, %4\n\ts_nop 0\n\tbuffer_load_dwordx4 %3, %0, %2 offen sc1 lds" :: "s"(rs), "s"(base), "s"(soff), "v"(v), "i"(IMM) : "memory", "scc");
}
template <int IMM> DI void set_m0(unsigned base) { asm volatile("s_add_u32 m0, %0, %1" :: "s"(base), "i"(IMM) : "memory", "scc"); }
template <int OFF> DI void glds_o(v4u rs, int soff, int v) { asm volatile("buffer_load_dwordx4 %2, %0, %1 offen offset:%3 sc1 lds" :: "s"(rs), "s"(soff), "v"(v), "i"(OFF) : "memory"); }
template <int N> using IC = std::integral_constant<int, N>;
template <int N> DI void waitv() { asm volatile("s_waitcnt vmcnt(%0)" :: "i"(N) : "memory"); }
template <int I, int N, class Fn> DI void static_for(Fn&& f) { if constexpr (I < N) { f(std::integral_constant<int, I>{}); static_for<I + 1, N>(f); } }

DI unsigned flag_ld(rsrc_t rws, int f, int n, int l) { asm volatile("" ::: "memory"); return __builtin_amdgcn_raw_buffer_load_b32(rws, (l < n ? l : 0) * 256, f * 256, SC1); }
DI void flag_wait(unsigned v, rsrc_t rws, int f, int n, unsigned tag, int l) {
  if (!__all(v == tag)) {
#pragma unroll 1
    for (int sp = 0; sp < (1 << 16); sp++) { if (__all(flag_ld(rws, f, n, l) == tag)) break; }
  }
  asm volatile("" ::: "memory");
}
DI void wait_flags(rsrc_t rws, int f, int n, unsigned tag, int l) { flag_wait(flag_ld(rws, f, n, l), rws, f, n, tag, l); }
DI void flag_st(rsrc_t rws, int f, unsigned tag, bool me, int l) {
  asm volatile("" ::: "memory"); sts1(v4u{tag, tag, tag, tag}, rws, (me && l < 8) ? l * 16 : OOB, f * 256); asm volatile("" ::: "memory");
}

template <int MAT> DI int frag_off(int T, int ks) {
  if constexpr (MAT == 0) return WOP_OFF + ((T / 5) * 100 + ks * 5 + T % 5) * 1024;
  else if constexpr (MAT == 1) return W1P_OFF + ((T / 3) * 60 + ks * 3 + T % 3) * 1024;
  else return W2P_OFF + ((T / 5) * 240 + ks * 5 + T % 5) * 1024;
}
template <int MAT> DI void pack_issue(int ks, int cb, int l, v4u rs, unsigned stg) {
  const int r = l >> 3;
  static_for<0, 4>([&](auto Ic) { constexpr int i = Ic;
    const int sw = ((l & 7) ^ ((((r >> 1) & 1) | ((i & 1) << 1)) << 1)) * 16, sg = 4 * ks + i;
    if constexpr (MAT == 0) glds1<i * 1024>(rs, stg, sg * 8 * 1280 + cb * 128, r * 1280 + sw);
    else if constexpr (MAT == 1) { const int q = ks % 10;
      glds1<i * 1024>(rs, stg, ((2 * i + ks / 10) * 80 + (q < 8 ? 32 * (q >> 2) + 4 * (q & 3) : 8 * q)) * 3072 + cb * 128, (q < 8 ? 16 * (r >> 2) + (r & 3) : r) * 3072 + sw); }
    else { const int q = sg % 6; glds1<i * 1024>(rs, stg, (48 * (sg / 6) + (q < 4 ? 4 * q : 8 * q)) * 1280 + cb * 128, (q < 4 ? 16 * (r >> 2) + (r & 3) : r) * 1280 + sw); }
  });
}
DI int unit_lane(int t, int l) { const int g = l >> 4, q = (l >> 2) & 3, p = l & 3, f = ((q >> 1) & 1) | ((g & 1) << 1); return (8 * g + q) * 128 + ((t ^ f) * 32) + p * 8; }
DI v4u unit_read(LAS char* p) { const v2u a = trread(p), b = trread(p + 512); return v4u{a.x, a.y, b.x, b.y}; }
template <int MAT> DI void pack_dst(int ks, int cb, int (&d)[4]) {
#pragma unroll
  for (int t = 0; t < 4; t++) d[t] = frag_off<MAT>(cb * 4 + t, ks);
}
DI void pack_finish(const int (&d)[4], const int (&ul)[4], int l, rsrc_t rws, LAS char* stg) {
  v4u f[4];
#pragma unroll
  for (int t = 0; t < 4; t++) f[t] = unit_read(stg + ul[t]);
#pragma unroll
  for (int t = 0; t < 4; t++) sts1(f[t], rws, l * 16, d[t]);
}
constexpr int TAB_OFF = 81920, SCR_OFF = TAB_OFF + 512, SNK_OFF = SCR_OFF + 2048, XT_OFF = 92160, XT_W = 17408, OB_ROW = 176, WO_L = 61440;
constexpr int RED_ROW = 336, RED_W = 16 * RED_ROW, RED_BUF = 4 * RED_W, A3_0 = 2 * RED_BUF, W2L_OFF = 65536;
constexpr int NL2 = 24;
static_assert(SNK_OFF + 1024 <= WO_L + 28 * 1024 && A3_0 + 20480 <= W2L_OFF, "LDS map");

constexpr int B0 = 15, N1 = B0 + 18, N2 = 42, N3 = 50, N5 = 66, X2 = 8, RD1 = 6, PQ = 50, PFK = 8, RK = 12;

__global__ __launch_bounds__(256, 1) void k_fused(const void* x, const void* wqkv, const void* wo, const void* w1, const void* b1p, const void* w2,
                                                  const void* b2p, void* out, char* ws) {
  extern __shared__ __attribute__((aligned(16))) char smem[];
  LAS char* L = (LAS char*)smem;
  const unsigned Lb = (unsigned)(size_t)L;
  const int tid = threadIdx.x, l = tid & 63, w = __builtin_amdgcn_readfirstlane(tid >> 6), c = l & 15, g = l >> 4;
  const int xg = blockIdx.x & 7, i = blockIdx.x >> 3;
  const unsigned Lx = Lb + XT_OFF + w * XT_W;
  LAS char* xt = L + XT_OFF + w * XT_W;
  {
    uint64_t pc; unsigned len;
    asm volatile("s_getpc_b64 %0\n.Lk_here:\n\ts_mov_b32 %1, .Lk_end-.Lk_here\n\ts_nop 2" : "=s"(pc), "=s"(len));
    glds1<SNK_OFF>(mkrs4((const void*)pc, len & ~15u), Lb, (i * 4 + w) * 640, l < 40 ? l * 16 : OOB);
  }
  const int grp = xg * 4 + (i >> 3), j = i & 7;
  const bool p0 = j < 5;
  const int pk = (i >> 3) * 5 + j;
  const v4u rx4 = mkrs4(x, S * D * 2), rws4 = mkrs4(ws, WS_END);
  const rsrc_t rws = mkrs(ws, WS_END);
  const int uc = 88 + (grp * 5 + j) * 3 + w - 1;
  const bool hasc = p0 && w > 0 && uc < 480;
  const int fi = grp * 3 + j - 5;
  if (p0) {
    const v4u rw4 = mkrs4(wqkv, D * 1920 * 2);
    const int rr = l / 30, e = l - rr * 30, sec = e / 10;
    const int v0 = l < 60 ? rr * 3840 + sec * 1280 + (e - sec * 10) * 16 : OOB, s0 = (32 * pk + 8 * w) * 3840 + xg * 160;
    static_for<0, 4>([&](auto Uc) { constexpr int u = Uc; glds1<u * 1024>(rw4, Lx, s0 + u * 2 * 3840, v0); });
  }
  glds1_sc1<16384>(rws4, Lx, 0, l == 0 ? EPOCH_OFF : (l == 1 ? (p0 ? F0 + xg * 32 + pk : FP + fi) * 256 : OOB));
  int va[2];
#pragma unroll
  for (int u = 0; u < 2; u++) va[u] = (grp * 64 + w * 16 + u * 8 + (l >> 3)) * 1280 + (((l & 7) ^ (g | (u << 2))) * 16);
  auto ldx = [&](auto Qc) { constexpr int q = Qc; int ln = (q >> 1) + (p0 ? 0 : (j - 5) * 3); ln = ln >= 10 ? ln - 10 : ln;
    glds1<(q & 1) * 1024>(rx4, Lb + w * 2048 + ln * 8192, ln * 128, va[q & 1]); };
  int ul[4], dc[4];
#pragma unroll
  for (int t = 0; t < 4; t++) ul[t] = unit_lane(t, l);
  unsigned tag = 0;
  auto get_tag = [&]() { tag = __builtin_amdgcn_readfirstlane(*(LAS unsigned*)(xt + 16384)) + 1u; };
  if (p0) {
    {
    const char* tp = l < 8 ? (const char*)wo + l * 102400 : l < 24 ? (const char*)w1 + (l - 8) * 122880 : l < 40 ? (const char*)w2 + (l - 24) * 122880 : l < 56 ? (const char*)out + (l - 40) * 163840 : l < 60 ? (const char*)b1p : (const char*)b2p;
    asm volatile("s_mov_b32 m0, %1\n\ts_nop 0\n\tglobal_load_lds_dword %0, off" :: "v"(tp), "s"(Lb + SNK_OFF) : "memory");
    }
    const int q = (l >> 2) & 3, p = l & 3;
    const int tp0 = P((q >> 1) * 1024 + (q & 1) * 480 + (g == 0 ? 160 + 8 * p : g == 1 ? 320 + 8 * p : g == 2 ? (p == 0 ? 192 : p == 1 ? 352 : 32) : 8 * p));
    const int vd = P(g * 1024 + c * 16), sd = PS(WQP_OFF + (xg * 80 + pk) * 4096 + w * 256);
    pack_dst<2>(uc % 48, uc / 48, dc);
#pragma unroll
    for (int t = 0; t < 4; t++) dc[t] = PS(dc[t]);
    waitv<2>();
    v4u f[4];
#pragma unroll
    for (int t = 0; t < 4; t++) { const v2u a = trread(xt + tp0 + 40 * t), b = trread(xt + tp0 + 40 * t + 2048); f[t] = v4u{a.x, a.y, b.x, b.y}; }
#pragma unroll
    for (int t = 0; t < 4; t++) sts1(f[t], rws, vd, sd + t * 81920);
    static_for<0, X2>(ldx);
    waitv<X2>();
    get_tag();
    BAR(); flag_st(rws, F0 + xg * 32 + pk, tag, w == 0, l);
    static_for<X2, 20>(ldx);
    if (hasc) pack_issue<2>(uc % 48, uc / 48, l, mkrs4(w2, D * F * 2), Lx + 4096);
  } else {
    static_for<0, 8>(ldx);
#pragma unroll 1
    for (int r = 0; r < 2; r++) { const int u = fi * 8 + w * 2 + r;
      if (u < 200) pack_issue<0>(u % 20, u / 20, l, mkrs4(wo, D * D * 2), Lx + r * 4096);
      else if (u < 680) pack_issue<1>(u % 20, u / 20 - 10, l, mkrs4(w1, D * F * 2), Lx + r * 4096);
      else pack_issue<2>((u - 680) % 48, (u - 680) / 48, l, mkrs4(w2, D * F * 2), Lx + r * 4096); }
    static_for<8, 20>(ldx);
    waitv<12>();
    get_tag();
#pragma unroll 1
    for (int r = 0; r < 2; r++) { const int u = fi * 8 + w * 2 + r;
      int d[4];
      if (u < 200) pack_dst<0>(u % 20, u / 20, d); else if (u < 680) pack_dst<1>(u % 20, u / 20 - 10, d); else pack_dst<2>((u - 680) % 48, (u - 680) / 48, d);
      pack_finish(d, ul, l, rws, xt + r * 4096); }
  }
  const bool mid = c >= 4 && c < 12;
  const int c4 = c < 4 ? c : 4;
  const int vk1 = P(mid ? 1024 + (12 + g * 8 + c) * 16 : OOB), vg0 = P(g == 0 ? c * 16 : OOB), vk3 = P((g == 0 && mid) ? 1024 + (44 + c) * 16 : OOB);
  const int vsc = P(c >= 12 ? SCR_OFF + w * 512 + g * 64 + (c - 12) * 16 : SNK_OFF + l * 8);
  const int vpp = P(g * 2048 + ((xg == 7 && c >= 8) ? 7 : c) * 16), vf2 = P(1024 + (16 + g * 8 + c4) * 16), vf3 = P(1024 + (48 + c4) * 16);
  const int vtc = P(l < 28 ? (l < 16 ? FE + grp * 128 + (l >> 2) * 32 + j * 4 + (l & 3) : (l < 20 ? FC : FD) + grp * 32 + (l & 3) * 8 + j) * 256 + (l >= 24 ? 128 : 0) : OOB);
  const int vhf = P((g & 1) ? OOB : (g >> 1) * 256 + c * 16);
  LAS char* ap[2];
#pragma unroll
  for (int kk = 0; kk < 2; kk++) ap[kk] = L + c * 128 + (((4 * kk + g) ^ ((c >> 1) & 7)) * 16);
  auto rdx = [&](int ks, int m) { return *(LAS v4u*)(ap[ks & 1] + (ks >> 1) * 8192 + m * 2048); };
  auto ldf = [&](int base, int q) { return lds1(rws, l * 16 + (q & 3) * 1024, base + (q >> 2) * 4096); };
  const int hd = 4 * j + w;
  v4u bq[80];
  auto ldq = [&](auto Qc) { constexpr int q = Qc, f = q < 60 ? (q / 3) * 4 + 2 - q % 3 : (q - 60) * 4 + 3; bq[f] = ldf(WQP_OFF + hd * 81920, f); };
  if (w == 0) wait_flags(rws, F0 + j * 32, 20, tag, l);
  BAR();
  static_for<0, B0 - 10>(ldq); SB();
  if (hasc) waitv<B0 - 6>(); else if (p0) waitv<B0 - 10>(); else waitv<B0 - 2>();
  {
    v4u a[20];
#pragma unroll
    for (int ks = 0; ks < 20; ks++) a[ks] = *(LAS v4u*)(ap[ks & 1] + (ks >> 1) * 8192 + w * 2048);
    SB();
    float s[2] = {0.f, 0.f}, q[2] = {0.f, 0.f};
    static_for<0, 20>([&](auto Kc) { constexpr int ks = Kc;
      if constexpr (ks % 2 == 0) ldq(IC<B0 - 10 + ks / 2>{});
#pragma unroll
      for (int e = 0; e < 4; e++) { s[e & 1] = dot2(a[ks][e], ONES2, s[e & 1]); q[e & 1] = dot2(a[ks][e], a[ks][e], q[e & 1]); }
      SB(); });
    const float mean = add_x32(add_x16(s[0] + s[1])) * (1.f / D);
    const float rs = __builtin_amdgcn_rsqf(fmaxf(add_x32(add_x16(q[0] + q[1])) * (1.f / D) - mean * mean, 0.f) + 1e-5f);
    *(LAS float*)(L + TAB_OFF + (g & 1) * 256 + (w * 16 + c) * 4) = (g & 1) ? -mean * rs : rs;
  }
  if (!p0) waitv<B0>();
  BAR();
  if (!p0) flag_st(rws, FP + fi, tag, w == 0, l);
  v4u ones = {ONES2, ONES2, ONES2, ONES2};
  pin(ones);
  v4u fa[2][4];
  unsigned fp = 0;
  const int fpa = FP + w * 24, fpn = 24;
  v4u bw1[60], bw2[60];
  const int w1b = W1P_OFF + hd * 61440, w2b = W2P_OFF + hd * 61440;
  auto ldw2 = [&](auto Qc) { constexpr int q = Qc; if constexpr (q >= 0 && q < 60) bw2[q] = ldf(w2b, q); };
  auto bulk = [&](auto Qc) { constexpr int q = Qc; if constexpr (q < 60) bw1[q] = ldf(w1b, q); else ldw2(IC<q - 60>{}); };
  auto run = [&](auto Nc, unsigned lds, int mem) { static_for<0, Nc>([&](auto Qc) { constexpr int q = Qc;
    if constexpr (q % 4 == 0) { set_m0<q * 1024>(lds); asm volatile("s_nop 0"); }
    glds_o<(q % 4) * 1024>(rws4, mem + (q / 4) * 4096, l * 16); }); };
#define W1F(r) bw1[r]
  {
    f32x4 acc[4][3], cs[3], trs[4], tnm[4];
    LAS char* tabp = L + TAB_OFF + g * 16;
#pragma unroll
    for (int m = 0; m < 4; m++) fa[0][m] = rdx(0, m);
    SB();
    static_for<0, 20>([&](auto Kc) { constexpr int ks = Kc;
      if constexpr (ks == PFK) { if (hasc) pack_finish(dc, ul, l, rws, xt + 4096); SB(); }
      if constexpr (ks == 19) { flag_wait(fp, rws, fpa, fpn, tag, l); BAR(); flag_st(rws, FP + 96 + grp * 5 + j, tag, p0 && w == 0, l); }
      static_for<0, 15>([&](auto Qc) { constexpr int Q = Qc, n = Q / 5, m = Q % 5;
        if constexpr (m < 4) mfma_w<ks == 0, false, true>(fa[ks & 1][m], bq[ks * 4 + n], acc[m][n]); else mfma_w<ks == 0, false, true>(ones, bq[ks * 4 + n], cs[n]);
        if constexpr (Q >= 1 && Q < 9 && (Q & 1)) fa[(ks + 1) & 1][3 - Q / 2] = rdx(ks < 19 ? ks + 1 : 0, 3 - Q / 2);
        if constexpr (m == 2 && B0 + ks * 3 + n < 80) ldq(IC<B0 + ks * 3 + n>{});
        if constexpr (ks == 15 && Q == 0) fp = flag_ld(rws, fpa, fpn, l);
        if constexpr (ks >= 18 && Q >= 9 && Q < 13) (ks == 18 ? trs : tnm)[Q - 9] = *(LAS f32x4*)(tabp + (ks - 18) * 256 + (Q - 9) * 64);
        SB(); });
    });
    static_assert(B0 >= 12 && B0 <= 20, "");
    asm volatile("s_nop 7");
#pragma unroll
    for (int n = 0; n < 3; n++) pin(cs[n]);
    v4u fk[2], fv[2], fr[2], fb[2];
    const f32x2 one2 = {1.f, 1.f}, mk = c < 8 ? one2 : f32x2{0.f, 0.f}, ck = c < 8 ? f32x2{0.f, 0.f} : one2;
    auto ride = [&](auto Ec) { if constexpr (B0 + 60 + Ec < 80) ldq(IC<B0 + 60 + Ec>{}); else bulk(IC<B0 + Ec - 20>{}); };
    static_for<0, 2>([&](auto Uc) { constexpr int u = Uc;
      f32x2 t[3][4], uu[8];
      unsigned o[8];
#pragma unroll
      for (int e = 0; e < 4; e++) { const f32x2 nm = (e & 1) ? hi2(tnm[2 * u + (e >> 1)]) : lo2(tnm[2 * u + (e >> 1)]);
        t[0][e] = P(fma2(nm, lo2(cs[0]), one2)); t[1][e] = P(nm * lo2(cs[1])); t[2][e] = P(nm * lo2(cs[2])); }
      ride(IC<4 * u>{});
      SB();
#pragma unroll
      for (int e = 0; e < 4; e++) { const int m = 2 * u + (e >> 1); const f32x2 rs = (e & 1) ? hi2(trs[m]) : lo2(trs[m]);
        uu[e] = fma2(rs, (e & 1) ? hi2(acc[m][0]) : lo2(acc[m][0]), t[0][e]); t[2][e] = P(fma2(rs, (e & 1) ? hi2(acc[m][2]) : lo2(acc[m][2]), t[2][e]));
        t[1][e] = fma2(rs, (e & 1) ? hi2(acc[m][1]) : lo2(acc[m][1]), t[1][e]); }
      ride(IC<4 * u + 1>{});
      SB();
#pragma unroll
      for (int e = 0; e < 4; e++) { uu[4 + e] = P(t[2][e] + one2); t[2][e] = fma2(t[2][e], mk, ck); fv[u][e] = pk2v(t[1][e]); }
      ride(IC<4 * u + 2>{});
      phi_n<8>(uu, o);
#pragma unroll
      for (int e = 0; e < 4; e++) { fb[u][e] = pk2v(t[2][e]); fk[u][e] = o[e]; fr[u][e] = o[4 + e]; }
      ride(IC<4 * u + 3>{});
      SB();
    });
#pragma unroll
    for (int u = 0; u < 2; u++) { pin(fk[u]); pin(fv[u]); pin(fr[u]); pin(fb[u]); }
    asm volatile("s_nop 1");
    f32x4 t00, t01, t10, t11;
    mfma_z(fk[0], fv[0], t00); mfma_z(fk[0], fb[0], t01); mfma_z(fr[0], fv[0], t10); mfma_z(fr[0], fb[0], t11);
    mfma_t(fk[1], fv[1], t00); mfma_t(fk[1], fb[1], t01); mfma_t(fr[1], fv[1], t10); mfma_t(fr[1], fb[1], t11);
#pragma unroll
    for (int u = 0; u < 2; u++) *(LAS v4u*)(L + (vsc & ~15) + u * 256) = fr[u];
    asm volatile("s_nop 5");
    SB();
    const int kvo = KVP_OFF + (hd * 32 + grp) * 2048;
    sts1(v4u{pk2(t00[0], t00[1]), tag, pk2(t00[2], t00[3]), tag}, rws, l * 16, kvo);
    sts1(v4u{pk2(t01[0], t01[1]), tag, pk2(t01[2], t01[3]), tag}, rws, vk1, kvo);
    sts1(v4u{pk2(t10[0], t10[1]), tag, pk2(t10[2], t10[3]), tag}, rws, vg0, kvo + 1024);
    sts1(v4u{pk2(t11[0], t11[1]), tag, pk2(t11[2], t11[3]), tag}, rws, vk3, kvo);
  }
  if (w == ((grp + 1) & 3)) glds1<SNK_OFF>(rws4, Lb, 0, vtc);
  const bool act = c < 10;
  v2u xr[5];
  {
    f32x4 acc[4], cs;
    float qrs[4], qnm[4];
    v2u q1[4];
    static_for<0, 21>([&](auto Kc) { constexpr int ks = Kc;
      if constexpr (ks == RK) {
        if (w == (grp & 3)) {
          v4u pp[8];
#pragma unroll 1
          for (int sp = 0; sp < (1 << 16); sp++) {
            asm volatile("" ::: "memory");
#pragma unroll
            for (int q = 0; q < 8; q++) pp[q] = lds1(rws, vpp, KVP_OFF + (hd * 32 + 4 * q) * 2048 + xg * 256);
            SB();
            unsigned bad = 0;
#pragma unroll
            for (int q = 0; q < 8; q++) bad |= (pp[q].y ^ tag) | (pp[q].w ^ tag);
            if (__all(bad == 0)) break;
          }
          float sm[4] = {0.f, 0.f, 0.f, 0.f};
#pragma unroll
          for (int q = 0; q < 8; q++) { sm[0] += bflo(pp[q].x); sm[1] += bfhi(pp[q].x); sm[2] += bflo(pp[q].z); sm[3] += bfhi(pp[q].z); }
#pragma unroll
          for (int e = 0; e < 4; e++) sm[e] = add_x32(add_x16(sm[e]));
          sts1(v4u{pk2(sm[0], sm[1]), tag, pk2(sm[2], sm[3]), tag}, rws, vg0, KVF_OFF + hd * 2048 + xg * 256);
        } else { const int f = (28 + ((w - grp - 1) & 3) * 24) * 1024; run(IC<24>{}, Lb + WO_L + f, WOP_OFF + j * 102400 + f); }
        SB();
      }
      if constexpr (ks < 20) static_for<0, 5>([&](auto Qc) { constexpr int m = Qc;
        if constexpr (m < 4) mfma_a<ks == 0>(bq[ks * 4 + 3], fa[ks & 1][m], acc[m]); else mfma_a<ks == 0>(bq[ks * 4 + 3], ones, cs);
        if constexpr (ks < 19 && m < 4) fa[(ks + 1) & 1][3 - m] = rdx(ks + 1, 3 - m);
        if constexpr (m == 4 || (m == 1 && (ks & 1))) bulk(IC<B0 - 12 + (ks / 2) * 3 + (ks & 1) + (m == 4 && (ks & 1))>{});
        SB(); });
    });
    {
      LAS char* qp = L + SCR_OFF + w * 512 + (l & 15) / 4 * 16 + (l & 3) * 64;
#pragma unroll
      for (int m = 0; m < 4; m++) {
        qrs[m] = *(LAS float*)(L + TAB_OFF + c * 4 + m * 64); qnm[m] = *(LAS float*)(L + TAB_OFF + c * 4 + 256 + m * 64);
        q1[m] = trread(qp + (m >> 1) * 256 + (m & 1) * 8);
      }
#pragma unroll
      for (int n = 0; n < 5; n++) { const int sg = 10 * j + 2 * n + (g >> 1);
        xr[n] = *(LAS v2u*)(L + (sg >> 3) * 8192 + (w * 16 + c) * 128 + (((sg & 7) ^ ((c >> 1) & 7)) * 16) + (g & 1) * 8); }
    }
    asm volatile("s_nop 7");
    SB();
    v4u ka0, ka1, kb0, kb1;
    auto kv_ld = [&]() { asm volatile("" ::: "memory");
      ka0 = lds1(rws, l * 16, KVF_OFF + hd * 2048); ka1 = lds1(rws, 1024 + c * 16, KVF_OFF + hd * 2048);
      kb0 = lds1(rws, vf2, KVF_OFF + hd * 2048); kb1 = lds1(rws, vf3, KVF_OFF + hd * 2048); };
    auto kv_ok = [&]() { return __all(((ka0.y ^ tag) | (ka0.w ^ tag) | (ka1.y ^ tag) | (ka1.w ^ tag) | (kb0.y ^ tag) | (kb0.w ^ tag) | (kb1.y ^ tag) | (kb1.w ^ tag)) == 0); };
    const unsigned fq = flag_ld(rws, FP + 96 + w * 33, 33, l);
    static_for<N1, N2>(bulk); SB();
    kv_ld();
    v4u pq[4];
    {
      f32x2 uu[8];
      unsigned o[8];
#pragma unroll
      for (int e = 0; e < 8; e++) uu[e] = P(fma2(f32x2{qnm[e >> 1], qnm[e >> 1]}, (e & 1) ? hi2(cs) : lo2(cs), f32x2{1.f, 1.f}));
      SB();
#pragma unroll
      for (int e = 0; e < 8; e++) uu[e] = P(fma2(f32x2{qrs[e >> 1], qrs[e >> 1]}, (e & 1) ? hi2(acc[e >> 1]) : lo2(acc[e >> 1]), uu[e]));
      SB();
      phi_n<8>(uu, o);
#pragma unroll
      for (int m = 0; m < 4; m++) { pq[m] = v4u{o[2 * m], o[2 * m + 1], q1[m].x, q1[m].y}; pin(pq[m]); }
    }
    flag_wait(fq, rws, FP + 96 + w * 33, 33, tag, l);
    BAR();
    if (!kv_ok()) {
#pragma unroll 1
      for (int sp = 0; sp < (1 << 16); sp++) { kv_ld(); if (kv_ok()) break; }
    }
    v4u kf0 = {ka0.x, ka0.z, g == 3 ? ka1.x : 0u, g == 3 ? ka1.z : 0u}, kf1 = {kb0.x, kb0.z, g == 3 ? kb1.x : 0u, g == 3 ? kb1.z : 0u};
    pin(kf0); pin(kf1);
    asm volatile("s_nop 1");
    f32x4 n0[4], n1[4];
    LAS char* ob0 = L + c * OB_ROW + w * 40 + g * 8;
    LAS char* ob1 = L + c * OB_ROW + (g == 0 ? w * 40 + 32 : 160);
#pragma unroll
    for (int m = 0; m < 4; m++) { mfma_z(kf0, pq[m], n0[m]); mfma_z(kf1, pq[m], n1[m]); }
    asm volatile("s_nop 7");
    SB();
    float rr[4];
#pragma unroll
    for (int m = 0; m < 4; m++) rr[m] = __uint_as_float(__builtin_amdgcn_permlane16_swap(__float_as_uint(n1[m][0]), __float_as_uint(n1[m][0]), false, false)[1]);
    SB();
#pragma unroll
    for (int m = 0; m < 4; m++) rr[m] = P(rr[m] + 1e-6f);
    SB();
#pragma unroll
    for (int m = 0; m < 4; m++) rr[m] = P(__builtin_amdgcn_rcpf(rr[m]));
    SB();
#pragma unroll
    for (int m = 0; m < 4; m++) { n0[m] = P(n0[m] * rr[m]); n1[m] = P(n1[m] * rr[m]); }
    SB();
#pragma unroll
    for (int m = 0; m < 4; m++) {
      *(LAS v2u*)(ob0 + m * 16 * OB_ROW) = v2u{pk2v(lo2(n0[m])), pk2v(hi2(n0[m]))};
      *(LAS v2u*)(ob1 + m * 16 * OB_ROW) = v2u{pk2v(lo2(n1[m])), pk2v(hi2(n1[m]))};
    }
    BAR();
    v4u o[3];
#pragma unroll
    for (int q = 0; q < 3; q++) o[q] = *(LAS v4u*)(L + (w * 16 + c) * OB_ROW + (q * 4 + (q == 2 ? g & 1 : g)) * 16);
#pragma unroll
    for (int q = 0; q < 3; q++) sts1(o[q], rws, (q < 2 || g < 2) ? l * 16 + q * 1024 : OOB, OF_OFF + (grp * 4 + w) * 20480 + j * 2560);
    SB();
    static_for<N2, N3>(bulk); SB();
    waitv<N3 - N2>();
    flag_st(rws, FC + grp * 32 + w * 8 + j, tag, true, l);
    run(IC<7>{}, Lb + WO_L + w * 7168, WOP_OFF + j * 102400 + w * 7168);
  }
  LAS char* rdst = L + w * RED_W + c * RED_ROW + g * 16;
  LAS char* rsrc = L + (4 * w + g) * RED_ROW + (act ? c : 9) * 32;
  const int rowo = act ? (grp * 64 + 4 * w + g) * 1280 + j * 160 + c * 16 : OOB;
  auto red_ld = [&](int m, f32x4 (&r)[8]) {
#pragma unroll
    for (int ww = 0; ww < 4; ww++) { LAS f32x4* p = (LAS f32x4*)(rsrc + (m & 1) * RED_BUF + ww * RED_W); r[2 * ww] = p[0]; r[2 * ww + 1] = p[1]; }
  };
  v4u hs[4][2], hk[4];
  float hsa[4], hsb[4];
  const int hsv = g * 512 + c * 16;
  auto h_poll = [&](int m) { asm volatile("" ::: "memory");
    hs[m][0] = lds1(rws, hsv, (FD + grp * 32 + m * 8) * 256); hs[m][1] = lds1(rws, hsv + 256, (FD + grp * 32 + m * 8) * 256); };
  auto h_ok = [&](int m) { return __all(((hs[m][0].y ^ tag) | (hs[m][0].w ^ tag) | (hs[m][1].y ^ tag) | (hs[m][1].w ^ tag)) == 0); };
  auto h_wait = [&](int m) {
    if (!h_ok(m)) {
#pragma unroll 1
      for (int sp = 0; sp < (1 << 16); sp++) { h_poll(m); if (h_ok(m)) break; }
    }
    asm volatile("" ::: "memory");
    hsa[m] = P(__uint_as_float(hs[m][0].x) + __uint_as_float(hs[m][1].x)); hsb[m] = P(__uint_as_float(hs[m][0].z) + __uint_as_float(hs[m][1].z));
  };
  v2u bias[3];
  const unsigned La = Lb + w * 5120;
  const int hso = HF_OFF + grp * 81920 + w * 5120;
  auto ldh = [&](auto Mc, auto Qc) { constexpr int m = Mc, q = Qc; glds1_sc1<((m & 1) ? 0 : A3_0) + q * 1024>(rws4, La, hso + m * 20480 + q * 1024, l * 16); };
  f32x4 cs[3];
  {
    const rsrc_t rb1 = mkrs(b1p, F * 2);
#pragma unroll
    for (int n = 0; n < 3; n++) bias[n] = __builtin_amdgcn_raw_buffer_load_b64(rb1, ((12 * j + 3 * w + n) * 16 + 4 * g) * 2, 0, 0);
    static_for<N3, N5>(bulk); SB();
    wait_flags(rws, FC + grp * 32 + w * 8, 8, tag, l);
    v4u a[20], bo[100];
    auto lda = [&](auto Qc) { constexpr int q = Qc; a[q] = ldf(OF_OFF + (grp * 4 + w) * 20480, q); };
    static_for<0, 12>(lda);
    BAR();
    LAS char* wp[2] = {L + WO_L + l * 16, L + WO_L + 51200 + l * 16};
    auto rdo = [&](int f) { return *(LAS v4u*)(wp[f / 50] + (f % 50) * 1024); };
#pragma unroll
    for (int f = 0; f < 10; f++) bo[f] = rdo(f);
    SB();
    f32x4 acc[5];
    static_for<0, 100>([&](auto Fc) { constexpr int f = Fc, k = f / 5, n = f % 5;
      mfma_a<k == 0>(bo[f], a[k], acc[n]);
      if constexpr (k < 18) bo[(k + 2) * 5 + 4 - n] = rdo((k + 2) * 5 + 4 - n);
      if constexpr (n == 2 && k + 12 < 20) lda(IC<k + 12>{});
      SB(); });
    asm volatile("s_nop 7");
    SB();
    unsigned hp[10];
    float s[2] = {0.f, 0.f}, q[2] = {0.f, 0.f};
#pragma unroll
    for (int n = 0; n < 5; n++) {
      hp[2 * n] = pk2v(lo2(acc[n]) + f32x2{bflo(xr[n].x), bfhi(xr[n].x)}); hp[2 * n + 1] = pk2v(hi2(acc[n]) + f32x2{bflo(xr[n].y), bfhi(xr[n].y)});
    }
    const int hb = HF_OFF + (grp * 4 + w) * 20480 + j * 2560;
    sts1(v4u{hp[0], hp[1], hp[2], hp[3]}, rws, l * 16, hb);
    sts1(v4u{hp[4], hp[5], hp[6], hp[7]}, rws, l * 16 + 1024, hb);
    auto s0 = __builtin_amdgcn_permlane16_swap(hp[8], hp[8], false, false);
    auto s1 = __builtin_amdgcn_permlane16_swap(hp[9], hp[9], false, false);
    sts1(v4u{s0[0], s1[0], s0[1], s1[1]}, rws, vhf, hb + 2048);
#pragma unroll
    for (int e = 0; e < 10; e++) { s[e & 1] = dot2(hp[e], ONES2, s[e & 1]); q[e & 1] = dot2(hp[e], hp[e], q[e & 1]); }
    const v4u hst = {__float_as_uint(add_x32(add_x16(s[0] + s[1]))), tag, __float_as_uint(add_x32(add_x16(q[0] + q[1]))), tag};
    static_for<N5, N5 + RD1>(bulk); SB();
    waitv<RD1>();
    sts1(hst, rws, vg0, (FD + grp * 32 + w * 8 + j) * 256);
    BAR();
    static_assert(N5 >= 60, "w1 has landed");
    asm volatile("s_nop 1");
    static_for<0, 60>([&](auto Qc) { constexpr int q = Qc; mfma_w<(q < 3), true>(W1F(q), ones, cs[q % 3]);
      if constexpr (q % 5 == 0 && q < PQ) bulk(IC<N5 + RD1 + q / 5>{});
      if constexpr (q == PQ) { h_poll(0); h_poll(1); }
      SB(); });
    h_wait(0); h_wait(1);
    run(IC<5>{}, La + A3_0, hso);
    h_poll(2); h_poll(3);
    static_for<N5 + RD1 + PQ / 5, 92>(bulk); SB();
    h_wait(2); h_wait(3);
    run(IC<5>{}, La, hso + 20480);
  }
  {
    const int few = FE + grp * 128, feo = few + j * 4 + w;
    unsigned fs = 0;
    f32x2 bf[6];
#pragma unroll
    for (int e = 0; e < 6; e++) bf[e] = P(f32x2{bflo(bias[e >> 1][e & 1]), bfhi(bias[e >> 1][e & 1])});
    SB();
    BAR();
    LAS char* ab = L + g * 5120 + c * 16;
    auto rda = [&](int t) { return *(LAS v4u*)(ab + (((t / 20) & 1) ? 0 : A3_0) + (t % 20) * 256); };
    const int cc = act ? c : 0;
    LAS char* hr0 = L + j * 2560 + (4 * w + g) * 16 + (cc < 8 ? ((cc >> 2) * 4 + (cc & 1) * 2) * 256 + ((cc >> 1) & 1) * 8 : cc * 256);
    LAS char* hr1 = hr0 + (cc < 8 ? 256 : 8);
    const unsigned L2w = Lb + W2L_OFF + w * (NL2 * 1024);
    constexpr int RD = 6;
    v4u af[RD];
    f32x4 acc[3];
    f32x2 bp[6], gv[6], gu[6];
    unsigned gp[6];
    float rs = 0.f, nr = 0.f, sa = 0.f, sb = 0.f;
    auto stats = [&](int m, auto Pc) { constexpr int p = Pc;
      if constexpr (p == 1) { auto x = __builtin_amdgcn_permlane16_swap(__float_as_uint(hsa[m]), __float_as_uint(hsb[m]), false, false); sa = P(__uint_as_float(x[0]) + __uint_as_float(x[1])); sb = P(sa); }
      if constexpr (p == 2) { auto x = __builtin_amdgcn_permlane32_swap(__float_as_uint(sa), __float_as_uint(sb), false, false); sa = P(__uint_as_float(x[0]) + __uint_as_float(x[1])); sb = P(sa); }
      if constexpr (p == 3) { auto x = __builtin_amdgcn_permlane16_swap(__float_as_uint(sa), __float_as_uint(sb), false, false);
        sa = P(__uint_as_float(x[0]) * (1.f / D)); sb = P(fmaxf(__uint_as_float(x[1]) * (1.f / D) - sa * sa, 0.f)); }
      if constexpr (p == 4) { const float r1 = __builtin_amdgcn_rsqf(sb + 1e-5f);
        rs = P(r1 * __builtin_amdgcn_rsqf(sb * r1 * r1 + 1e-5f));
        nr = P(-sa * rs); }
    };
    auto epi1 = [&]() {
#pragma unroll
      for (int e = 0; e < 6; e++) bp[e] = P(fma2(f32x2{nr, nr}, (e & 1) ? hi2(cs[e >> 1]) : lo2(cs[e >> 1]), bf[e]));
      asm volatile("s_nop 1");
      SB();
      constexpr float C0 = -0.7978845608028654f * 2.f * LOG2E, C1 = C0 * 0.044715f;
      const f32x2 rs2 = {rs, rs};
#pragma unroll
      for (int e = 0; e < 6; e++) gv[e] = P(fma2(rs2, (e & 1) ? hi2(acc[e >> 1]) : lo2(acc[e >> 1]), bp[e]));
      SB();
#pragma unroll
      for (int e = 0; e < 6; e++) gu[e] = P(gv[e] * gv[e]);
      SB();
#pragma unroll
      for (int e = 0; e < 6; e++) gu[e] = P(fma2(gu[e], f32x2{C1, C1}, f32x2{C0, C0}));
      SB();
#pragma unroll
      for (int e = 0; e < 6; e++) gu[e] = P(gu[e] * gv[e]);
      SB();
    };
    auto gstore = [&](int m) {
      const int base = GF_OFF + (grp * 4 + m) * 49152 + 6 * hd * 256;
      sts1(v4u{gp[0], gp[1], gp[2], gp[3]}, rws, l * 16, base);
      auto s0 = __builtin_amdgcn_permlane16_swap(gp[4], gp[4], false, false);
      auto s1 = __builtin_amdgcn_permlane16_swap(gp[5], gp[5], false, false);
      sts1(v4u{s0[0], s1[0], s0[1], s1[1]}, rws, vhf, base + 1024);
    };
    auto egap = [&](auto Mc, auto Gc) { constexpr int G = Gc;
      if constexpr (G < 18 && G % 3 < 2) { constexpr int e = G / 3, z = G % 3; gu[e][z] = P(__builtin_amdgcn_exp2f(gu[e][z])); }
      if constexpr (G >= 2 && G < 20 && G % 3 == 2) { constexpr int e = G / 3; gu[e][0] = P(gu[e][0] + 1.0f); SB(); gu[e][1] = P(gu[e][1] + 1.0f); }
      if constexpr (G >= 18 && G < 36 && G % 3 < 2) { constexpr int e = (G - 18) / 3, z = G % 3; gu[e][z] = P(__builtin_amdgcn_rcpf(gu[e][z])); }
      if constexpr (G >= 20 && G < 38 && G % 3 == 2) { constexpr int e = (G - 20) / 3; gv[e][0] = P(gv[e][0] * gu[e][0]); SB(); gv[e][1] = P(gv[e][1] * gu[e][1]); }
      if constexpr (G >= 36 && G < 39 + 1 && G != 38) { constexpr int e = (G == 39 ? 2 : G - 36) * 2; gp[e] = P(pk2v(gv[e])); gp[e + 1] = P(pk2v(gv[e + 1])); }
      if constexpr (G == 40) gstore(Mc);
    };
    v4u a4[12];
    auto lda4 = [&](int q) { return ldf(GF_OFF + ((grp * 4 + q / 12) * 48 + w * 12) * 1024, q % 12); };
    const rsrc_t ro = mkrs(out, S * D * 2), rb2 = mkrs(b2p, D * 2);
    v4u br;
#pragma unroll
    for (int q = 0; q < RD; q++) af[q] = rda(q);
    SB();
    asm volatile("s_nop 1");
    auto rider = [&](auto Qc) { constexpr int q = Qc; if constexpr (q < NL2) glds1_sc1<q * 1024>(rws4, L2w, w2b + (60 - NL2 + q) * 1024, l * 16); };
    static_for<0, 80>([&](auto Tc) {
      constexpr int T = Tc, m = T / 20, k = T % 20;
      if constexpr (k == 14 && m < 3) {
        waitv<(m == 0 ? 14 : m == 1 ? 9 : 6)>();
        BAR();
      }
      if constexpr (T == 66) flag_wait(fs, rws, few, 32, tag, l);
      const v4u cur = af[T % RD];
      static_for<0, 3>([&](auto Nc) {
        constexpr int n = Nc, G = k * 3 + n;
        mfma_w<k == 0, true>(W1F(G), cur, acc[n]);
        if constexpr (m > 0) egap(IC<m - 1>{}, IC<G>{});
        if constexpr (n == 2 && (T & 1) && T + RD < 80) { af[T % RD] = rda(T + RD); SB(); af[(T - 1) % RD] = rda(T + RD - 1); }
        if constexpr (G == 41) { const v2u u0 = *(LAS v2u*)(hr0 + ((m & 1) ? 0 : A3_0)), u1 = *(LAS v2u*)(hr1 + ((m & 1) ? 0 : A3_0)); hk[m] = v4u{u0.x, u0.y, u1.x, u1.y}; }
        if constexpr (m < 2 && n == 0 && k >= 14 && k < 19) ldh(IC<m + 2>{}, IC<k - 14>{});
        if constexpr (m == 0 && n == 1 && k < 14) rider(IC<k>{});
        if constexpr (m > 0 && m < 3 && n == 1 && (k & 1) == 0 && k < 14) rider(IC<14 + (m - 1) * 7 + k / 2>{});
        if constexpr (G >= 46 && G < 58 && G % 3 == 1) stats(m, IC<(G - 43) / 3>{});

        if constexpr (n == 0 && T == 60) br = ldp(rb2, act ? j * 160 + c * 16 : OOB, 0);
        if constexpr (n == 1 && T >= 62 && T < 66) ldw2(IC<32 + T - 62>{});
        if constexpr (n == 0 && T == 61) fs = flag_ld(rws, few, 32, l);
        if constexpr (n == 0 && T >= 66 && T < 78) a4[T - 66] = lda4(T - 66);
        SB();
      });
      if constexpr (k == 19) { epi1(); if constexpr (m >= 1) { waitv<(m == 1 ? 5 : m == 2 ? 0 : 4)>(); flag_st(rws, feo + (m - 1) * 32, tag, true, l); } }
    });
    fs = flag_ld(rws, few + 32, 32, l);
#pragma unroll
    for (int e = 0; e < 6; e++) gu[e] = P(f32x2{__builtin_amdgcn_exp2f(gu[e][0]), __builtin_amdgcn_exp2f(gu[e][1])});
    SB();
#pragma unroll
    for (int e = 0; e < 6; e++) gu[e] = P(gu[e] + f32x2{1.f, 1.f});
    SB();
#pragma unroll
    for (int e = 0; e < 6; e++) gu[e] = P(f32x2{__builtin_amdgcn_rcpf(gu[e][0]), __builtin_amdgcn_rcpf(gu[e][1])});
    SB();
#pragma unroll
    for (int e = 0; e < 6; e++) gu[e] = P(gv[e] * gu[e]);
    SB();
#pragma unroll
    for (int e = 0; e < 6; e++) gp[e] = pk2v(gu[e]);
    gstore(3);
    SB();
    f32x4 ac4[2][5], r[8];
    float b2f[8], ov[8], ow[2];
#pragma unroll
    for (int e = 0; e < 8; e++) b2f[e] = (e & 1) ? bfhi(br[e >> 1]) : bflo(br[e >> 1]);
    auto fop = [&](auto Mc, auto Pc, auto Sc) { constexpr int m = Mc, st = Sc;
      static_for<0, 2>([&](auto Zc) { constexpr int z = Zc, e = 2 * Pc + z;
        if constexpr (st == 0) ov[e] = P(r[e >> 2][e & 3] + r[2 + (e >> 2)][e & 3]);
        if constexpr (st == 1) ow[z] = P(r[4 + (e >> 2)][e & 3] + r[6 + (e >> 2)][e & 3]);
        if constexpr (st == 2) ov[e] = P(ov[e] + ow[z]);
        if constexpr (st == 3) ow[z] = P(z ? bfhi(hk[m][e >> 1]) : bflo(hk[m][e >> 1]));
        if constexpr (st == 4) ow[z] = P(ow[z] + b2f[e]);
        if constexpr (st == 5) ov[e] = P(ov[e] + ow[z]);
        SB(); });
    };
    auto ost = [&](int m) { sts1(v4u{pk2(ov[0], ov[1]), pk2(ov[2], ov[3]), pk2(ov[4], ov[5]), pk2(ov[6], ov[7])}, ro, rowo, m * 20480); };
    SB();
    static_for<0, 48>([&](auto Tc) {
      constexpr int T = Tc, m = T / 12, k = T % 12;
      if constexpr (k == 0 && m < 3) flag_wait(fs, rws, few + (m + 1) * 32, 32, tag, l);
      if constexpr (k == 4) BAR();
      const v4u cur = a4[k];
      static_for<0, 5>([&](auto Nc) { constexpr int n = Nc, G = k * 5 + n;
        mfma_w<k == 0, (G >= 32 || G < 3), true>(bw2[G], cur, ac4[m & 1][n]);
        if constexpr (m == 0 && G < NL2) bw2[60 - NL2 + G] = *(LAS v4u*)(L + W2L_OFF + w * (NL2 * 1024) + G * 1024 + l * 16);
        if constexpr (m > 0 && G >= 3 && G < 18 && G % 3 == 0) *(LAS f32x4*)(rdst + ((m - 1) & 1) * RED_BUF + (G / 3 - 1) * 64) = ac4[(m - 1) & 1][G / 3 - 1];
        if constexpr (m > 0 && G >= 20 && G < 28) { constexpr int q = ((G - 20) & 3) * 2 + ((G - 20) >> 2); r[q] = ((LAS f32x4*)(rsrc + ((m - 1) & 1) * RED_BUF + (q >> 1) * RED_W))[q & 1]; }
        if constexpr (m > 0 && G >= 32 && G < 56) fop(IC<m - 1>{}, IC<(G - 32) / 6>{}, IC<(G - 32) % 6>{});
        if constexpr (m > 0 && G == 57) ost(m - 1);
        if constexpr (m < 3 && n == 4) a4[k] = lda4(m * 12 + k + 12);
        if constexpr (n == 2 && T == 6) { waitv<6>(); flag_st(rws, feo + 96, tag, true, l); }
        if constexpr (n == 2 && m < 2 && k == 8) fs = flag_ld(rws, few + (m + 2) * 32, 32, l);
        SB();
      });
    });
    asm volatile("s_nop 7");
    SB();
#pragma unroll
    for (int n = 0; n < 5; n++) *(LAS f32x4*)(rdst + RED_BUF + n * 64) = ac4[1][n];
    BAR(); red_ld(3, r);
#pragma unroll
    for (int e = 0; e < 8; e++) ov[e] = ((r[e >> 2][e & 3] + r[2 + (e >> 2)][e & 3]) + (r[4 + (e >> 2)][e & 3] + r[6 + (e >> 2)][e & 3])) + (((e & 1) ? bfhi(hk[3][e >> 1]) : bflo(hk[3][e >> 1])) + b2f[e]);
    ost(3);
    flag_st(rws, 0, tag, blockIdx.x == 0 && w == 0, l);
  }
  asm volatile(".Lk_end:");
}

extern "C" void kernel_launch(void* const* d_in, const int* in_sizes, int n_in, void* d_out, int out_size, void* d_ws, size_t ws_size, hipStream_t stream) {
  (void)hipFuncSetAttribute((const void*)k_fused, hipFuncAttributeMaxDynamicSharedMemorySize, 160 * 1024);
  k_fused<<<256, 256, 160 * 1024, stream>>>(d_in[0], d_in[1], d_in[2], d_in[4], d_in[5], d_in[6], d_in[7], d_out, (char*)d_ws);
}
```

```cpp
#include <hip/hip_runtime.h>
#include <hip/hip_bf16.h>
#include <stdint.h>
#include <type_traits>

#define LAS __attribute__((address_space(3)))
#define DI __device__ __forceinline__
typedef __attribute__((ext_vector_type(2))) __bf16 bf16x2;
typedef __attribute__((ext_vector_type(4))) float f32x4;
typedef __attribute__((ext_vector_type(2))) float f32x2;
typedef __attribute__((ext_vector_type(4))) unsigned v4u;
typedef __attribute__((ext_vector_type(2))) unsigned v2u;
typedef __attribute__((ext_vector_type(4))) short s16x4;
typedef __amdgpu_buffer_rsrc_t rsrc_t;

constexpr int S = 2048, D = 640, F = 1536;
constexpr int ACT = S * D * 2;
constexpr int EPOCH_OFF = 0;
constexpr int F0 = 1;
constexpr int FP = F0 + 256;
constexpr int FC = FP + 256;
constexpr int FD = FC + 1024;
constexpr int FE = FD + 1024;
constexpr int NFLAG = FE + 4096;
constexpr int WQP_OFF = (NFLAG * 256 + 65535) / 65536 * 65536, OF_OFF = WQP_OFF + 32 * 80 * 1024, HF_OFF = OF_OFF + ACT;
constexpr int GF_OFF = HF_OFF + ACT, WOP_OFF = GF_OFF + S * F * 2, W1P_OFF = WOP_OFF + D * D * 2, W2P_OFF = W1P_OFF + D * F * 2;
constexpr int KVP_OFF = W2P_OFF + D * F * 2, KVF_OFF = KVP_OFF + 32 * 32 * 2048, WS_END = KVF_OFF + 32 * 2048;
constexpr int OOB = 0x7ffffff0;
constexpr int SC1 = 16;
constexpr unsigned ONES2 = 0x3f803f80u;

DI rsrc_t mkrs(const void* p, int bytes) { return __builtin_amdgcn_make_buffer_rsrc((void*)p, 0, bytes, 0x00020000); }
DI v4u mkrs4(const void* p, int bytes) { const uint64_t a = (uint64_t)p; return v4u{(unsigned)a, (unsigned)(a >> 32) & 0xffffu, (unsigned)bytes, 0x00020000u}; }
DI void mfma_t(v4u a, v4u b, f32x4& c) { asm volatile("v_mfma_f32_16x16x32_bf16 %0, %1, %2, %0" : "+v"(c) : "v"(a), "v"(b)); }
DI void mfma_z(v4u a, v4u b, f32x4& c) { asm volatile("v_mfma_f32_16x16x32_bf16 %0, %1, %2, 0" : "=&v"(c) : "v"(a), "v"(b)); }
template <bool FIRST, bool AG, bool BG = false> DI void mfma_w(v4u a, v4u b, f32x4& c) {
#define MF_(CA, CB) do { if constexpr (FIRST) asm volatile("v_mfma_f32_16x16x32_bf16 %0, %1, %2, 0" : "=&v"(c) : CA(a), CB(b)); \
                         else asm volatile("v_mfma_f32_16x16x32_bf16 %0, %1, %2, %0" : "+v"(c) : CA(a), CB(b)); } while (0)
  if constexpr (AG && BG) MF_("a", "a"); else if constexpr (AG) MF_("a", "v"); else if constexpr (BG) MF_("v", "a"); else MF_("v", "v");
#undef MF_
}
template <bool FIRST> DI void mfma_a(v4u a, v4u b, f32x4& c) { if constexpr (FIRST) mfma_z(a, b, c); else mfma_t(a, b, c); }
DI unsigned pk2(float a, float b) { f32x2 f = {a, b}; return __builtin_bit_cast(unsigned, __builtin_convertvector(f, bf16x2)); }
DI float bflo(unsigned u) { return __uint_as_float(u << 16); }
DI float bfhi(unsigned u) { return __uint_as_float(u & 0xffff0000u); }
DI v4u ldp(rsrc_t r, int voff, int soff) { return __builtin_amdgcn_raw_buffer_load_b128(r, voff, soff, 0); }
DI v4u lds1(rsrc_t r, int voff, int soff) { return __builtin_amdgcn_raw_buffer_load_b128(r, voff, soff, SC1); }
DI void sts1(v4u v, rsrc_t r, int voff, int soff) { asm volatile("" : "+v"(v)); __builtin_amdgcn_raw_buffer_store_b128(v, r, voff, soff, SC1); asm volatile("s_nop 1" :: "v"(v) : "memory"); }
DI v2u trread(LAS char* p) { return __builtin_bit_cast(v2u, __builtin_amdgcn_ds_read_tr16_b64_v4i16((LAS s16x4*)p)); }
#define BAR() do { asm volatile("s_waitcnt lgkmcnt(0)" ::: "memory"); __builtin_amdgcn_s_barrier(); asm volatile("" ::: "memory"); } while (0)
#define SB() __builtin_amdgcn_sched_barrier(0)
constexpr float LOG2E = 1.4426950408889634f;
template <class T> DI void pin(T& x) { asm volatile("" : "+v"(x)); }
template <class T> DI T P(T x) { pin(x); return x; }
template <class T> DI T PS(T x) { asm volatile("" : "+s"(x)); return x; }
DI f32x2 fma2(f32x2 a, f32x2 b, f32x2 c) { return __builtin_elementwise_fma(a, b, c); }
DI f32x2 lo2(f32x4 v) { return f32x2{v[0], v[1]}; }
DI f32x2 hi2(f32x4 v) { return f32x2{v[2], v[3]}; }
DI unsigned pk2v(f32x2 f) { return __builtin_bit_cast(unsigned, __builtin_convertvector(f, bf16x2)); }
template <int N> DI void phi_n(const f32x2 (&u)[N], unsigned (&o)[N]) {
  f32x2 a[N];
#pragma unroll
  for (int e = 0; e < N; e++) a[e] = P(fma2(u[e], f32x2{LOG2E, LOG2E}, f32x2{-LOG2E, -LOG2E}));
  SB();
#pragma unroll
  for (int e = 0; e < N; e++) a[e] = P(f32x2{__builtin_amdgcn_fmed3f(__builtin_amdgcn_exp2f(a[e][0]), 0.f, 1.f), __builtin_amdgcn_fmed3f(__builtin_amdgcn_exp2f(a[e][1]), 0.f, 1.f)});
  SB();
#pragma unroll
  for (int e = 0; e < N; e++) a[e] = P(f32x2{__builtin_amdgcn_fmed3f(u[e][0], a[e][0], 3.0e38f), __builtin_amdgcn_fmed3f(u[e][1], a[e][1], 3.0e38f)});
  SB();
#pragma unroll
  for (int e = 0; e < N; e++) a[e] = P(a[e] + f32x2{1e-6f, 1e-6f});
  SB();
#pragma unroll
  for (int e = 0; e < N; e++) o[e] = pk2v(a[e]);
  SB();
}
DI float dot2(unsigned a, unsigned b, float c) { return __builtin_amdgcn_fdot2_f32_bf16(__builtin_bit_cast(bf16x2, a), __builtin_bit_cast(bf16x2, b), c, false); }
DI float add_x16(float v) { auto r = __builtin_amdgcn_permlane16_swap(__float_as_uint(v), __float_as_uint(v), false, false); return __uint_as_float(r[0]) + __uint_as_float(r[1]); }
DI float add_x32(float v) { auto r = __builtin_amdgcn_permlane32_swap(__float_as_uint(v), __float_as_uint(v), false, false); return __uint_as_float(r[0]) + __uint_as_float(r[1]); }

template <int IMM> DI void glds1(v4u rs, unsigned base, int soff, int v) {
  asm volatile("s_add_u32 m0, %1, %4\n\ts_nop 0\n\tbuffer_load_dwordx4 %3, %0, %2 offen lds" :: "s"(rs), "s"(base), "s"(soff), "v"(v), "i"(IMM) : "memory", "scc");
}
template <int IMM> DI void glds1_sc1(v4u rs, unsigned base, int soff, int v) {
  asm volatile("s_add_u32 m0, %1, %4\n\ts_nop 0\n\tbuffer_load_dwordx4 %3, %0, %2 offen sc1 lds" :: "s"(rs), "s"(base), "s"(soff), "v"(v), "i"(IMM) : "memory", "scc");
}
template <int IMM> DI void set_m0(unsigned base) { asm volatile("s_add_u32 m0, %0, %1" :: "s"(base), "i"(IMM) : "memory", "scc"); }
template <int OFF> DI void glds_o(v4u rs, int soff, int v) { asm volatile("buffer_load_dwordx4 %2, %0, %1 offen offset:%3 sc1 lds" :: "s"(rs), "s"(soff), "v"(v), "i"(OFF) : "memory"); }
template <int N> using IC = std::integral_constant<int, N>;
template <int N> DI void waitv() { asm volatile("s_waitcnt vmcnt(%0)" :: "i"(N) : "memory"); }
template <int I, int N, class Fn> DI void static_for(Fn&& f) { if constexpr (I < N) { f(std::integral_constant<int, I>{}); static_for<I + 1, N>(f); } }

DI unsigned flag_ld(rsrc_t rws, int f, int n, int l) { asm volatile("" ::: "memory"); return __builtin_amdgcn_raw_buffer_load_b32(rws, (l < n ? l : 0) * 256, f * 256, SC1); }
DI void flag_wait(unsigned v, rsrc_t rws, int f, int n, unsigned tag, int l) {
  if (__builtin_expect(!__all(v == tag), 0)) {
#pragma unroll 1
    for (int sp = 0; sp < (1 << 16); sp++) { if (__all(flag_ld(rws, f, n, l) == tag)) break; }
  }
  asm volatile("" ::: "memory");
}
DI void wait_flags(rsrc_t rws, int f, int n, unsigned tag, int l) { flag_wait(flag_ld(rws, f, n, l), rws, f, n, tag, l); }
DI void flag_st(rsrc_t rws, int f, unsigned tag, bool me, int l) {
  asm volatile("" ::: "memory"); sts1(v4u{tag, tag, tag, tag}, rws, (me && l < 8) ? l * 16 : OOB, f * 256); asm volatile("" ::: "memory");
}

template <int MAT> DI int frag_off(int T, int ks) {
  if constexpr (MAT == 0) return WOP_OFF + ((T / 5) * 100 + ks * 5 + T % 5) * 1024;
  else if constexpr (MAT == 1) return W1P_OFF + ((T / 3) * 60 + ks * 3 + T % 3) * 1024;
  else return W2P_OFF + ((T / 5) * 240 + ks * 5 + T % 5) * 1024;
}
template <int MAT> DI void pack_issue(int ks, int cb, int l, v4u rs, unsigned stg) {
  const int r = l >> 3;
  static_for<0, 4>([&](auto Ic) { constexpr int i = Ic;
    const int sw = ((l & 7) ^ ((((r >> 1) & 1) | ((i & 1) << 1)) << 1)) * 16, sg = 4 * ks + i;
    if constexpr (MAT == 0) glds1<i * 1024>(rs, stg, sg * 8 * 1280 + cb * 128, r * 1280 + sw);
    else if constexpr (MAT == 1) { const int q = ks % 10;
      glds1<i * 1024>(rs, stg, ((2 * i + ks / 10) * 80 + (q < 8 ? 32 * (q >> 2) + 4 * (q & 3) : 8 * q)) * 3072 + cb * 128, (q < 8 ? 16 * (r >> 2) + (r & 3) : r) * 3072 + sw); }
    else { const int q = sg % 6; glds1<i * 1024>(rs, stg, (48 * (sg / 6) + (q < 4 ? 4 * q : 8 * q)) * 1280 + cb * 128, (q < 4 ? 16 * (r >> 2) + (r & 3) : r) * 1280 + sw); }
  });
}
DI int unit_lane(int t, int l) { const int g = l >> 4, q = (l >> 2) & 3, p = l & 3, f = ((q >> 1) & 1) | ((g & 1) << 1); return (8 * g + q) * 128 + ((t ^ f) * 32) + p * 8; }
DI v4u unit_read(LAS char* p) { const v2u a = trread(p), b = trread(p + 512); return v4u{a.x, a.y, b.x, b.y}; }
template <int MAT> DI void pack_dst(int ks, int cb, int (&d)[4]) {
#pragma unroll
  for (int t = 0; t < 4; t++) d[t] = frag_off<MAT>(cb * 4 + t, ks);
}
DI void pack_finish(const int (&d)[4], const int (&ul)[4], int l, rsrc_t rws, LAS char* stg) {
  v4u f[4];
#pragma unroll
  for (int t = 0; t < 4; t++) f[t] = unit_read(stg + ul[t]);
#pragma unroll
  for (int t = 0; t < 4; t++) sts1(f[t], rws, l * 16, d[t]);
}
constexpr int TAB_OFF = 81920, SCR_OFF = TAB_OFF + 512, SNK_OFF = SCR_OFF + 2048, XT_OFF = 92160, XT_W = 17408, OB_ROW = 176, WO_L = 61440;
constexpr int RED_ROW = 336, RED_W = 16 * RED_ROW, RED_BUF = 4 * RED_W, A3_0 = 2 * RED_BUF, W2L_OFF = 65536;
constexpr int NL2 = 24;
static_assert(SNK_OFF + 1024 <= WO_L + 28 * 1024 && A3_0 + 20480 <= W2L_OFF, "LDS map");

constexpr int B0 = 15, N1 = B0 + 18, N2 = 42, N3 = 50, N5 = 66, X2 = 8, RD1 = 6, PQ = 50, PFK = 8, RK = 12;

__global__ __launch_bounds__(256, 1) void k_fused(const void* x, const void* wqkv, const void* wo, const void* w1, const void* b1p, const void* w2,
                                                  const void* b2p, void* out, char* ws) {
  extern __shared__ __attribute__((aligned(16))) char smem[];
  LAS char* L = (LAS char*)smem;
  const unsigned Lb = (unsigned)(size_t)L;
  const int tid = threadIdx.x, l = tid & 63, w = __builtin_amdgcn_readfirstlane(tid >> 6), c = l & 15, g = l >> 4;
  const int xg = blockIdx.x & 7, i = blockIdx.x >> 3;
  const unsigned Lx = Lb + XT_OFF + w * XT_W;
  LAS char* xt = L + XT_OFF + w * XT_W;
  {
    uint64_t pc; unsigned len;
    asm volatile("s_getpc_b64 %0\n.Lk_here:\n\ts_mov_b32 %1, .Lk_end-.Lk_here\n\ts_nop 2" : "=s"(pc), "=s"(len));
    glds1<SNK_OFF>(mkrs4((const void*)pc, len & ~15u), Lb, (i * 4 + w) * 640, l < 40 ? l * 16 : OOB);
  }
  const int grp = xg * 4 + (i >> 3), j = i & 7;
  const bool p0 = j < 5;
  const int pk = (i >> 3) * 5 + j;
  const v4u rx4 = mkrs4(x, S * D * 2), rws4 = mkrs4(ws, WS_END);
  const rsrc_t rws = mkrs(ws, WS_END);
  const int uc = 88 + (grp * 5 + j) * 3 + w - 1;
  const bool hasc = p0 && w > 0 && uc < 480;
  const int fi = grp * 3 + j - 5;
  if (p0) {
    const v4u rw4 = mkrs4(wqkv, D * 1920 * 2);
    const int rr = l / 30, e = l - rr * 30, sec = e / 10;
    const int v0 = l < 60 ? rr * 3840 + sec * 1280 + (e - sec * 10) * 16 : OOB, s0 = (32 * pk + 8 * w) * 3840 + xg * 160;
    static_for<0, 4>([&](auto Uc) { constexpr int u = Uc; glds1<u * 1024>(rw4, Lx, s0 + u * 2 * 3840, v0); });
  }
  glds1_sc1<16384>(rws4, Lx, 0, l == 0 ? EPOCH_OFF : (l == 1 ? (p0 ? F0 + xg * 32 + pk : FP + fi) * 256 : OOB));
  int va[2];
#pragma unroll
  for (int u = 0; u < 2; u++) va[u] = (grp * 64 + w * 16 + u * 8 + (l >> 3)) * 1280 + (((l & 7) ^ (g | (u << 2))) * 16);
  auto ldx = [&](auto Qc) { constexpr int q = Qc; int ln = (q >> 1) + (p0 ? 0 : (j - 5) * 3); ln = ln >= 10 ? ln - 10 : ln;
    glds1<(q & 1) * 1024>(rx4, Lb + w * 2048 + ln * 8192, ln * 128, va[q & 1]); };
  int ul[4], dc[4];
#pragma unroll
  for (int t = 0; t < 4; t++) ul[t] = unit_lane(t, l);
  unsigned tag = 0;
  auto get_tag = [&]() { tag = __builtin_amdgcn_readfirstlane(*(LAS unsigned*)(xt + 16384)) + 1u; };
  if (p0) {
    {
    const char* tp = l < 8 ? (const char*)wo + l * 102400 : l < 24 ? (const char*)w1 + (l - 8) * 122880 : l < 40 ? (const char*)w2 + (l - 24) * 122880 : l < 56 ? (const char*)out + (l - 40) * 163840 : l < 60 ? (const char*)b1p : (const char*)b2p;
    asm volatile("s_mov_b32 m0, %1\n\ts_nop 0\n\tglobal_load_lds_dword %0, off" :: "v"(tp), "s"(Lb + SNK_OFF) : "memory");
    }
    const int q = (l >> 2) & 3, p = l & 3;
    const int tp0 = P((q >> 1) * 1024 + (q & 1) * 480 + (g == 0 ? 160 + 8 * p : g == 1 ? 320 + 8 * p : g == 2 ? (p == 0 ? 192 : p == 1 ? 352 : 32) : 8 * p));
    const int vd = P(g * 1024 + c * 16), sd = PS(WQP_OFF + (xg * 80 + pk) * 4096 + w * 256);
    pack_dst<2>(uc % 48, uc / 48, dc);
#pragma unroll
    for (int t = 0; t < 4; t++) dc[t] = PS(dc[t]);
    waitv<2>();
    v4u f[4];
#pragma unroll
    for (int t = 0; t < 4; t++) { const v2u a = trread(xt + tp0 + 40 * t), b = trread(xt + tp0 + 40 * t + 2048); f[t] = v4u{a.x, a.y, b.x, b.y}; }
#pragma unroll
    for (int t = 0; t < 4; t++) sts1(f[t], rws, vd, sd + t * 81920);
    static_for<0, X2>(ldx);
    waitv<X2>();
    get_tag();
    BAR(); flag_st(rws, F0 + xg * 32 + pk, tag, w == 0, l);
    static_for<X2, 20>(ldx);
    if (hasc) pack_issue<2>(uc % 48, uc / 48, l, mkrs4(w2, D * F * 2), Lx + 4096);
  } else {
    static_for<0, 8>(ldx);
#pragma unroll 1
    for (int r = 0; r < 2; r++) { const int u = fi * 8 + w * 2 + r;
      if (u < 200) pack_issue<0>(u % 20, u / 20, l, mkrs4(wo, D * D * 2), Lx + r * 4096);
      else if (u < 680) pack_issue<1>(u % 20, u / 20 - 10, l, mkrs4(w1, D * F * 2), Lx + r * 4096);
      else pack_issue<2>((u - 680) % 48, (u - 680) / 48, l, mkrs4(w2, D * F * 2), Lx + r * 4096); }
    static_for<8, 20>(ldx);
    waitv<12>();
    get_tag();
#pragma unroll 1
    for (int r = 0; r < 2; r++) { const int u = fi * 8 + w * 2 + r;
      int d[4];
      if (u < 200) pack_dst<0>(u % 20, u / 20, d); else if (u < 680) pack_dst<1>(u % 20, u / 20 - 10, d); else pack_dst<2>((u - 680) % 48, (u - 680) / 48, d);
      pack_finish(d, ul, l, rws, xt + r * 4096); }
  }
  const bool mid = c >= 4 && c < 12;
  const int c4 = c < 4 ? c : 4;
  const int vk1 = P(mid ? 1024 + (12 + g * 8 + c) * 16 : OOB), vg0 = P(g == 0 ? c * 16 : OOB), vk3 = P((g == 0 && mid) ? 1024 + (44 + c) * 16 : OOB);
  const int vsc = P(c >= 12 ? SCR_OFF + w * 512 + g * 64 + (c - 12) * 16 : SNK_OFF + l * 8);
  const int vpp = P(g * 2048 + ((xg == 7 && c >= 8) ? 7 : c) * 16), vf2 = P(1024 + (16 + g * 8 + c4) * 16), vf3 = P(1024 + (48 + c4) * 16);
  const int vtc = P(l < 28 ? (l < 16 ? FE + grp * 128 + (l >> 2) * 32 + j * 4 + (l & 3) : (l < 20 ? FC : FD) + grp * 32 + (l & 3) * 8 + j) * 256 + (l >= 24 ? 128 : 0) : OOB);
  const int vhf = P((g & 1) ? OOB : (g >> 1) * 256 + c * 16);
  LAS char* ap[2];
#pragma unroll
  for (int kk = 0; kk < 2; kk++) ap[kk] = L + c * 128 + (((4 * kk + g) ^ ((c >> 1) & 7)) * 16);
  auto rdx = [&](int ks, int m) { return *(LAS v4u*)(ap[ks & 1] + (ks >> 1) * 8192 + m * 2048); };
  auto ldf = [&](int base, int q) { return lds1(rws, l * 16 + (q & 3) * 1024, base + (q >> 2) * 4096); };
  const int hd = 4 * j + w;
  v4u bq[80];
  auto ldq = [&](auto Qc) { constexpr int q = Qc, f = q < 60 ? (q / 3) * 4 + 2 - q % 3 : (q - 60) * 4 + 3; bq[f] = ldf(WQP_OFF + hd * 81920, f); };
  if (w == 0) wait_flags(rws, F0 + j * 32, 20, tag, l);
  BAR();
  static_for<0, B0 - 10>(ldq); SB();
  if (hasc) waitv<B0 - 6>(); else if (p0) waitv<B0 - 10>(); else waitv<B0 - 2>();
  {
    v4u a[20];
#pragma unroll
    for (int ks = 0; ks < 20; ks++) a[ks] = *(LAS v4u*)(ap[ks & 1] + (ks >> 1) * 8192 + w * 2048);
    SB();
    float s[2] = {0.f, 0.f}, q[2] = {0.f, 0.f};
    static_for<0, 20>([&](auto Kc) { constexpr int ks = Kc;
      if constexpr (ks % 2 == 0) ldq(IC<B0 - 10 + ks / 2>{});
#pragma unroll
      for (int e = 0; e < 4; e++) { s[e & 1] = dot2(a[ks][e], ONES2, s[e & 1]); q[e & 1] = dot2(a[ks][e], a[ks][e], q[e & 1]); }
      SB(); });
    const float mean = add_x32(add_x16(s[0] + s[1])) * (1.f / D);
    const float rs = __builtin_amdgcn_rsqf(fmaxf(add_x32(add_x16(q[0] + q[1])) * (1.f / D) - mean * mean, 0.f) + 1e-5f);
    *(LAS float*)(L + TAB_OFF + (g & 1) * 256 + (w * 16 + c) * 4) = (g & 1) ? -mean * rs : rs;
  }
  if (!p0) waitv<B0>();
  BAR();
  if (!p0) flag_st(rws, FP + fi, tag, w == 0, l);
  v4u ones = {ONES2, ONES2, ONES2, ONES2};
  pin(ones);
  v4u fa[2][4];
  unsigned fp = 0;
  const int fpa = FP + w * 24, fpn = 24;
  v4u bw1[60], bw2[60];
  const int w1b = W1P_OFF + hd * 61440, w2b = W2P_OFF + hd * 61440;
  auto ldw2 = [&](auto Qc) { constexpr int q = Qc; if constexpr (q >= 0 && q < 60) bw2[q] = ldf(w2b, q); };
  auto bulk = [&](auto Qc) { constexpr int q = Qc; if constexpr (q < 60) bw1[q] = ldf(w1b, q); else ldw2(IC<q - 60>{}); };
  auto run = [&](auto Nc, unsigned lds, int mem) { static_for<0, Nc>([&](auto Qc) { constexpr int q = Qc;
    if constexpr (q % 4 == 0) { set_m0<q * 1024>(lds); asm volatile("s_nop 0"); }
    glds_o<(q % 4) * 1024>(rws4, mem + (q / 4) * 4096, l * 16); }); };
#define W1F(r) bw1[r]
  {
    f32x4 acc[4][3], cs[3], trs[4], tnm[4];
    LAS char* tabp = L + TAB_OFF + g * 16;
#pragma unroll
    for (int m = 0; m < 4; m++) fa[0][m] = rdx(0, m);
    SB();
    static_for<0, 20>([&](auto Kc) { constexpr int ks = Kc;
      if constexpr (ks == PFK) { if (hasc) pack_finish(dc, ul, l, rws, xt + 4096); SB(); }
      if constexpr (ks == 19) { flag_wait(fp, rws, fpa, fpn, tag, l); BAR(); flag_st(rws, FP + 96 + grp * 5 + j, tag, p0 && w == 0, l); }
      static_for<0, 15>([&](auto Qc) { constexpr int Q = Qc, n = Q / 5, m = Q % 5;
        if constexpr (m < 4) mfma_w<ks == 0, false, true>(fa[ks & 1][m], bq[ks * 4 + n], acc[m][n]); else mfma_w<ks == 0, false, true>(ones, bq[ks * 4 + n], cs[n]);
        if constexpr (Q >= 1 && Q < 9 && (Q & 1)) fa[(ks + 1) & 1][3 - Q / 2] = rdx(ks < 19 ? ks + 1 : 0, 3 - Q / 2);
        if constexpr (m == 2 && B0 + ks * 3 + n < 80) ldq(IC<B0 + ks * 3 + n>{});
        if constexpr (ks == 15 && Q == 0) fp = flag_ld(rws, fpa, fpn, l);
        if constexpr (ks >= 18 && Q >= 9 && Q < 13) (ks == 18 ? trs : tnm)[Q - 9] = *(LAS f32x4*)(tabp + (ks - 18) * 256 + (Q - 9) * 64);
        SB(); });
    });
    static_assert(B0 >= 12 && B0 <= 20, "");
    asm volatile("s_nop 7");
#pragma unroll
    for (int n = 0; n < 3; n++) pin(cs[n]);
    v4u fk[2], fv[2], fr[2], fb[2];
    const f32x2 one2 = {1.f, 1.f}, mk = c < 8 ? one2 : f32x2{0.f, 0.f}, ck = c < 8 ? f32x2{0.f, 0.f} : one2;
    auto ride = [&](auto Ec) { if constexpr (B0 + 60 + Ec < 80) ldq(IC<B0 + 60 + Ec>{}); else bulk(IC<B0 + Ec - 20>{}); };
    static_for<0, 2>([&](auto Uc) { constexpr int u = Uc;
      f32x2 t[3][4], uu[8];
      unsigned o[8];
#pragma unroll
      for (int e = 0; e < 4; e++) { const f32x2 nm = (e & 1) ? hi2(tnm[2 * u + (e >> 1)]) : lo2(tnm[2 * u + (e >> 1)]);
        t[0][e] = P(fma2(nm, lo2(cs[0]), one2)); t[1][e] = P(nm * lo2(cs[1])); t[2][e] = P(nm * lo2(cs[2])); }
      ride(IC<4 * u>{});
      SB();
#pragma unroll
      for (int e = 0; e < 4; e++) { const int m = 2 * u + (e >> 1); const f32x2 rs = (e & 1) ? hi2(trs[m]) : lo2(trs[m]);
        uu[e] = fma2(rs, (e & 1) ? hi2(acc[m][0]) : lo2(acc[m][0]), t[0][e]); t[2][e] = P(fma2(rs, (e & 1) ? hi2(acc[m][2]) : lo2(acc[m][2]), t[2][e]));
        t[1][e] = fma2(rs, (e & 1) ? hi2(acc[m][1]) : lo2(acc[m][1]), t[1][e]); }
      ride(IC<4 * u + 1>{});
      SB();
#pragma unroll
      for (int e = 0; e < 4; e++) { uu[4 + e] = P(t[2][e] + one2); t[2][e] = fma2(t[2][e], mk, ck); fv[u][e] = pk2v(t[1][e]); }
      ride(IC<4 * u + 2>{});
      phi_n<8>(uu, o);
#pragma unroll
      for (int e = 0; e < 4; e++) { fb[u][e] = pk2v(t[2][e]); fk[u][e] = o[e]; fr[u][e] = o[4 + e]; }
      ride(IC<4 * u + 3>{});
      SB();
    });
#pragma unroll
    for (int u = 0; u < 2; u++) { pin(fk[u]); pin(fv[u]); pin(fr[u]); pin(fb[u]); }
    asm volatile("s_nop 1");
    f32x4 t00, t01, t10, t11;
    mfma_z(fk[0], fv[0], t00); mfma_z(fk[0], fb[0], t01); mfma_z(fr[0], fv[0], t10); mfma_z(fr[0], fb[0], t11);
    mfma_t(fk[1], fv[1], t00); mfma_t(fk[1], fb[1], t01); mfma_t(fr[1], fv[1], t10); mfma_t(fr[1], fb[1], t11);
#pragma unroll
    for (int u = 0; u < 2; u++) *(LAS v4u*)(L + (vsc & ~15) + u * 256) = fr[u];
    asm volatile("s_nop 5");
    SB();
    const int kvo = KVP_OFF + (hd * 32 + grp) * 2048;
    sts1(v4u{pk2(t00[0], t00[1]), tag, pk2(t00[2], t00[3]), tag}, rws, l * 16, kvo);
    sts1(v4u{pk2(t01[0], t01[1]), tag, pk2(t01[2], t01[3]), tag}, rws, vk1, kvo);
    sts1(v4u{pk2(t10[0], t10[1]), tag, pk2(t10[2], t10[3]), tag}, rws, vg0, kvo + 1024);
    sts1(v4u{pk2(t11[0], t11[1]), tag, pk2(t11[2], t11[3]), tag}, rws, vk3, kvo);
  }
  if (w == ((grp + 1) & 3)) glds1_sc1<SNK_OFF>(rws4, Lb, 0, vtc);
  const bool act = c < 10;
  v2u xr[5];
  {
    f32x4 acc[4], cs;
    float qrs[4], qnm[4];
    v2u q1[4];
    static_for<0, 21>([&](auto Kc) { constexpr int ks = Kc;
      if constexpr (ks == RK) {
        if (w == (grp & 3)) {
          v4u pp[8];
#pragma unroll 1
          for (int sp = 0; sp < (1 << 16); sp++) {
            asm volatile("" ::: "memory");
#pragma unroll
            for (int q = 0; q < 8; q++) pp[q] = lds1(rws, vpp, KVP_OFF + (hd * 32 + 4 * q) * 2048 + xg * 256);
            SB();
            unsigned bad = 0;
#pragma unroll
            for (int q = 0; q < 8; q++) bad |= (pp[q].y ^ tag) | (pp[q].w ^ tag);
            if (__all(bad == 0)) break;
          }
          float sm[4] = {0.f, 0.f, 0.f, 0.f};
#pragma unroll
          for (int q = 0; q < 8; q++) { sm[0] += bflo(pp[q].x); sm[1] += bfhi(pp[q].x); sm[2] += bflo(pp[q].z); sm[3] += bfhi(pp[q].z); }
#pragma unroll
          for (int e = 0; e < 4; e++) sm[e] = add_x32(add_x16(sm[e]));
          sts1(v4u{pk2(sm[0], sm[1]), tag, pk2(sm[2], sm[3]), tag}, rws, vg0, KVF_OFF + hd * 2048 + xg * 256);
        } else { const int f = (28 + ((w - grp - 1) & 3) * 24) * 1024; run(IC<24>{}, Lb + WO_L + f, WOP_OFF + j * 102400 + f); }
        SB();
      }
      if constexpr (ks < 20) static_for<0, 5>([&](auto Qc) { constexpr int m = Qc;
        if constexpr (m < 4) mfma_a<ks == 0>(bq[ks * 4 + 3], fa[ks & 1][m], acc[m]); else mfma_a<ks == 0>(bq[ks * 4 + 3], ones, cs);
        if constexpr (ks < 19 && m < 4) fa[(ks + 1) & 1][3 - m] = rdx(ks + 1, 3 - m);
        if constexpr (m == 4 || (m == 1 && (ks & 1))) bulk(IC<B0 - 12 + (ks / 2) * 3 + (ks & 1) + (m == 4 && (ks & 1))>{});
        SB(); });
    });
    {
      LAS char* qp = L + SCR_OFF + w * 512 + (l & 15) / 4 * 16 + (l & 3) * 64;
#pragma unroll
      for (int m = 0; m < 4; m++) {
        qrs[m] = *(LAS float*)(L + TAB_OFF + c * 4 + m * 64); qnm[m] = *(LAS float*)(L + TAB_OFF + c * 4 + 256 + m * 64);
        q1[m] = trread(qp + (m >> 1) * 256 + (m & 1) * 8);
      }
#pragma unroll
      for (int n = 0; n < 5; n++) { const int sg = 10 * j + 2 * n + (g >> 1);
        xr[n] = *(LAS v2u*)(L + (sg >> 3) * 8192 + (w * 16 + c) * 128 + (((sg & 7) ^ ((c >> 1) & 7)) * 16) + (g & 1) * 8); }
    }
    asm volatile("s_nop 7");
    SB();
    v4u ka0, ka1, kb0, kb1;
    auto kv_ld = [&]() { asm volatile("" ::: "memory");
      ka0 = lds1(rws, l * 16, KVF_OFF + hd * 2048); ka1 = lds1(rws, 1024 + c * 16, KVF_OFF + hd * 2048);
      kb0 = lds1(rws, vf2, KVF_OFF + hd * 2048); kb1 = lds1(rws, vf3, KVF_OFF + hd * 2048); };
    auto kv_ok = [&]() { return __all(((ka0.y ^ tag) | (ka0.w ^ tag) | (ka1.y ^ tag) | (ka1.w ^ tag) | (kb0.y ^ tag) | (kb0.w ^ tag) | (kb1.y ^ tag) | (kb1.w ^ tag)) == 0); };
    const unsigned fq = flag_ld(rws, FP + 96 + w * 33, 33, l);
    static_for<N1, N2>(bulk); SB();
    kv_ld();
    v4u pq[4];
    {
      f32x2 uu[8];
      unsigned o[8];
#pragma unroll
      for (int e = 0; e < 8; e++) uu[e] = P(fma2(f32x2{qnm[e >> 1], qnm[e >> 1]}, (e & 1) ? hi2(cs) : lo2(cs), f32x2{1.f, 1.f}));
      SB();
#pragma unroll
      for (int e = 0; e < 8; e++) uu[e] = P(fma2(f32x2{qrs[e >> 1], qrs[e >> 1]}, (e & 1) ? hi2(acc[e >> 1]) : lo2(acc[e >> 1]), uu[e]));
      SB();
      phi_n<8>(uu, o);
#pragma unroll
      for (int m = 0; m < 4; m++) { pq[m] = v4u{o[2 * m], o[2 * m + 1], q1[m].x, q1[m].y}; pin(pq[m]); }
    }
    flag_wait(fq, rws, FP + 96 + w * 33, 33, tag, l);
    BAR();
    if (__builtin_expect(!kv_ok(), 0)) {
#pragma unroll 1
      for (int sp = 0; sp < (1 << 16); sp++) { kv_ld(); if (kv_ok()) break; }
    }
    v4u kf0 = {ka0.x, ka0.z, g == 3 ? ka1.x : 0u, g == 3 ? ka1.z : 0u}, kf1 = {kb0.x, kb0.z, g == 3 ? kb1.x : 0u, g == 3 ? kb1.z : 0u};
    pin(kf0); pin(kf1);
    asm volatile("s_nop 1");
    f32x4 n0[4], n1[4];
    LAS char* ob0 = L + c * OB_ROW + w * 40 + g * 8;
    LAS char* ob1 = L + c * OB_ROW + (g == 0 ? w * 40 + 32 : 160);
#pragma unroll
    for (int m = 0; m < 4; m++) { mfma_z(kf0, pq[m], n0[m]); mfma_z(kf1, pq[m], n1[m]); }
    asm volatile("s_nop 7");
    SB();
    float rr[4];
#pragma unroll
    for (int m = 0; m < 4; m++) rr[m] = __uint_as_float(__builtin_amdgcn_permlane16_swap(__float_as_uint(n1[m][0]), __float_as_uint(n1[m][0]), false, false)[1]);
    SB();
#pragma unroll
    for (int m = 0; m < 4; m++) rr[m] = P(rr[m] + 1e-6f);
    SB();
#pragma unroll
    for (int m = 0; m < 4; m++) rr[m] = P(__builtin_amdgcn_rcpf(rr[m]));
    SB();
#pragma unroll
    for (int m = 0; m < 4; m++) { n0[m] = P(n0[m] * rr[m]); n1[m] = P(n1[m] * rr[m]); }
    SB();
#pragma unroll
    for (int m = 0; m < 4; m++) {
      *(LAS v2u*)(ob0 + m * 16 * OB_ROW) = v2u{pk2v(lo2(n0[m])), pk2v(hi2(n0[m]))};
      *(LAS v2u*)(ob1 + m * 16 * OB_ROW) = v2u{pk2v(lo2(n1[m])), pk2v(hi2(n1[m]))};
    }
    BAR();
    v4u o[3];
#pragma unroll
    for (int q = 0; q < 3; q++) o[q] = *(LAS v4u*)(L + (w * 16 + c) * OB_ROW + (q * 4 + (q == 2 ? g & 1 : g)) * 16);
#pragma unroll
    for (int q = 0; q < 3; q++) sts1(o[q], rws, (q < 2 || g < 2) ? l * 16 + q * 1024 : OOB, OF_OFF + (grp * 4 + w) * 20480 + j * 2560);
    SB();
    static_for<N2, N3>(bulk); SB();
    waitv<N3 - N2>();
    flag_st(rws, FC + grp * 32 + w * 8 + j, tag, true, l);
    run(IC<7>{}, Lb + WO_L + w * 7168, WOP_OFF + j * 102400 + w * 7168);
  }
  LAS char* rdst = L + w * RED_W + c * RED_ROW + g * 16;
  LAS char* rsrc = L + (4 * w + g) * RED_ROW + (act ? c : 9) * 32;
  const int rowo = act ? (grp * 64 + 4 * w + g) * 1280 + j * 160 + c * 16 : OOB;
  auto red_ld = [&](int m, f32x4 (&r)[8]) {
#pragma unroll
    for (int ww = 0; ww < 4; ww++) { LAS f32x4* p = (LAS f32x4*)(rsrc + (m & 1) * RED_BUF + ww * RED_W); r[2 * ww] = p[0]; r[2 * ww + 1] = p[1]; }
  };
  v4u hs[4][2], hk[4];
  float hsa[4], hsb[4];
  const int hsv = g * 512 + c * 16;
  auto h_poll = [&](int m) { asm volatile("" ::: "memory");
    hs[m][0] = lds1(rws, hsv, (FD + grp * 32 + m * 8) * 256); hs[m][1] = lds1(rws, hsv + 256, (FD + grp * 32 + m * 8) * 256); };
  auto h_ok = [&](int m) { return __all(((hs[m][0].y ^ tag) | (hs[m][0].w ^ tag) | (hs[m][1].y ^ tag) | (hs[m][1].w ^ tag)) == 0); };
  auto h_wait = [&](int m) {
    if (__builtin_expect(!h_ok(m), 0)) {
#pragma unroll 1
      for (int sp = 0; sp < (1 << 16); sp++) { h_poll(m); if (h_ok(m)) break; }
    }
    asm volatile("" ::: "memory");
    hsa[m] = P(__uint_as_float(hs[m][0].x) + __uint_as_float(hs[m][1].x)); hsb[m] = P(__uint_as_float(hs[m][0].z) + __uint_as_float(hs[m][1].z));
  };
  v2u bias[3];
  const unsigned La = Lb + w * 5120;
  const int hso = HF_OFF + grp * 81920 + w * 5120;
  auto ldh = [&](auto Mc, auto Qc) { constexpr int m = Mc, q = Qc; glds1_sc1<((m & 1) ? 0 : A3_0) + q * 1024>(rws4, La, hso + m * 20480 + q * 1024, l * 16); };
  f32x4 cs[3];
  {
    const rsrc_t rb1 = mkrs(b1p, F * 2);
#pragma unroll
    for (int n = 0; n < 3; n++) bias[n] = __builtin_amdgcn_raw_buffer_load_b64(rb1, ((12 * j + 3 * w + n) * 16 + 4 * g) * 2, 0, 0);
    static_for<N3, N5>(bulk); SB();
    wait_flags(rws, FC + grp * 32 + w * 8, 8, tag, l);
    v4u a[20], bo[100];
    auto lda = [&](auto Qc) { constexpr int q = Qc; a[q] = ldf(OF_OFF + (grp * 4 + w) * 20480, q); };
    static_for<0, 12>(lda);
    BAR();
    LAS char* wp[2] = {L + WO_L + l * 16, L + WO_L + 51200 + l * 16};
    auto rdo = [&](int f) { return *(LAS v4u*)(wp[f / 50] + (f % 50) * 1024); };
#pragma unroll
    for (int f = 0; f < 10; f++) bo[f] = rdo(f);
    SB();
    f32x4 acc[5];
    static_for<0, 100>([&](auto Fc) { constexpr int f = Fc, k = f / 5, n = f % 5;
      mfma_a<k == 0>(bo[f], a[k], acc[n]);
      if constexpr (k < 18) bo[(k + 2) * 5 + 4 - n] = rdo((k + 2) * 5 + 4 - n);
      if constexpr (n == 2 && k + 12 < 20) lda(IC<k + 12>{});
      SB(); });
    asm volatile("s_nop 7");
    SB();
    unsigned hp[10];
    float s[2] = {0.f, 0.f}, q[2] = {0.f, 0.f};
#pragma unroll
    for (int n = 0; n < 5; n++) {
      hp[2 * n] = pk2v(lo2(acc[n]) + f32x2{bflo(xr[n].x), bfhi(xr[n].x)}); hp[2 * n + 1] = pk2v(hi2(acc[n]) + f32x2{bflo(xr[n].y), bfhi(xr[n].y)});
    }
    const int hb = HF_OFF + (grp * 4 + w) * 20480 + j * 2560;
    sts1(v4u{hp[0], hp[1], hp[2], hp[3]}, rws, l * 16, hb);
    sts1(v4u{hp[4], hp[5], hp[6], hp[7]}, rws, l * 16 + 1024, hb);
    auto s0 = __builtin_amdgcn_permlane16_swap(hp[8], hp[8], false, false);
    auto s1 = __builtin_amdgcn_permlane16_swap(hp[9], hp[9], false, false);
    sts1(v4u{s0[0], s1[0], s0[1], s1[1]}, rws, vhf, hb + 2048);
#pragma unroll
    for (int e = 0; e < 10; e++) { s[e & 1] = dot2(hp[e], ONES2, s[e & 1]); q[e & 1] = dot2(hp[e], hp[e], q[e & 1]); }
    const v4u hst = {__float_as_uint(add_x32(add_x16(s[0] + s[1]))), tag, __float_as_uint(add_x32(add_x16(q[0] + q[1]))), tag};
    static_for<N5, N5 + RD1>(bulk); SB();
    waitv<RD1>();
    sts1(hst, rws, vg0, (FD + grp * 32 + w * 8 + j) * 256);
    BAR();
    static_assert(N5 >= 60, "w1 has landed");
    asm volatile("s_nop 1");
    static_for<0, 60>([&](auto Qc) { constexpr int q = Qc; mfma_w<(q < 3), true>(W1F(q), ones, cs[q % 3]);
      if constexpr (q % 5 == 0 && q < PQ) bulk(IC<N5 + RD1 + q / 5>{});
      if constexpr (q == PQ) { h_poll(0); h_poll(1); }
      SB(); });
    h_wait(0); h_wait(1);
    run(IC<5>{}, La + A3_0, hso);
    h_poll(2); h_poll(3);
    static_for<N5 + RD1 + PQ / 5, 92>(bulk); SB();
    h_wait(2); h_wait(3);
    run(IC<5>{}, La, hso + 20480);
  }
  {
    const int few = FE + grp * 128, feo = few + j * 4 + w;
    unsigned fs = 0;
    f32x2 bf[6];
#pragma unroll
    for (int e = 0; e < 6; e++) bf[e] = P(f32x2{bflo(bias[e >> 1][e & 1]), bfhi(bias[e >> 1][e & 1])});
    SB();
    BAR();
    LAS char* ab = L + g * 5120 + c * 16;
    auto rda = [&](int t) { return *(LAS v4u*)(ab + (((t / 20) & 1) ? 0 : A3_0) + (t % 20) * 256); };
    const int cc = act ? c : 0;
    LAS char* hr0 = L + j * 2560 + (4 * w + g) * 16 + (cc < 8 ? ((cc >> 2) * 4 + (cc & 1) * 2) * 256 + ((cc >> 1) & 1) * 8 : cc * 256);
    LAS char* hr1 = hr0 + (cc < 8 ? 256 : 8);
    const unsigned L2w = Lb + W2L_OFF + w * (NL2 * 1024);
    constexpr int RD = 6;
    v4u af[RD];
    f32x4 acc[3];
    f32x2 bp[6], gv[6], gu[6];
    unsigned gp[6];
    float rs = 0.f, nr = 0.f, sa = 0.f, sb = 0.f;
    auto stats = [&](int m, auto Pc) { constexpr int p = Pc;
      if constexpr (p == 1) { auto x = __builtin_amdgcn_permlane16_swap(__float_as_uint(hsa[m]), __float_as_uint(hsb[m]), false, false); sa = P(__uint_as_float(x[0]) + __uint_as_float(x[1])); sb = P(sa); }
      if constexpr (p == 2) { auto x = __builtin_amdgcn_permlane32_swap(__float_as_uint(sa), __float_as_uint(sb), false, false); sa = P(__uint_as_float(x[0]) + __uint_as_float(x[1])); sb = P(sa); }
      if constexpr (p == 3) { auto x = __builtin_amdgcn_permlane16_swap(__float_as_uint(sa), __float_as_uint(sb), false, false);
        sa = P(__uint_as_float(x[0]) * (1.f / D)); sb = P(fmaxf(__uint_as_float(x[1]) * (1.f / D) - sa * sa, 0.f)); }
      if constexpr (p == 4) { const float r1 = __builtin_amdgcn_rsqf(sb + 1e-5f);
        rs = P(r1 * __builtin_amdgcn_rsqf(sb * r1 * r1 + 1e-5f));
        nr = P(-sa * rs); }
    };
    auto epi1 = [&]() {
#pragma unroll
      for (int e = 0; e < 6; e++) bp[e] = P(fma2(f32x2{nr, nr}, (e & 1) ? hi2(cs[e >> 1]) : lo2(cs[e >> 1]), bf[e]));
      asm volatile("s_nop 1");
      SB();
      constexpr float C0 = -0.7978845608028654f * 2.f * LOG2E, C1 = C0 * 0.044715f;
      const f32x2 rs2 = {rs, rs};
#pragma unroll
      for (int e = 0; e < 6; e++) gv[e] = P(fma2(rs2, (e & 1) ? hi2(acc[e >> 1]) : lo2(acc[e >> 1]), bp[e]));
      SB();
#pragma unroll
      for (int e = 0; e < 6; e++) gu[e] = P(gv[e] * gv[e]);
      SB();
#pragma unroll
      for (int e = 0; e < 6; e++) gu[e] = P(fma2(gu[e], f32x2{C1, C1}, f32x2{C0, C0}));
      SB();
#pragma unroll
      for (int e = 0; e < 6; e++) gu[e] = P(gu[e] * gv[e]);
      SB();
    };
    auto gstore = [&](int m) {
      const int base = GF_OFF + (grp * 4 + m) * 49152 + 6 * hd * 256;
      sts1(v4u{gp[0], gp[1], gp[2], gp[3]}, rws, l * 16, base);
      auto s0 = __builtin_amdgcn_permlane16_swap(gp[4], gp[4], false, false);
      auto s1 = __builtin_amdgcn_permlane16_swap(gp[5], gp[5], false, false);
      sts1(v4u{s0[0], s1[0], s0[1], s1[1]}, rws, vhf, base + 1024);
    };
    auto egap = [&](auto Mc, auto Gc) { constexpr int G = Gc;
      if constexpr (G < 18 && G % 3 < 2) { constexpr int e = G / 3, z = G % 3; gu[e][z] = P(__builtin_amdgcn_exp2f(gu[e][z])); }
      if constexpr (G >= 2 && G < 20 && G % 3 == 2) { constexpr int e = G / 3; gu[e][0] = P(gu[e][0] + 1.0f); SB(); gu[e][1] = P(gu[e][1] + 1.0f); }
      if constexpr (G >= 18 && G < 36 && G % 3 < 2) { constexpr int e = (G - 18) / 3, z = G % 3; gu[e][z] = P(__builtin_amdgcn_rcpf(gu[e][z])); }
      if constexpr (G >= 20 && G < 38 && G % 3 == 2) { constexpr int e = (G - 20) / 3; gv[e][0] = P(gv[e][0] * gu[e][0]); SB(); gv[e][1] = P(gv[e][1] * gu[e][1]); }
      if constexpr (G >= 36 && G < 39 + 1 && G != 38) { constexpr int e = (G == 39 ? 2 : G - 36) * 2; gp[e] = P(pk2v(gv[e])); gp[e + 1] = P(pk2v(gv[e + 1])); }
      if constexpr (G == 40) gstore(Mc);
    };
    v4u a4[12];
    auto lda4 = [&](int q) { return ldf(GF_OFF + ((grp * 4 + q / 12) * 48 + w * 12) * 1024, q % 12); };
    const rsrc_t ro = mkrs(out, S * D * 2), rb2 = mkrs(b2p, D * 2);
    v4u br;
#pragma unroll
    for (int q = 0; q < RD; q++) af[q] = rda(q);
    SB();
    asm volatile("s_nop 1");
    auto rider = [&](auto Qc) { constexpr int q = Qc; if constexpr (q < NL2) glds1_sc1<q * 1024>(rws4, L2w, w2b + (60 - NL2 + q) * 1024, l * 16); };
    static_for<0, 80>([&](auto Tc) {
      constexpr int T = Tc, m = T / 20, k = T % 20;
      if constexpr (k == 14 && m < 3) {
        waitv<(m == 0 ? 14 : m == 1 ? 9 : 6)>();
        BAR();
      }
      if constexpr (T == 66) flag_wait(fs, rws, few, 32, tag, l);
      const v4u cur = af[T % RD];
      static_for<0, 3>([&](auto Nc) {
        constexpr int n = Nc, G = k * 3 + n;
        mfma_w<k == 0, true>(W1F(G), cur, acc[n]);
        if constexpr (m > 0) egap(IC<m - 1>{}, IC<G>{});
        if constexpr (n == 2 && (T & 1) && T + RD < 80) { af[T % RD] = rda(T + RD); SB(); af[(T - 1) % RD] = rda(T + RD - 1); }
        if constexpr (G == 41) { const v2u u0 = *(LAS v2u*)(hr0 + ((m & 1) ? 0 : A3_0)), u1 = *(LAS v2u*)(hr1 + ((m & 1) ? 0 : A3_0)); hk[m] = v4u{u0.x, u0.y, u1.x, u1.y}; }
        if constexpr (m < 2 && n == 0 && k >= 14 && k < 19) ldh(IC<m + 2>{}, IC<k - 14>{});
        if constexpr (m == 0 && n == 1 && k < 14) rider(IC<k>{});
        if constexpr (m > 0 && m < 3 && n == 1 && (k & 1) == 0 && k < 14) rider(IC<14 + (m - 1) * 7 + k / 2>{});
        if constexpr (G >= 46 && G < 58 && G % 3 == 1) stats(m, IC<(G - 43) / 3>{});

        if constexpr (n == 0 && T == 60) br = ldp(rb2, act ? j * 160 + c * 16 : OOB, 0);
        if constexpr (n == 1 && T >= 62 && T < 66) ldw2(IC<32 + T - 62>{});
        if constexpr (n == 0 && T == 61) fs = flag_ld(rws, few, 32, l);
        if constexpr (n == 0 && T >= 66 && T < 78) a4[T - 66] = lda4(T - 66);
        SB();
      });
      if constexpr (k == 19) { epi1(); if constexpr (m >= 1) { waitv<(m == 1 ? 5 : m == 2 ? 0 : 4)>(); flag_st(rws, feo + (m - 1) * 32, tag, true, l); } }
    });
    fs = flag_ld(rws, few + 32, 32, l);
#pragma unroll
    for (int e = 0; e < 6; e++) gu[e] = P(f32x2{__builtin_amdgcn_exp2f(gu[e][0]), __builtin_amdgcn_exp2f(gu[e][1])});
    SB();
#pragma unroll
    for (int e = 0; e < 6; e++) gu[e] = P(gu[e] + f32x2{1.f, 1.f});
    SB();
#pragma unroll
    for (int e = 0; e < 6; e++) gu[e] = P(f32x2{__builtin_amdgcn_rcpf(gu[e][0]), __builtin_amdgcn_rcpf(gu[e][1])});
    SB();
#pragma unroll
    for (int e = 0; e < 6; e++) gu[e] = P(gv[e] * gu[e]);
    SB();
#pragma unroll
    for (int e = 0; e < 6; e++) gp[e] = pk2v(gu[e]);
    gstore(3);
    SB();
    f32x4 ac4[2][5], r[8];
    float b2f[8], ov[8], ow[2];
#pragma unroll
    for (int e = 0; e < 8; e++) b2f[e] = (e & 1) ? bfhi(br[e >> 1]) : bflo(br[e >> 1]);
    auto fop = [&](auto Mc, auto Pc, auto Sc) { constexpr int m = Mc, st = Sc;
      static_for<0, 2>([&](auto Zc) { constexpr int z = Zc, e = 2 * Pc + z;
        if constexpr (st == 0) ov[e] = P(r[e >> 2][e & 3] + r[2 + (e >> 2)][e & 3]);
        if constexpr (st == 1) ow[z] = P(r[4 + (e >> 2)][e & 3] + r[6 + (e >> 2)][e & 3]);
        if constexpr (st == 2) ov[e] = P(ov[e] + ow[z]);
        if constexpr (st == 3) ow[z] = P(z ? bfhi(hk[m][e >> 1]) : bflo(hk[m][e >> 1]));
        if constexpr (st == 4) ow[z] = P(ow[z] + b2f[e]);
        if constexpr (st == 5) ov[e] = P(ov[e] + ow[z]);
        SB(); });
    };
    auto ost = [&](int m) { sts1(v4u{pk2(ov[0], ov[1]), pk2(ov[2], ov[3]), pk2(ov[4], ov[5]), pk2(ov[6], ov[7])}, ro, rowo, m * 20480); };
    SB();
    static_for<0, 48>([&](auto Tc) {
      constexpr int T = Tc, m = T / 12, k = T % 12;
      if constexpr (k == 0 && m < 3) flag_wait(fs, rws, few + (m + 1) * 32, 32, tag, l);
      if constexpr (k == 4) BAR();
      const v4u cur = a4[k];
      static_for<0, 5>([&](auto Nc) { constexpr int n = Nc, G = k * 5 + n;
        mfma_w<k == 0, (G >= 32 || G < 3), true>(bw2[G], cur, ac4[m & 1][n]);
        if constexpr (m == 0 && G < NL2) bw2[60 - NL2 + G] = *(LAS v4u*)(L + W2L_OFF + w * (NL2 * 1024) + G * 1024 + l * 16);
        if constexpr (m > 0 && G >= 3 && G < 18 && G % 3 == 0) *(LAS f32x4*)(rdst + ((m - 1) & 1) * RED_BUF + (G / 3 - 1) * 64) = ac4[(m - 1) & 1][G / 3 - 1];
        if constexpr (m > 0 && G >= 20 && G < 28) { constexpr int q = ((G - 20) & 3) * 2 + ((G - 20) >> 2); r[q] = ((LAS f32x4*)(rsrc + ((m - 1) & 1) * RED_BUF + (q >> 1) * RED_W))[q & 1]; }
        if constexpr (m > 0 && G >= 32 && G < 56) fop(IC<m - 1>{}, IC<(G - 32) / 6>{}, IC<(G - 32) % 6>{});
        if constexpr (m > 0 && G == 57) ost(m - 1);
        if constexpr (m < 3 && n == 4) a4[k] = lda4(m * 12 + k + 12);
        if constexpr (n == 2 && T == 6) { waitv<6>(); flag_st(rws, feo + 96, tag, true, l); }
        if constexpr (n == 2 && m < 2 && k == 8) fs = flag_ld(rws, few + (m + 2) * 32, 32, l);
        SB();
      });
    });
    asm volatile("s_nop 7");
    SB();
#pragma unroll
    for (int n = 0; n < 5; n++) *(LAS f32x4*)(rdst + RED_BUF + n * 64) = ac4[1][n];
    BAR(); red_ld(3, r);
#pragma unroll
    for (int e = 0; e < 8; e++) ov[e] = ((r[e >> 2][e & 3] + r[2 + (e >> 2)][e & 3]) + (r[4 + (e >> 2)][e & 3] + r[6 + (e >> 2)][e & 3])) + (((e & 1) ? bfhi(hk[3][e >> 1]) : bflo(hk[3][e >> 1])) + b2f[e]);
    ost(3);
    flag_st(rws, 0, tag, blockIdx.x == 0 && w == 0, l);
  }
  asm volatile(".Lk_end:");
}

extern "C" void kernel_launch(void* const* d_in, const int* in_sizes, int n_in, void* d_out, int out_size, void* d_ws, size_t ws_size, hipStream_t stream) {
  (void)hipFuncSetAttribute((const void*)k_fused, hipFuncAttributeMaxDynamicSharedMemorySize, 160 * 1024);
  k_fused<<<256, 256, 160 * 1024, stream>>>(d_in[0], d_in[1], d_in[2], d_in[4], d_in[5], d_in[6], d_in[7], d_out, (char*)d_ws);
}
```
